```python
import torch

def _softmax(x, axis=-1):
    return torch.softmax(x, dim=axis)
def _gelu(x):
    return torch.nn.functional.gelu(x, approximate="tanh")
def _silu(x):
    return torch.nn.functional.silu(x)
def _sigmoid(x):
    return torch.sigmoid(x)
def _layernorm(x, axis=-1, eps=1e-5):
    m = x.mean(dim=axis, keepdim=True)
    v = x.var(dim=axis, keepdim=True, unbiased=False)
    return (x - m) / torch.sqrt(v + eps)
def _scatter_add(tgt, idx, src, axis=0):
    return tgt.clone().index_add_(axis, idx, src)
def _segment_sum(src, seg, n):
    out = torch.zeros((n,) + src.shape[1:], dtype=src.dtype)
    return out.index_add_(0, seg, src)
def _pad(x, pw):
    flat = tuple(v for pair in reversed(pw) for v in pair)
    return torch.nn.functional.pad(x, flat)

kernel_name = 'interleave_layers__linear_kernel_hedgehog__mixer_channel_mlp__prompt'

def setup_inputs():

    _w = (lambda a: (lambda t: t.to(torch.bfloat16) if t.is_floating_point() else t)(torch.as_tensor(a)))
    def _A_setup():
        B, S, D, H = 1, 2048, 512, 32
        Dh = D // H
        return {
            "x": _w((lambda *s: torch.randn(*s, dtype=torch.bfloat16) * 0.05)(B, S, D)),
            "wqkv": _w((lambda *s: torch.randn(*s, dtype=torch.bfloat16) * 0.4)(D, 3 * D)),
            "w_phi": _w((lambda *s: torch.randn(*s, dtype=torch.bfloat16) * 0.4)(H, Dh, Dh) * (Dh ** -0.5)),
            "wo": _w((lambda *s: torch.randn(*s, dtype=torch.bfloat16) * 0.05)(D, D)),
        }
    _A_in = _A_setup()

    def _B_setup():
        B, S, D, F = 1, 2048, 512, 1792
        return {
            "x": _w((lambda *s: torch.randn(*s, dtype=torch.bfloat16) * 0.05)(B, S, D)),
            "w1": _w((lambda *s: torch.randn(*s, dtype=torch.bfloat16) * 3.2)(D, F) * (D ** -0.5)),
            "b1": _w((lambda *s: torch.randn(*s, dtype=torch.bfloat16) * 0.8)(F)),
            "w2": _w((lambda *s: torch.randn(*s, dtype=torch.bfloat16) * 0.8)(F, D) * (F ** -0.5)),
            "b2": _w((lambda *s: torch.randn(*s, dtype=torch.bfloat16) * 0.8)(D)),
        }
    _B_in = _B_setup()

    return {
        "A_x": _A_in["x"],
        "A_wqkv": _A_in["wqkv"],
        "A_w_phi": _A_in["w_phi"],
        "A_wo": _A_in["wo"],
        "B_x": _B_in["x"],
        "B_w1": _B_in["w1"],
        "B_b1": _B_in["b1"],
        "B_w2": _B_in["w2"],
        "B_b2": _B_in["b2"],
    }

def reference(A_x, A_wqkv, A_w_phi, A_wo, B_x, B_w1, B_b1, B_w2, B_b2):
    def _A_ref(x, wqkv, w_phi, wo):
        B, S, D = x.shape
        H = 32; Dh = D // H
        qkv = (lambda x, *d: x.permute(*d))((x @ wqkv).reshape(B, S, 3, H, Dh), 2, 0, 3, 1, 4)
        q, k, v = qkv[0], qkv[1], qkv[2]
        pq = torch.einsum("bhsd,hdf->bhsf", q, w_phi)
        pk = torch.einsum("bhsd,hdf->bhsf", k, w_phi)
        phi_q = torch.concatenate([_softmax(pq, axis=-1), _softmax(-pq, axis=-1)], axis=-1)
        phi_k = torch.concatenate([_softmax(pk, axis=-1), _softmax(-pk, axis=-1)], axis=-1)
        kv = torch.einsum("bhsf,bhsd->bhfd", phi_k, v)
        z  = torch.einsum("bhsf->bhf", phi_k)
        num = torch.einsum("bhqf,bhfd->bhqd", phi_q, kv)
        den = torch.einsum("bhqf,bhf->bhq", phi_q, z)[..., None] + 1e-6
        o = num / den
        return (lambda x, *d: x.permute(*d))(o, 0, 2, 1, 3).reshape(B, S, D) @ wo

    def _B_ref(x, w1, b1, w2, b2):
        y = _layernorm(x, axis=-1)
        h = _gelu(torch.einsum("bsd,df->bsf", y, w1) + b1)
        return torch.einsum("bsf,fd->bsd", h, w2) + b2

    x = A_x
    x = x + _A_ref(_layernorm(x, axis=-1), A_wqkv, A_w_phi, A_wo)
    x = x + _B_ref(_layernorm(x, axis=-1), B_w1, B_b1, B_w2, B_b2)
    x = x + _A_ref(_layernorm(x, axis=-1), A_wqkv, A_w_phi, A_wo)
    x = x + _B_ref(_layernorm(x, axis=-1), B_w1, B_b1, B_w2, B_b2)
    return x
```

```cpp
#include <hip/hip_runtime.h>
#include <hip/hip_bf16.h>
#include <stdint.h>
typedef unsigned short u16;
__device__ __forceinline__ float bf2f(u16 v){ return __uint_as_float(((unsigned)v)<<16); }
__device__ __forceinline__ u16 f2bf(float f){ __hip_bfloat16 h = __float2bfloat16(f); return *(u16*)&h; }
constexpr int S=2048,D=512,H=32,DH=16,F=1792;

__global__ void k_cvt(const u16* x, float* o, int n){ int i=blockIdx.x*256+threadIdx.x; if(i<n) o[i]=bf2f(x[i]); }
__global__ void k_out(const float* x, u16* o, int n){ int i=blockIdx.x*256+threadIdx.x; if(i<n) o[i]=f2bf(x[i]); }
__global__ void k_ln(const float* x, float* y){
  int row=blockIdx.x; int l=threadIdx.x;
  float v[8]; float s=0;
  for(int i=0;i<8;i++){ v[i]=x[row*D+l+64*i]; s+=v[i]; }
  for(int o=32;o;o>>=1) s+=__shfl_xor(s,o);
  float m=s/D; float q=0;
  for(int i=0;i<8;i++){ float d=v[i]-m; q+=d*d; }
  for(int o=32;o;o>>=1) q+=__shfl_xor(q,o);
  float r=1.0f/sqrtf(q/D+1e-5f);
  for(int i=0;i<8;i++) y[row*D+l+64*i]=(v[i]-m)*r;
}
template<int MODE>
__global__ void k_gemm(const float* A, const u16* W, const u16* bias, const float* res, float* C, int N, int K){
  __shared__ float As[16][17]; __shared__ float Ws[16][17];
  int tx=threadIdx.x&15, ty=threadIdx.x>>4;
  int row=blockIdx.y*16+ty, col=blockIdx.x*16+tx;
  float acc=0;
  for(int k0=0;k0<K;k0+=16){
    As[ty][tx]=A[(size_t)row*K+k0+tx];
    Ws[ty][tx]=bf2f(W[(size_t)(k0+ty)*N+col]);
    __syncthreads();
    for(int k=0;k<16;k++) acc+=As[ty][k]*Ws[k][tx];
    __syncthreads();
  }
  if(MODE==1){ acc+=bf2f(bias[col]); float u=0.7978845608028654f*(acc+0.044715f*acc*acc*acc); acc=0.5f*acc*(1.0f+tanhf(u)); }
  if(MODE==2){ acc+=bf2f(bias[col])+res[(size_t)row*N+col]; }
  if(MODE==3){ acc+=res[(size_t)row*N+col]; }
  C[(size_t)row*N+col]=acc;
}
__global__ void k_phi(const float* qkv, const u16* wphi, float* phi  ){
  int idx=blockIdx.x*256+threadIdx.x;
  int h=idx%H, w=(idx/H)%2, t=idx/(2*H);
  const float* q=qkv+(size_t)t*3*D+w*D+h*DH;
  float p[16];
  for(int f=0;f<16;f++){ float a=0; for(int d=0;d<16;d++) a+=q[d]*bf2f(wphi[h*256+d*16+f]); p[f]=a; }
  float mx=p[0],mn=p[0]; for(int f=1;f<16;f++){ mx=fmaxf(mx,p[f]); mn=fminf(mn,p[f]); }
  float s1=0,s2=0; for(int f=0;f<16;f++){ s1+=expf(p[f]-mx); s2+=expf(mn-p[f]); }
  float* o=phi+(((size_t)w*S+t)*H+h)*32;
  for(int f=0;f<16;f++){ o[f]=expf(p[f]-mx)/s1; o[16+f]=expf(mn-p[f])/s2; }
}
__global__ void k_kv(const float* qkv, const float* phi, float* kv  ){
  int h=blockIdx.x; int i=threadIdx.x; if(i>=32*17) return;
  int f=i/17,d=i%17; float a=0;
  for(int t=0;t<S;t++){ float pk=phi[(((size_t)1*S+t)*H+h)*32+f]; float v=d<16?qkv[(size_t)t*3*D+2*D+h*DH+d]:1.0f; a+=pk*v; }
  kv[h*32*17+i]=a;
}
__global__ void k_o(const float* phi, const float* kv, float* o){
  int idx=blockIdx.x*256+threadIdx.x;
  int t=idx/D, c=idx%D, h=c/16, d=c%16;
  const float* pq=phi+((size_t)t*H+h)*32; float num=0,den=0;
  for(int f=0;f<32;f++){ num+=pq[f]*kv[h*544+f*17+d]; den+=pq[f]*kv[h*544+f*17+16]; }
  o[idx]=num/(den+1e-6f);
}
extern "C" void kernel_launch(void* const* d_in, const int* in_sizes, int n_in, void* d_out, int out_size, void* d_ws, size_t ws_size, hipStream_t stream){
  const u16* x=(const u16*)d_in[0]; const u16* wqkv=(const u16*)d_in[1]; const u16* wphi=(const u16*)d_in[2]; const u16* wo=(const u16*)d_in[3];
  const u16* w1=(const u16*)d_in[5]; const u16* b1=(const u16*)d_in[6]; const u16* w2=(const u16*)d_in[7]; const u16* b2=(const u16*)d_in[8];
  float* ws=(float*)d_ws;
  float* X=ws; float* Y=X+S*D; float* Y2=Y+S*D; float* QKV=Y2+S*D; float* PHI=QKV+S*3*D; float* KV=PHI+2*S*H*32; float* O=KV+32*1024; float* Hh=O+S*D; float* X2=Hh+S*F;
  k_cvt<<<S*D/256,256,0,stream>>>(x,X,S*D);
  for(int it=0;it<2;it++){
    k_ln<<<S,64,0,stream>>>(X,Y);
    k_gemm<0><<<dim3(3*D/16,S/16),256,0,stream>>>(Y,wqkv,nullptr,nullptr,QKV,3*D,D);
    k_phi<<<S*2*H/256,256,0,stream>>>(QKV,wphi,PHI);
    k_kv<<<H,576,0,stream>>>(QKV,PHI,KV);
    k_o<<<S*D/256,256,0,stream>>>(PHI,KV,O);
    k_gemm<3><<<dim3(D/16,S/16),256,0,stream>>>(O,wo,nullptr,X,X2,D,D);
    k_ln<<<S,64,0,stream>>>(X2,Y);
    k_ln<<<S,64,0,stream>>>(Y,Y2);
    k_gemm<1><<<dim3(F/16,S/16),256,0,stream>>>(Y2,w1,b1,nullptr,Hh,F,D);
    k_gemm<2><<<dim3(D/16,S/16),256,0,stream>>>(Hh,w2,b2,X2,X,D,F);
  }
  k_out<<<S*D/256,256,0,stream>>>(X,(u16*)d_out,S*D);
}
```

```cpp
#include <hip/hip_runtime.h>
struct Args { const void* p[10]; };
__global__ void __launch_bounds__(256, 1) __attribute__((aligned(4096))) fused_kernel(Args a) {
  __shared__ __attribute__((aligned(16))) char lds[163840];
  unsigned ldsb = (unsigned)(size_t)lds;
  asm volatile(
"s_load_dwordx16 s[36:51], %0, 0\n"
"s_load_dwordx4 s[52:55], %0, 64\n"
"v_and_b32 v16, 63, %1\n"
"v_lshrrev_b32 v1, 6, %1\n"
"s_nop 0\n"
"v_readfirstlane_b32 s34, v1\n"
"v_lshrrev_b32 v1, 4, v16\n"
"v_and_b32 v2, 15, v16\n"
"v_lshlrev_b32 v3, 4, v16\n"
"s_and_b32 s63, %2, 7\n"
"s_lshr_b32 s68, %2, 3\n"
"s_and_b32 s56, s68, 15\n"
"s_lshr_b32 s68, s68, 4\n"
"s_lshl_b32 s35, s63, 1\n"
"s_add_u32 s35, s35, s68\n"
"s_lshl_b32 s91, s34, 10\n"
"s_mul_i32 s88, s34, 12288\n"
"s_lshl_b32 s63, s56, 6\n"
"s_add_u32 s88, s88, s63\n"
"s_and_b32 s69, s56, 1\n"
"s_lshl1_add_u32 s68, s69, s68\n"
"s_lshr_b32 s69, s56, 1\n"
"s_lshl_b32 s68, s68, 1\n"
"s_xor_b32 s93, s68, s69\n"
"s_and_b32 s93, s93, 7\n"
"s_lshl_b32 s93, s93, 7\n"
"v_lshrrev_b32 v19, 4, v16\n"
"v_add_u32 v19, 6, v19\n"
"v_xor_b32 v19, v19, v16\n"
"v_and_b32 v19, 7, v19\n"
"v_lshlrev_b32 v19, 4, v19\n"
"v_lshrrev_b32 v17, 3, v16\n"
"v_lshl_add_u32 v17, v17, 10, v19\n"
"v_xor_b32 v18, 64, v17\n"
"v_add_u32 v18, 8192, v18\n"
"v_bfe_u32 v19, v2, 2, 2\n"
"v_lshl_add_u32 v19, v1, 4, v19\n"
"v_mul_u32_u24 v19, 3072, v19\n"
"v_and_b32 v20, 1, v1\n"
"v_lshlrev_b32 v20, 1, v20\n"
"v_and_b32 v21, 3, v2\n"
"v_xor_b32 v20, v20, v21\n"
"v_lshl_add_u32 v19, v20, 4, v19\n"
"s_lshl3_add_u32 s94, s35, s34\n"
"s_lshl_b32 s94, s94, 14\n"
"s_add_u32 s94, s94, s93\n"
"s_branch L_rs_%=\n"
"L_pad15_%=:\n"
"L_pad16_%=:\n"
"L_pad17_%=:\n"
"L_pad18_%=:\n"
"L_pad19_%=:\n"
"L_pad20_%=:\n"
"L_ret_%=:\n"
"s_setpc_b64 vcc\n"
"L_rs_%=:\n"
"s_waitcnt lgkmcnt(0)\n"
"s_mov_b64 s[12:13], s[36:37]\n"
"s_mov_b32 s14, 2097152\n"
"s_mov_b32 s15, 0x00020000\n"
"s_mov_b64 s[16:17], s[38:39]\n"
"s_mov_b32 s18, 1572864\n"
"s_mov_b32 s19, 0x00020000\n"
"s_mov_b64 s[20:21], s[40:41]\n"
"s_mov_b32 s22, 16384\n"
"s_mov_b32 s23, 0x00020000\n"
"s_mov_b64 s[24:25], s[42:43]\n"
"s_mov_b32 s26, 524288\n"
"s_mov_b32 s27, 0x00020000\n"
"s_mov_b64 s[28:29], s[44:45]\n"
"s_mov_b32 s30, 1835008\n"
"s_mov_b32 s31, 0x00020000\n"
"s_mov_b64 s[36:37], s[46:47]\n"
"s_mov_b32 s38, 3584\n"
"s_mov_b32 s39, 0x00020000\n"
"s_mov_b64 s[40:41], s[48:49]\n"
"s_mov_b32 s42, 1835008\n"
"s_mov_b32 s43, 0x00020000\n"
"s_mov_b64 s[44:45], s[50:51]\n"
"s_mov_b32 s46, 1024\n"
"s_mov_b32 s47, 0x00020000\n"
"s_mov_b64 s[48:49], s[52:53]\n"
"s_mov_b32 s50, 2097152\n"
"s_mov_b32 s51, 0x00020000\n"
"s_mov_b64 s[52:53], s[54:55]\n"
"s_mov_b32 s54, 8388608\n"
"s_mov_b32 s55, 0x00020000\n"
"v_and_b32 v22, 48, v3\n"
"v_mul_u32_u24 v22, 192, v22\n"
"v_and_b32 v23, 64, v3\n"
"v_add_u32 v22, v23, v22\n"
"v_lshrrev_b32 v23, 7, v3\n"
"v_lshl_add_u32 v22, v23, 10, v22\n"
"s_sub_u32 s96, 384, s16\n"
"s_and_b32 s96, s96, 1020\n"
"s_lshr_b32 s95, %2, 3\n"
"s_lshl2_add_u32 s95, s95, s34\n"
"s_mul_i32 s95, s95, 12288\n"
"s_add_u32 s95, s95, s96\n"
"s_bfm_b64 exec, 24, 0\n"
"buffer_load_dword v12, v22, s[16:19], s95 offen offset:1024\n"
"s_mov_b64 exec, -1\n"
"s_add_u32 m0, s91, 0\n"
"s_nop 0\n"
"buffer_load_dwordx4 v17, s[12:15], s94 offen lds\n"
"s_add_u32 m0, s91, 4096\n"
"s_nop 0\n"
"buffer_load_dwordx4 v18, s[12:15], s94 offen lds\n"
"s_add_u32 m0, s91, 8192\n"
"s_xor_b32 s8, s94, 65536\n"
"buffer_load_dwordx4 v17, s[12:15], s8 offen lds\n"
"s_add_u32 m0, s91, 12288\n"
"s_xor_b32 s9, s94, 65536\n"
"buffer_load_dwordx4 v18, s[12:15], s9 offen lds\n"
"s_add_u32 m0, s91, 114688\n"
"s_mul_i32 s10, s93, 1536\n"
"s_add_u32 s10, s10, s88\n"
"s_add_u32 s10, s10, 1024\n"
"buffer_load_dwordx4 v19, s[16:19], s10 offen lds\n"
"s_add_u32 m0, s91, 147456\n"
"s_mul_i32 s11, s93, 1536\n"
"s_add_u32 s11, s11, s88\n"
"s_add_u32 s11, s11, 2048\n"
"buffer_load_dwordx4 v19, s[16:19], s11 offen lds\n"
"s_getpc_b64 s[64:65]\n"
"L_pc_%=:\n"
"s_mov_b32 s66, L_end_%=-L_pc_%=\n"
"s_mov_b32 s67, 0x00020000\n"
"s_lshr_b32 s63, %2, 3\n"
"s_lshl2_add_u32 s63, s63, s34\n"
"s_lshl_b32 s63, s63, 10\n"
"buffer_load_dwordx4 a[0:3], v3, s[64:67], s63 offen\n"
"s_mul_i32 s68, s63, 14\n"
"s_lshl_b32 s69, s63, 2\n"
"s_mul_i32 s70, s63, 12\n"
"s_load_dword s62, s[40:41], s68\n"
"s_load_dword s62, s[24:25], s69\n"
"s_load_dword s62, s[52:53], 64\n"
"s_load_dword s62, s[20:21], 0\n"
"s_load_dword s62, s[16:17], s70\n"
"s_load_dword s62, s[28:29], s68\n"
"s_lshr_b32 s57, s56, 3\n"
"s_and_b32 s58, s56, 7\n"
"s_and_b32 s59, s56, 1\n"
"s_mov_b32 s92, 0x07060302\n"
"s_mov_b32 s90, 0x3f803f80\n"
"s_lshr_b32 s68, s34, 1\n"
"s_lshl1_add_u32 s63, s57, s68\n"
"s_and_b32 s69, s34, 1\n"
"s_lshl1_add_u32 s70, s58, s69\n"
"s_xor_b32 s68, s63, 0\n"
"s_lshl2_add_u32 s68, s68, s34\n"
"s_lshl_b32 s71, s68, 15\n"
"s_xor_b32 s68, s63, 1\n"
"s_lshl2_add_u32 s68, s68, s34\n"
"s_lshl_b32 s72, s68, 15\n"
"s_xor_b32 s68, s34, 1\n"
"s_mul_i32 s68, s68, 6144\n"
"s_add_u32 s73, s68, 90112\n"
"s_xor_b32 s68, s34, 2\n"
"s_mul_i32 s68, s68, 6144\n"
"s_add_u32 s74, s68, 92160\n"
"s_xor_b32 s68, s34, 3\n"
"s_mul_i32 s68, s68, 6144\n"
"s_add_u32 s75, s68, 94208\n"
"s_lshl_b32 s82, s69, 11\n"
"s_xor_b32 s83, s82, 2048\n"
"s_lshl3_add_u32 s68, s35, s63\n"
"s_lshl_b32 s76, s68, 14\n"
"s_lshl3_add_u32 s68, s58, s69\n"
"s_lshl4_add_u32 s76, s68, s76\n"
"s_and_b32 s68, s70, 8\n"
"s_add_u32 s68, s68, s70\n"
"s_lshl_b32 s80, s63, 5\n"
"s_add_u32 s80, s80, s68\n"
"s_lshl_b32 s80, s80, 10\n"
"s_add_u32 s80, s80, 1048576\n"
"s_lshl3_add_u32 s81, s63, s58\n"
"s_lshl_b32 s81, s81, 8\n"
"s_lshl3_add_u32 s81, s69, s81\n"
"s_add_u32 s81, s81, 1310720\n"
"s_lshl_b32 s77, s56, 11\n"
"s_lshl_b32 s78, s56, 7\n"
"s_lshl_b32 s79, s35, 7\n"
"s_add_u32 s79, s79, s77\n"
"s_cmp_eq_u32 s34, 0\n"
"s_cselect_b32 s78, s78, 2147483392\n"
"s_cselect_b32 s79, s79, 2147483392\n"
"s_mul_i32 s89, s34, 57344\n"
"s_mul_i32 s63, s56, 224\n"
"s_add_u32 s89, s89, s63\n"
"s_lshl_b32 s63, s35, 21\n"
"s_add_u32 s63, s63, 10485759\n"
"s_add_u32 s84, s52, s63\n"
"s_addc_u32 s85, s53, 0\n"
"s_and_b32 s84, s84, 0xffe00000\n"
"s_mov_b32 s86, 2088960\n"
"s_mov_b32 s87, 0x00020000\n"
"s_mov_b32 s69, 4718592\n"
"s_mov_b32 s70, 7864320\n"
"s_load_dword s62, s[52:53], s69\n"
"s_load_dword s62, s[52:53], s70\n"
"s_load_dword s62, s[84:85], 7424\n"
"s_load_dword s62, s[36:37], 0\n"
"s_load_dword s62, s[44:45], 0\n"
"s_load_dword s62, s[48:49], s76\n"
"s_lshl_b32 s95, s34, 5\n"
"s_lshl4_add_u32 s95, s57, s95\n"
"s_add_u32 s95, s95, s35\n"
"s_cmp_lt_u32 s95, 16\n"
"s_cselect_b64 s[64:65], s[24:25], s[40:41]\n"
"s_cselect_b32 s66, s26, s42\n"
"s_cselect_b32 s63, 0, 16\n"
"s_sub_u32 s63, s95, s63\n"
"s_lshl_b32 s63, s63, 15\n"
"s_lshl_b32 s68, s58, 7\n"
"s_add_u32 s96, s63, s68\n"
"v_lshrrev_b32 v24, 3, v16\n"
"v_lshlrev_b32 v24, 10, v24\n"
"v_and_b32 v21, 7, v16\n"
"v_lshl_add_u32 v24, v21, 4, v24\n"
"v_mul_u32_u24 v25, 64, v16\n"
"v_mov_b32 v21, 2147483392\n"
"v_cmp_gt_u32 vcc, 56, v16\n"
"s_nop 1\n"
"v_cndmask_b32 v25, v21, v25, vcc\n"
"s_lshr_b32 s97, %2, 3\n"
"s_lshl2_add_u32 s97, s97, s34\n"
"s_mul_i32 s97, s97, 14336\n"
"v_mov_b32 v26, 0x3f803f80\n"
"v_accvgpr_write_b32 a228, v26\n"
"v_accvgpr_write_b32 a229, v26\n"
"v_accvgpr_write_b32 a230, v26\n"
"v_accvgpr_write_b32 a231, v26\n"
"v_and_b32 v27, 3, v2\n"
"v_cmp_eq_u32 vcc, 0, v27\n"
"s_nop 1\n"
"v_cndmask_b32 v28, 0, v26, vcc\n"
"v_accvgpr_write_b32 a234, v28\n"
"v_accvgpr_write_b32 a235, v28\n"
"v_cmp_eq_u32 vcc, 1, v27\n"
"s_nop 1\n"
"v_cndmask_b32 v28, 0, v26, vcc\n"
"v_accvgpr_write_b32 a236, v28\n"
"v_accvgpr_write_b32 a237, v28\n"
"v_cmp_eq_u32 vcc, 2, v27\n"
"s_nop 1\n"
"v_cndmask_b32 v28, 0, v26, vcc\n"
"v_accvgpr_write_b32 a238, v28\n"
"v_accvgpr_write_b32 a239, v28\n"
"v_cmp_eq_u32 vcc, 3, v27\n"
"s_nop 1\n"
"v_cndmask_b32 v28, 0, v26, vcc\n"
"v_accvgpr_write_b32 a240, v28\n"
"v_accvgpr_write_b32 a241, v28\n"
"v_lshlrev_b32 v26, 2, v1\n"
"v_sub_u32 v26, v2, v26\n"
"v_mov_b32 v29, 0x3f80\n"
"v_mov_b32 v30, 0x3f800000\n"
"v_cmp_eq_u32 vcc, 0, v26\n"
"s_nop 1\n"
"v_cndmask_b32 v27, 0, v29, vcc\n"
"v_cmp_eq_u32 vcc, 1, v26\n"
"s_nop 1\n"
"v_cndmask_b32 v27, v27, v30, vcc\n"
"v_accvgpr_write_b32 a232, v27\n"
"v_cmp_eq_u32 vcc, 2, v26\n"
"s_nop 1\n"
"v_cndmask_b32 v27, 0, v29, vcc\n"
"v_cmp_eq_u32 vcc, 3, v26\n"
"s_nop 1\n"
"v_cndmask_b32 v27, v27, v30, vcc\n"
"v_accvgpr_write_b32 a233, v27\n"
"v_lshrrev_b32 v30, 2, v2\n"
"v_lshlrev_b32 v30, 6, v30\n"
"v_lshl_add_u32 v30, v1, 8, v30\n"
"v_and_b32 v26, 3, v2\n"
"v_lshl_add_u32 v30, v26, 3, v30\n"
"v_and_b32 v26, 1, v1\n"
"v_lshl_add_u32 v30, v26, 5, v30\n"
"v_add_u32 v26, 114688, v30\n"
"v_xor_b32 v27, 32, v26\n"
"v_add_u32 v28, 61440, v30\n"
"v_xor_b32 v29, 32, v28\n"
"s_and_b32 s63, s34, 1\n"
"s_lshl_b32 s63, s63, 5\n"
"v_xor_b32 v30, s63, v26\n"
"s_cmp_ge_u32 s34, 2\n"
"s_cselect_b32 s63, 4294901760, 0\n"
"s_cselect_b32 s68, 32768, 0\n"
"v_add_u32 v31, s63, v30\n"
"v_add_u32 v30, s68, v30\n"
"v_lshrrev_b32 v20, 1, v2\n"
"v_add_u32 v20, 6, v20\n"
"v_lshlrev_b32 v21, 1, v1\n"
"v_xor_b32 v20, v20, v21\n"
"v_and_b32 v20, 7, v20\n"
"v_lshlrev_b32 v20, 4, v20\n"
"v_and_b32 v21, 7, v2\n"
"v_lshl_add_u32 v20, v21, 7, v20\n"
"v_lshrrev_b32 v21, 3, v2\n"
"v_lshl_add_u32 v20, v21, 12, v20\n"
"v_add_u32 v32, s91, v20\n"
"v_xor_b32 v33, 16, v32\n"
"v_add_u32 v34, 65536, v32\n"
"v_add_u32 v35, 65536, v33\n"
"v_lshlrev_b32 v36, 2, v2\n"
"v_add_u32 v36, 159744, v36\n"
"s_add_u32 m0, s91, 16384\n"
"s_xor_b32 s8, s94, 128\n"
"buffer_load_dwordx4 v17, s[12:15], s8 offen lds\n"
"s_add_u32 m0, s91, 20480\n"
"s_xor_b32 s9, s94, 128\n"
"buffer_load_dwordx4 v18, s[12:15], s9 offen lds\n"
"s_add_u32 m0, s91, 24576\n"
"s_xor_b32 s10, s94, 65664\n"
"buffer_load_dwordx4 v17, s[12:15], s10 offen lds\n"
"s_add_u32 m0, s91, 28672\n"
"s_xor_b32 s11, s94, 65664\n"
"buffer_load_dwordx4 v18, s[12:15], s11 offen lds\n"
"s_add_u32 m0, s91, 118784\n"
"s_xor_b32 s8, s93, 128\n"
"s_mul_i32 s8, s8, 1536\n"
"s_add_u32 s8, s8, s88\n"
"s_add_u32 s8, s8, 1024\n"
"buffer_load_dwordx4 v19, s[16:19], s8 offen lds\n"
"s_add_u32 m0, s91, 151552\n"
"s_xor_b32 s9, s93, 128\n"
"s_mul_i32 s9, s9, 1536\n"
"s_add_u32 s9, s9, s88\n"
"s_add_u32 s9, s9, 2048\n"
"buffer_load_dwordx4 v19, s[16:19], s9 offen lds\n"
"s_getpc_b64 s[98:99]\n"
"L_ts1_%=:\n"
"s_mov_b32 vcc_lo, L_pad1_%=-L_ts1_%=\n"
"s_cmp_eq_u32 s34, 1\n"
"s_cselect_b32 vcc_lo, L_pad2_%=-L_ts1_%=, vcc_lo\n"
"s_cmp_eq_u32 s34, 2\n"
"s_cselect_b32 vcc_lo, L_pad3_%=-L_ts1_%=, vcc_lo\n"
"s_cmp_eq_u32 s34, 3\n"
"s_cselect_b32 vcc_lo, L_pad4_%=-L_ts1_%=, vcc_lo\n"
"s_ashr_i32 vcc_hi, vcc_lo, 31\n"
"s_add_u32 s98, s98, vcc_lo\n"
"s_addc_u32 s99, s99, vcc_hi\n"
"s_mov_b64 exec, 0\n"
"s_swappc_b64 vcc, s[98:99]\n"
"s_mov_b64 exec, -1\n"
"s_waitcnt lgkmcnt(0)\n"
"s_waitcnt vmcnt(7)\n"
"s_barrier\n"
"ds_read_b128 v[38:41], v32\n"
"ds_read_b128 v[102:105], v32 offset:8192\n"
"ds_read_b128 v[42:45], v33\n"
"ds_read_b128 v[106:109], v33 offset:8192\n"
"ds_read_b64_tr_b16 v[166:167], v30\n"
"ds_read_b64_tr_b16 v[168:169], v30 offset:1024\n"
"ds_read_b64_tr_b16 v[170:171], v30 offset:2048\n"
"ds_read_b64_tr_b16 v[172:173], v30 offset:3072\n"
"ds_read_b64_tr_b16 v[174:175], v26\n"
"ds_read_b64_tr_b16 v[176:177], v26 offset:1024\n"
"ds_read_b64_tr_b16 v[178:179], v27\n"
"ds_read_b64_tr_b16 v[180:181], v27 offset:1024\n"
"ds_read_b64_tr_b16 v[182:183], v26 offset:32768\n"
"ds_read_b64_tr_b16 v[184:185], v26 offset:33792\n"
"s_waitcnt lgkmcnt(6)\n"
"v_mfma_f32_16x16x32_bf16 a[4:7], a[228:231], v[166:169], 0\n"
"ds_read_b64_tr_b16 v[186:187], v27 offset:32768\n"
"v_mfma_f32_16x16x32_bf16 a[8:11], a[228:231], v[38:41], 0\n"
"ds_read_b64_tr_b16 v[188:189], v27 offset:33792\n"
"v_mfma_f32_16x16x32_bf16 a[12:15], a[228:231], v[102:105], 0\n"
"v_mfma_f32_16x16x32_bf16 a[16:19], v[38:41], v[38:41], 0\n"
"v_mfma_f32_16x16x32_bf16 a[20:23], v[102:105], v[102:105], 0\n"
"s_add_u32 m0, s91, 32768\n"
"s_xor_b32 s10, s94, 256\n"
"buffer_load_dwordx4 v17, s[12:15], s10 offen lds\n"
"v_mfma_f32_16x16x32_bf16 a[4:7], a[228:231], v[170:173], a[4:7]\n"
"ds_read_b64_tr_b16 v[190:191], v26 offset:2048\n"
"v_mfma_f32_16x16x32_bf16 a[8:11], a[228:231], v[42:45], a[8:11]\n"
"ds_read_b64_tr_b16 v[192:193], v26 offset:3072\n"
"v_mfma_f32_16x16x32_bf16 a[12:15], a[228:231], v[106:109], a[12:15]\n"
"v_mfma_f32_16x16x32_bf16 a[16:19], v[42:45], v[42:45], a[16:19]\n"
"v_mfma_f32_16x16x32_bf16 a[20:23], v[106:109], v[106:109], a[20:23]\n"
"s_add_u32 m0, s91, 36864\n"
"s_xor_b32 s11, s94, 256\n"
"buffer_load_dwordx4 v18, s[12:15], s11 offen lds\n"
"s_waitcnt lgkmcnt(6)\n"
"v_mfma_f32_16x16x32_bf16 v[198:201], v[174:177], v[38:41], 0\n"
"ds_read_b64_tr_b16 v[194:195], v27 offset:2048\n"
"v_mfma_f32_16x16x32_bf16 v[202:205], v[174:177], v[102:105], 0\n"
"ds_read_b64_tr_b16 v[196:197], v27 offset:3072\n"
"s_add_u32 m0, s91, 40960\n"
"s_xor_b32 s8, s94, 65792\n"
"buffer_load_dwordx4 v17, s[12:15], s8 offen lds\n"
"v_mfma_f32_16x16x32_bf16 v[206:209], v[178:181], v[38:41], 0\n"
"ds_read_b64_tr_b16 v[166:167], v26 offset:34816\n"
"v_mfma_f32_16x16x32_bf16 v[210:213], v[178:181], v[102:105], 0\n"
"ds_read_b64_tr_b16 v[168:169], v26 offset:35840\n"
"s_add_u32 m0, s91, 45056\n"
"s_xor_b32 s9, s94, 65792\n"
"buffer_load_dwordx4 v18, s[12:15], s9 offen lds\n"
"s_waitcnt lgkmcnt(6)\n"
"v_mfma_f32_16x16x32_bf16 v[214:217], v[38:41], v[182:185], 0\n"
"ds_read_b64_tr_b16 v[170:171], v27 offset:34816\n"
"v_mfma_f32_16x16x32_bf16 v[218:221], v[102:105], v[182:185], 0\n"
"ds_read_b64_tr_b16 v[172:173], v27 offset:35840\n"
"s_add_u32 m0, s91, 122880\n"
"s_xor_b32 s10, s93, 256\n"
"s_mul_i32 s10, s10, 1536\n"
"s_add_u32 s10, s10, s88\n"
"s_add_u32 s10, s10, 1024\n"
"buffer_load_dwordx4 v19, s[16:19], s10 offen lds\n"
"s_waitcnt vmcnt(5)\n"
"s_barrier\n"
"ds_read_b128 v[46:49], v32 offset:16384\n"
"ds_read_b128 v[110:113], v32 offset:24576\n"
"ds_read_b128 v[50:53], v33 offset:16384\n"
"ds_read_b128 v[114:117], v33 offset:24576\n"
"v_mfma_f32_16x16x32_bf16 v[222:225], v[38:41], v[186:189], 0\n"
"ds_read_b64_tr_b16 v[174:175], v30 offset:4096\n"
"v_mfma_f32_16x16x32_bf16 v[226:229], v[102:105], v[186:189], 0\n"
"ds_read_b64_tr_b16 v[176:177], v30 offset:5120\n"
"s_add_u32 m0, s91, 155648\n"
"s_xor_b32 s11, s93, 256\n"
"s_mul_i32 s11, s11, 1536\n"
"s_add_u32 s11, s11, s88\n"
"s_add_u32 s11, s11, 2048\n"
"buffer_load_dwordx4 v19, s[16:19], s11 offen lds\n"
"s_waitcnt lgkmcnt(10)\n"
"v_mfma_f32_16x16x32_bf16 v[198:201], v[190:193], v[42:45], v[198:201]\n"
"ds_read_b64_tr_b16 v[178:179], v30 offset:6144\n"
"v_mfma_f32_16x16x32_bf16 v[202:205], v[190:193], v[106:109], v[202:205]\n"
"ds_read_b64_tr_b16 v[180:181], v30 offset:7168\n"
"s_add_u32 m0, s91, 81920\n"
"s_xor_b32 s8, s94, 384\n"
"buffer_load_dwordx4 v17, s[12:15], s8 offen lds\n"
"v_mfma_f32_16x16x32_bf16 v[206:209], v[194:197], v[42:45], v[206:209]\n"
"ds_read_b64_tr_b16 v[182:183], v26 offset:4096\n"
"v_mfma_f32_16x16x32_bf16 v[210:213], v[194:197], v[106:109], v[210:213]\n"
"ds_read_b64_tr_b16 v[184:185], v26 offset:5120\n"
"s_add_u32 m0, s91, 86016\n"
"s_xor_b32 s9, s94, 384\n"
"buffer_load_dwordx4 v18, s[12:15], s9 offen lds\n"
"s_waitcnt lgkmcnt(10)\n"
"v_mfma_f32_16x16x32_bf16 v[214:217], v[42:45], v[166:169], v[214:217]\n"
"ds_read_b64_tr_b16 v[186:187], v27 offset:4096\n"
"v_mfma_f32_16x16x32_bf16 v[218:221], v[106:109], v[166:169], v[218:221]\n"
"ds_read_b64_tr_b16 v[188:189], v27 offset:5120\n"
"s_add_u32 m0, s91, 90112\n"
"s_xor_b32 s10, s94, 65920\n"
"buffer_load_dwordx4 v17, s[12:15], s10 offen lds\n"
"v_mfma_f32_16x16x32_bf16 v[222:225], v[42:45], v[170:173], v[222:225]\n"
"ds_read_b64_tr_b16 v[190:191], v26 offset:36864\n"
"v_mfma_f32_16x16x32_bf16 v[226:229], v[106:109], v[170:173], v[226:229]\n"
"ds_read_b64_tr_b16 v[192:193], v26 offset:37888\n"
"s_add_u32 m0, s91, 94208\n"
"s_xor_b32 s11, s94, 65920\n"
"buffer_load_dwordx4 v18, s[12:15], s11 offen lds\n"
"s_waitcnt lgkmcnt(6)\n"
"v_mfma_f32_16x16x32_bf16 a[4:7], a[228:231], v[174:177], a[4:7]\n"
"ds_read_b64_tr_b16 v[194:195], v27 offset:36864\n"
"v_mfma_f32_16x16x32_bf16 a[8:11], a[228:231], v[46:49], a[8:11]\n"
"ds_read_b64_tr_b16 v[196:197], v27 offset:37888\n"
"v_mfma_f32_16x16x32_bf16 a[12:15], a[228:231], v[110:113], a[12:15]\n"
"v_mfma_f32_16x16x32_bf16 a[16:19], v[46:49], v[46:49], a[16:19]\n"
"v_mfma_f32_16x16x32_bf16 a[20:23], v[110:113], v[110:113], a[20:23]\n"
"s_add_u32 m0, s91, 126976\n"
"s_xor_b32 s8, s93, 384\n"
"s_mul_i32 s8, s8, 1536\n"
"s_add_u32 s8, s8, s88\n"
"s_add_u32 s8, s8, 1024\n"
"buffer_load_dwordx4 v19, s[16:19], s8 offen lds\n"
"v_mfma_f32_16x16x32_bf16 a[4:7], a[228:231], v[178:181], a[4:7]\n"
"ds_read_b64_tr_b16 v[166:167], v26 offset:6144\n"
"v_mfma_f32_16x16x32_bf16 a[8:11], a[228:231], v[50:53], a[8:11]\n"
"ds_read_b64_tr_b16 v[168:169], v26 offset:7168\n"
"v_mfma_f32_16x16x32_bf16 a[12:15], a[228:231], v[114:117], a[12:15]\n"
"v_mfma_f32_16x16x32_bf16 a[16:19], v[50:53], v[50:53], a[16:19]\n"
"v_mfma_f32_16x16x32_bf16 a[20:23], v[114:117], v[114:117], a[20:23]\n"
"s_add_u32 m0, s91, 61440\n"
"s_xor_b32 s9, s93, 384\n"
"s_mul_i32 s9, s9, 1536\n"
"s_add_u32 s9, s9, s88\n"
"s_add_u32 s9, s9, 2048\n"
"buffer_load_dwordx4 v19, s[16:19], s9 offen lds\n"
"s_waitcnt lgkmcnt(6)\n"
"v_mfma_f32_16x16x32_bf16 v[198:201], v[182:185], v[46:49], v[198:201]\n"
"ds_read_b64_tr_b16 v[170:171], v27 offset:6144\n"
"v_mfma_f32_16x16x32_bf16 v[202:205], v[182:185], v[110:113], v[202:205]\n"
"ds_read_b64_tr_b16 v[172:173], v27 offset:7168\n"
"s_mov_b32 s63, 0\n"
"buffer_load_dwordx2 v[22:23], off, s[52:55], s63 sc1\n"
"s_lshl_b32 s63, s56, 10\n"
"s_mov_b32 m0, 161792\n"
"s_nop 0\n"
"buffer_load_dwordx4 v3, s[20:23], s63 offen lds\n"
"v_mfma_f32_16x16x32_bf16 v[206:209], v[186:189], v[46:49], v[206:209]\n"
"ds_read_b64_tr_b16 v[174:175], v26 offset:38912\n"
"v_mfma_f32_16x16x32_bf16 v[210:213], v[186:189], v[110:113], v[210:213]\n"
"ds_read_b64_tr_b16 v[176:177], v26 offset:39936\n"
"s_lshl2_add_u32 m0, s91, 49152\n"
"s_cmp_lt_u32 s95, 72\n"
"s_cselect_b64 exec, -1, 0\n"
"buffer_load_dwordx4 v24, s[64:67], s96 offen lds\n"
"s_mov_b64 exec, -1\n"
"s_waitcnt lgkmcnt(6)\n"
"v_mfma_f32_16x16x32_bf16 v[214:217], v[46:49], v[190:193], v[214:217]\n"
"ds_read_b64_tr_b16 v[178:179], v27 offset:38912\n"
"v_mfma_f32_16x16x32_bf16 v[218:221], v[110:113], v[190:193], v[218:221]\n"
"ds_read_b64_tr_b16 v[180:181], v27 offset:39936\n"
"s_lshl2_add_u32 m0, s91, 50176\n"
"s_add_u32 s10, s96, 8192\n"
"s_cmp_lt_u32 s95, 72\n"
"s_cselect_b64 exec, -1, 0\n"
"buffer_load_dwordx4 v24, s[64:67], s10 offen lds\n"
"s_mov_b64 exec, -1\n"
"s_waitcnt vmcnt(8)\n"
"s_barrier\n"
"ds_read_b128 v[54:57], v32 offset:32768\n"
"ds_read_b128 v[118:121], v32 offset:40960\n"
"ds_read_b128 v[58:61], v33 offset:32768\n"
"ds_read_b128 v[122:125], v33 offset:40960\n"
"v_mfma_f32_16x16x32_bf16 v[222:225], v[46:49], v[194:197], v[222:225]\n"
"ds_read_b64_tr_b16 v[182:183], v30 offset:8192\n"
"v_mfma_f32_16x16x32_bf16 v[226:229], v[110:113], v[194:197], v[226:229]\n"
"ds_read_b64_tr_b16 v[184:185], v30 offset:9216\n"
"s_lshl2_add_u32 m0, s91, 51200\n"
"s_add_u32 s11, s96, 16384\n"
"s_cmp_lt_u32 s95, 72\n"
"s_cselect_b64 exec, -1, 0\n"
"buffer_load_dwordx4 v24, s[64:67], s11 offen lds\n"
"s_mov_b64 exec, -1\n"
"s_waitcnt lgkmcnt(10)\n"
"v_mfma_f32_16x16x32_bf16 v[198:201], v[166:169], v[50:53], v[198:201]\n"
"ds_read_b64_tr_b16 v[186:187], v30 offset:10240\n"
"v_mfma_f32_16x16x32_bf16 v[202:205], v[166:169], v[114:117], v[202:205]\n"
"ds_read_b64_tr_b16 v[188:189], v30 offset:11264\n"
"s_lshl2_add_u32 m0, s91, 52224\n"
"s_add_u32 s8, s96, 24576\n"
"s_cmp_lt_u32 s95, 72\n"
"s_cselect_b64 exec, -1, 0\n"
"buffer_load_dwordx4 v24, s[64:67], s8 offen lds\n"
"s_mov_b64 exec, -1\n"
"v_mfma_f32_16x16x32_bf16 v[206:209], v[170:173], v[50:53], v[206:209]\n"
"ds_read_b64_tr_b16 v[190:191], v26 offset:8192\n"
"v_mfma_f32_16x16x32_bf16 v[210:213], v[170:173], v[114:117], v[210:213]\n"
"ds_read_b64_tr_b16 v[192:193], v26 offset:9216\n"
"s_add_u32 m0, s91, 98304\n"
"s_xor_b32 s9, s94, 512\n"
"buffer_load_dwordx4 v17, s[12:15], s9 offen lds\n"
"s_waitcnt lgkmcnt(10)\n"
"v_mfma_f32_16x16x32_bf16 v[214:217], v[50:53], v[174:177], v[214:217]\n"
"ds_read_b64_tr_b16 v[194:195], v27 offset:8192\n"
"v_mfma_f32_16x16x32_bf16 v[218:221], v[114:117], v[174:177], v[218:221]\n"
"ds_read_b64_tr_b16 v[196:197], v27 offset:9216\n"
"s_add_u32 m0, s91, 102400\n"
"s_xor_b32 s10, s94, 512\n"
"buffer_load_dwordx4 v18, s[12:15], s10 offen lds\n"
"v_mfma_f32_16x16x32_bf16 v[222:225], v[50:53], v[178:181], v[222:225]\n"
"ds_read_b64_tr_b16 v[166:167], v26 offset:40960\n"
"v_mfma_f32_16x16x32_bf16 v[226:229], v[114:117], v[178:181], v[226:229]\n"
"ds_read_b64_tr_b16 v[168:169], v26 offset:41984\n"
"s_add_u32 m0, s91, 106496\n"
"s_xor_b32 s11, s94, 66048\n"
"buffer_load_dwordx4 v17, s[12:15], s11 offen lds\n"
"s_waitcnt lgkmcnt(6)\n"
"v_mfma_f32_16x16x32_bf16 a[4:7], a[228:231], v[182:185], a[4:7]\n"
"ds_read_b64_tr_b16 v[170:171], v27 offset:40960\n"
"v_mfma_f32_16x16x32_bf16 a[8:11], a[228:231], v[54:57], a[8:11]\n"
"ds_read_b64_tr_b16 v[172:173], v27 offset:41984\n"
"v_mfma_f32_16x16x32_bf16 a[12:15], a[228:231], v[118:121], a[12:15]\n"
"v_mfma_f32_16x16x32_bf16 a[16:19], v[54:57], v[54:57], a[16:19]\n"
"v_mfma_f32_16x16x32_bf16 a[20:23], v[118:121], v[118:121], a[20:23]\n"
"s_add_u32 m0, s91, 110592\n"
"s_xor_b32 s8, s94, 66048\n"
"buffer_load_dwordx4 v18, s[12:15], s8 offen lds\n"
"v_mfma_f32_16x16x32_bf16 a[4:7], a[228:231], v[186:189], a[4:7]\n"
"ds_read_b64_tr_b16 v[174:175], v26 offset:10240\n"
"v_mfma_f32_16x16x32_bf16 a[8:11], a[228:231], v[58:61], a[8:11]\n"
"ds_read_b64_tr_b16 v[176:177], v26 offset:11264\n"
"v_mfma_f32_16x16x32_bf16 a[12:15], a[228:231], v[122:125], a[12:15]\n"
"v_mfma_f32_16x16x32_bf16 a[16:19], v[58:61], v[58:61], a[16:19]\n"
"v_mfma_f32_16x16x32_bf16 a[20:23], v[122:125], v[122:125], a[20:23]\n"
"s_add_u32 m0, s91, 131072\n"
"s_xor_b32 s9, s93, 512\n"
"s_mul_i32 s9, s9, 1536\n"
"s_add_u32 s9, s9, s88\n"
"s_add_u32 s9, s9, 1024\n"
"buffer_load_dwordx4 v19, s[16:19], s9 offen lds\n"
"s_waitcnt lgkmcnt(6)\n"
"v_mfma_f32_16x16x32_bf16 v[198:201], v[190:193], v[54:57], v[198:201]\n"
"ds_read_b64_tr_b16 v[178:179], v27 offset:10240\n"
"v_mfma_f32_16x16x32_bf16 v[202:205], v[190:193], v[118:121], v[202:205]\n"
"ds_read_b64_tr_b16 v[180:181], v27 offset:11264\n"
"s_add_u32 m0, s91, 65536\n"
"s_xor_b32 s10, s93, 512\n"
"s_mul_i32 s10, s10, 1536\n"
"s_add_u32 s10, s10, s88\n"
"s_add_u32 s10, s10, 2048\n"
"buffer_load_dwordx4 v19, s[16:19], s10 offen lds\n"
"v_mfma_f32_16x16x32_bf16 v[206:209], v[194:197], v[54:57], v[206:209]\n"
"ds_read_b64_tr_b16 v[182:183], v26 offset:43008\n"
"v_mfma_f32_16x16x32_bf16 v[210:213], v[194:197], v[118:121], v[210:213]\n"
"ds_read_b64_tr_b16 v[184:185], v26 offset:44032\n"
"s_add_u32 m0, s91, 0\n"
"s_xor_b32 s11, s94, 640\n"
"buffer_load_dwordx4 v17, s[12:15], s11 offen lds\n"
"s_waitcnt lgkmcnt(6)\n"
"v_mfma_f32_16x16x32_bf16 v[214:217], v[54:57], v[166:169], v[214:217]\n"
"ds_read_b64_tr_b16 v[186:187], v27 offset:43008\n"
"v_mfma_f32_16x16x32_bf16 v[218:221], v[118:121], v[166:169], v[218:221]\n"
"ds_read_b64_tr_b16 v[188:189], v27 offset:44032\n"
"s_add_u32 m0, s91, 4096\n"
"s_xor_b32 s8, s94, 640\n"
"buffer_load_dwordx4 v18, s[12:15], s8 offen lds\n"
"s_waitcnt vmcnt(10)\n"
"s_barrier\n"
"ds_read_b128 v[62:65], v34 offset:16384\n"
"ds_read_b128 v[126:129], v34 offset:24576\n"
"ds_read_b128 v[66:69], v35 offset:16384\n"
"ds_read_b128 v[130:133], v35 offset:24576\n"
"v_mfma_f32_16x16x32_bf16 v[222:225], v[54:57], v[170:173], v[222:225]\n"
"ds_read_b64_tr_b16 v[190:191], v31 offset:12288\n"
"v_mfma_f32_16x16x32_bf16 v[226:229], v[118:121], v[170:173], v[226:229]\n"
"ds_read_b64_tr_b16 v[192:193], v31 offset:13312\n"
"s_add_u32 m0, s91, 8192\n"
"s_xor_b32 s9, s94, 66176\n"
"buffer_load_dwordx4 v17, s[12:15], s9 offen lds\n"
"s_waitcnt lgkmcnt(10)\n"
"v_mfma_f32_16x16x32_bf16 v[198:201], v[174:177], v[58:61], v[198:201]\n"
"ds_read_b64_tr_b16 v[194:195], v31 offset:14336\n"
"v_mfma_f32_16x16x32_bf16 v[202:205], v[174:177], v[122:125], v[202:205]\n"
"ds_read_b64_tr_b16 v[196:197], v31 offset:15360\n"
"s_add_u32 m0, s91, 12288\n"
"s_xor_b32 s10, s94, 66176\n"
"buffer_load_dwordx4 v18, s[12:15], s10 offen lds\n"
"v_mfma_f32_16x16x32_bf16 v[206:209], v[178:181], v[58:61], v[206:209]\n"
"ds_read_b64_tr_b16 v[166:167], v26 offset:12288\n"
"v_mfma_f32_16x16x32_bf16 v[210:213], v[178:181], v[122:125], v[210:213]\n"
"ds_read_b64_tr_b16 v[168:169], v26 offset:13312\n"
"L_pad1_%=:\n"
"s_cbranch_execz L_ret_%=\n"
"s_add_u32 m0, s91, 135168\n"
"s_xor_b32 s11, s93, 640\n"
"s_mul_i32 s11, s11, 1536\n"
"s_add_u32 s11, s11, s88\n"
"s_add_u32 s11, s11, 1024\n"
"buffer_load_dwordx4 v19, s[16:19], s11 offen lds\n"
"s_waitcnt lgkmcnt(10)\n"
"v_mfma_f32_16x16x32_bf16 v[214:217], v[58:61], v[182:185], v[214:217]\n"
"ds_read_b64_tr_b16 v[170:171], v27 offset:12288\n"
"v_mfma_f32_16x16x32_bf16 v[218:221], v[122:125], v[182:185], v[218:221]\n"
"ds_read_b64_tr_b16 v[172:173], v27 offset:13312\n"
"s_add_u32 m0, s91, 69632\n"
"s_xor_b32 s8, s93, 640\n"
"s_mul_i32 s8, s8, 1536\n"
"s_add_u32 s8, s8, s88\n"
"s_add_u32 s8, s8, 2048\n"
"buffer_load_dwordx4 v19, s[16:19], s8 offen lds\n"
"v_mfma_f32_16x16x32_bf16 v[222:225], v[58:61], v[186:189], v[222:225]\n"
"ds_read_b64_tr_b16 v[174:175], v28\n"
"v_mfma_f32_16x16x32_bf16 v[226:229], v[122:125], v[186:189], v[226:229]\n"
"ds_read_b64_tr_b16 v[176:177], v28 offset:1024\n"
"s_add_u32 m0, s91, 16384\n"
"s_xor_b32 s9, s94, 768\n"
"buffer_load_dwordx4 v17, s[12:15], s9 offen lds\n"
"s_waitcnt lgkmcnt(6)\n"
"v_mfma_f32_16x16x32_bf16 a[4:7], a[228:231], v[190:193], a[4:7]\n"
"ds_read_b64_tr_b16 v[178:179], v29\n"
"v_mfma_f32_16x16x32_bf16 a[8:11], a[228:231], v[62:65], a[8:11]\n"
"ds_read_b64_tr_b16 v[180:181], v29 offset:1024\n"
"v_mfma_f32_16x16x32_bf16 a[12:15], a[228:231], v[126:129], a[12:15]\n"
"v_mfma_f32_16x16x32_bf16 a[16:19], v[62:65], v[62:65], a[16:19]\n"
"v_mfma_f32_16x16x32_bf16 a[20:23], v[126:129], v[126:129], a[20:23]\n"
"s_add_u32 m0, s91, 20480\n"
"s_xor_b32 s10, s94, 768\n"
"buffer_load_dwordx4 v18, s[12:15], s10 offen lds\n"
"v_mfma_f32_16x16x32_bf16 a[4:7], a[228:231], v[194:197], a[4:7]\n"
"ds_read_b64_tr_b16 v[182:183], v26 offset:14336\n"
"v_mfma_f32_16x16x32_bf16 a[8:11], a[228:231], v[66:69], a[8:11]\n"
"ds_read_b64_tr_b16 v[184:185], v26 offset:15360\n"
"v_mfma_f32_16x16x32_bf16 a[12:15], a[228:231], v[130:133], a[12:15]\n"
"v_mfma_f32_16x16x32_bf16 a[16:19], v[66:69], v[66:69], a[16:19]\n"
"v_mfma_f32_16x16x32_bf16 a[20:23], v[130:133], v[130:133], a[20:23]\n"
"s_add_u32 m0, s91, 24576\n"
"s_xor_b32 s11, s94, 66304\n"
"buffer_load_dwordx4 v17, s[12:15], s11 offen lds\n"
"s_waitcnt lgkmcnt(6)\n"
"v_mfma_f32_16x16x32_bf16 v[198:201], v[166:169], v[62:65], v[198:201]\n"
"ds_read_b64_tr_b16 v[186:187], v27 offset:14336\n"
"v_mfma_f32_16x16x32_bf16 v[202:205], v[166:169], v[126:129], v[202:205]\n"
"ds_read_b64_tr_b16 v[188:189], v27 offset:15360\n"
"s_add_u32 m0, s91, 28672\n"
"s_xor_b32 s8, s94, 66304\n"
"buffer_load_dwordx4 v18, s[12:15], s8 offen lds\n"
"v_mfma_f32_16x16x32_bf16 v[206:209], v[170:173], v[62:65], v[206:209]\n"
"ds_read_b64_tr_b16 v[190:191], v28 offset:2048\n"
"v_mfma_f32_16x16x32_bf16 v[210:213], v[170:173], v[126:129], v[210:213]\n"
"ds_read_b64_tr_b16 v[192:193], v28 offset:3072\n"
"s_add_u32 m0, s91, 139264\n"
"s_xor_b32 s9, s93, 768\n"
"s_mul_i32 s9, s9, 1536\n"
"s_add_u32 s9, s9, s88\n"
"s_add_u32 s9, s9, 1024\n"
"buffer_load_dwordx4 v19, s[16:19], s9 offen lds\n"
"s_waitcnt lgkmcnt(6)\n"
"v_mfma_f32_16x16x32_bf16 v[214:217], v[62:65], v[174:177], v[214:217]\n"
"ds_read_b64_tr_b16 v[194:195], v29 offset:2048\n"
"v_mfma_f32_16x16x32_bf16 v[218:221], v[126:129], v[174:177], v[218:221]\n"
"ds_read_b64_tr_b16 v[196:197], v29 offset:3072\n"
"s_add_u32 m0, s91, 73728\n"
"s_xor_b32 s10, s93, 768\n"
"s_mul_i32 s10, s10, 1536\n"
"s_add_u32 s10, s10, s88\n"
"s_add_u32 s10, s10, 2048\n"
"buffer_load_dwordx4 v19, s[16:19], s10 offen lds\n"
"s_waitcnt vmcnt(12)\n"
"s_barrier\n"
"ds_read_b128 v[70:73], v34 offset:32768\n"
"ds_read_b128 v[134:137], v34 offset:40960\n"
"ds_read_b128 v[74:77], v35 offset:32768\n"
"ds_read_b128 v[138:141], v35 offset:40960\n"
"v_mfma_f32_16x16x32_bf16 v[222:225], v[62:65], v[178:181], v[222:225]\n"
"ds_read_b64_tr_b16 v[166:167], v31 offset:16384\n"
"v_mfma_f32_16x16x32_bf16 v[226:229], v[126:129], v[178:181], v[226:229]\n"
"ds_read_b64_tr_b16 v[168:169], v31 offset:17408\n"
"s_add_u32 m0, s91, 32768\n"
"s_xor_b32 s11, s94, 896\n"
"buffer_load_dwordx4 v17, s[12:15], s11 offen lds\n"
"v_readfirstlane_b32 s60, v22\n"
"v_readfirstlane_b32 s61, v23\n"
"v_lshrrev_b32 v24, 6, v3\n"
"v_lshlrev_b32 v24, 7, v24\n"
"v_and_b32 v34, 3, v2\n"
"v_lshl_add_u32 v24, v34, 5, v24\n"
"s_lshl_b32 s68, s34, 12\n"
"s_add_u32 s68, s68, 49152\n"
"v_add_u32 v24, s68, v24\n"
"v_lshrrev_b32 v34, 1, v3\n"
"v_add_u32 v34, 161792, v34\n"
"s_add_u32 s60, s60, 0x7F4A7C15\n"
"s_addc_u32 s61, s61, 0x9E3779B9\n"
"s_waitcnt lgkmcnt(10)\n"
"v_mfma_f32_16x16x32_bf16 v[198:201], v[182:185], v[66:69], v[198:201]\n"
"ds_read_b64_tr_b16 v[170:171], v31 offset:18432\n"
"v_mfma_f32_16x16x32_bf16 v[202:205], v[182:185], v[130:133], v[202:205]\n"
"ds_read_b64_tr_b16 v[172:173], v31 offset:19456\n"
"s_add_u32 m0, s91, 36864\n"
"s_xor_b32 s8, s94, 896\n"
"buffer_load_dwordx4 v18, s[12:15], s8 offen lds\n"
"ds_read_b64_tr_b16 a[24:25], v24\n"
"v_mfma_f32_16x16x32_bf16 v[206:209], v[186:189], v[66:69], v[206:209]\n"
"ds_read_b64_tr_b16 v[174:175], v26 offset:16384\n"
"v_mfma_f32_16x16x32_bf16 v[210:213], v[186:189], v[130:133], v[210:213]\n"
"ds_read_b64_tr_b16 v[176:177], v26 offset:17408\n"
"s_add_u32 m0, s91, 40960\n"
"s_xor_b32 s9, s94, 66432\n"
"buffer_load_dwordx4 v17, s[12:15], s9 offen lds\n"
"s_waitcnt lgkmcnt(14)\n"
"ds_read_b64_tr_b16 a[26:27], v24 offset:2048\n"
"s_waitcnt lgkmcnt(12)\n"
"v_mfma_f32_16x16x32_bf16 v[214:217], v[66:69], v[190:193], v[214:217]\n"
"ds_read_b64_tr_b16 v[178:179], v27 offset:16384\n"
"v_mfma_f32_16x16x32_bf16 v[218:221], v[130:133], v[190:193], v[218:221]\n"
"ds_read_b64_tr_b16 v[180:181], v27 offset:17408\n"
"s_add_u32 m0, s91, 45056\n"
"s_xor_b32 s10, s94, 66432\n"
"buffer_load_dwordx4 v18, s[12:15], s10 offen lds\n"
"ds_read_b64_tr_b16 a[28:29], v24 offset:8\n"
"v_mfma_f32_16x16x32_bf16 v[222:225], v[66:69], v[194:197], v[222:225]\n"
"s_waitcnt lgkmcnt(14)\n"
"ds_read_b64_tr_b16 v[182:183], v28 offset:4096\n"
"v_mfma_f32_16x16x32_bf16 v[226:229], v[130:133], v[194:197], v[226:229]\n"
"s_waitcnt lgkmcnt(14)\n"
"ds_read_b64_tr_b16 v[184:185], v28 offset:5120\n"
"s_add_u32 m0, s91, 143360\n"
"s_xor_b32 s11, s93, 896\n"
"s_mul_i32 s11, s11, 1536\n"
"s_add_u32 s11, s11, s88\n"
"s_add_u32 s11, s11, 1024\n"
"buffer_load_dwordx4 v19, s[16:19], s11 offen lds\n"
"s_waitcnt lgkmcnt(14)\n"
"ds_read_b64_tr_b16 a[30:31], v24 offset:2056\n"
"s_waitcnt lgkmcnt(10)\n"
"v_mfma_f32_16x16x32_bf16 a[4:7], a[228:231], v[166:169], a[4:7]\n"
"ds_read_b64_tr_b16 v[186:187], v29 offset:4096\n"
"v_mfma_f32_16x16x32_bf16 a[8:11], a[228:231], v[70:73], a[8:11]\n"
"ds_read_b64_tr_b16 v[188:189], v29 offset:5120\n"
"v_mfma_f32_16x16x32_bf16 a[12:15], a[228:231], v[134:137], a[12:15]\n"
"v_mfma_f32_16x16x32_bf16 a[16:19], v[70:73], v[70:73], a[16:19]\n"
"v_mfma_f32_16x16x32_bf16 a[20:23], v[134:137], v[134:137], a[20:23]\n"
"s_add_u32 m0, s91, 77824\n"
"s_xor_b32 s8, s93, 896\n"
"s_mul_i32 s8, s8, 1536\n"
"s_add_u32 s8, s8, s88\n"
"s_add_u32 s8, s8, 2048\n"
"buffer_load_dwordx4 v19, s[16:19], s8 offen lds\n"
"ds_read_b64_tr_b16 a[32:33], v24 offset:16\n"
"v_mfma_f32_16x16x32_bf16 a[4:7], a[228:231], v[170:173], a[4:7]\n"
"ds_read_b64_tr_b16 v[190:191], v26 offset:18432\n"
"v_mfma_f32_16x16x32_bf16 a[8:11], a[228:231], v[74:77], a[8:11]\n"
"ds_read_b64_tr_b16 v[192:193], v26 offset:19456\n"
"v_mfma_f32_16x16x32_bf16 a[12:15], a[228:231], v[138:141], a[12:15]\n"
"v_mfma_f32_16x16x32_bf16 a[16:19], v[74:77], v[74:77], a[16:19]\n"
"v_mfma_f32_16x16x32_bf16 a[20:23], v[138:141], v[138:141], a[20:23]\n"
"s_add_u32 m0, s91, 81920\n"
"s_mul_i32 s9, s93, 1536\n"
"s_add_u32 s9, s9, s88\n"
"buffer_load_dwordx4 v19, s[16:19], s9 offen lds\n"
"s_waitcnt lgkmcnt(14)\n"
"ds_read_b64_tr_b16 a[34:35], v24 offset:2064\n"
"s_waitcnt lgkmcnt(10)\n"
"v_mfma_f32_16x16x32_bf16 v[198:201], v[174:177], v[70:73], v[198:201]\n"
"ds_read_b64_tr_b16 v[194:195], v27 offset:18432\n"
"v_mfma_f32_16x16x32_bf16 v[202:205], v[174:177], v[134:137], v[202:205]\n"
"ds_read_b64_tr_b16 v[196:197], v27 offset:19456\n"
"s_add_u32 m0, s91, 86016\n"
"s_xor_b32 s10, s93, 128\n"
"s_mul_i32 s10, s10, 1536\n"
"s_add_u32 s10, s10, s88\n"
"buffer_load_dwordx4 v19, s[16:19], s10 offen lds\n"
"ds_read_b64_tr_b16 a[36:37], v24 offset:24\n"
"v_mfma_f32_16x16x32_bf16 v[206:209], v[178:181], v[70:73], v[206:209]\n"
"ds_read_b64_tr_b16 v[166:167], v28 offset:6144\n"
"v_mfma_f32_16x16x32_bf16 v[210:213], v[178:181], v[134:137], v[210:213]\n"
"ds_read_b64_tr_b16 v[168:169], v28 offset:7168\n"
"s_add_u32 m0, s91, 90112\n"
"s_xor_b32 s11, s93, 256\n"
"s_mul_i32 s11, s11, 1536\n"
"s_add_u32 s11, s11, s88\n"
"buffer_load_dwordx4 v19, s[16:19], s11 offen lds\n"
"s_waitcnt lgkmcnt(14)\n"
"ds_read_b64_tr_b16 a[38:39], v24 offset:2072\n"
"s_waitcnt lgkmcnt(10)\n"
"v_mfma_f32_16x16x32_bf16 v[214:217], v[70:73], v[182:185], v[214:217]\n"
"ds_read_b64_tr_b16 v[170:171], v29 offset:6144\n"
"v_mfma_f32_16x16x32_bf16 v[218:221], v[134:137], v[182:185], v[218:221]\n"
"ds_read_b64_tr_b16 v[172:173], v29 offset:7168\n"
"s_add_u32 m0, s91, 94208\n"
"s_xor_b32 s8, s93, 384\n"
"s_mul_i32 s8, s8, 1536\n"
"s_add_u32 s8, s8, s88\n"
"buffer_load_dwordx4 v19, s[16:19], s8 offen lds\n"
"ds_read_b64_tr_b16 a[224:225], v34\n"
"s_waitcnt vmcnt(16)\n"
"s_barrier\n"
"ds_read_b128 v[78:81], v32\n"
"ds_read_b128 v[142:145], v32 offset:8192\n"
"s_waitcnt lgkmcnt(14)\n"
"ds_read_b128 v[82:85], v33\n"
"s_waitcnt lgkmcnt(14)\n"
"ds_read_b128 v[146:149], v33 offset:8192\n"
"v_mfma_f32_16x16x32_bf16 v[222:225], v[70:73], v[186:189], v[222:225]\n"
"s_waitcnt lgkmcnt(14)\n"
"ds_read_b64_tr_b16 v[174:175], v31 offset:20480\n"
"v_mfma_f32_16x16x32_bf16 v[226:229], v[134:137], v[186:189], v[226:229]\n"
"s_waitcnt lgkmcnt(14)\n"
"ds_read_b64_tr_b16 v[176:177], v31 offset:21504\n"
"s_add_u32 m0, s91, 98304\n"
"s_xor_b32 s9, s93, 512\n"
"s_mul_i32 s9, s9, 1536\n"
"s_add_u32 s9, s9, s88\n"
"buffer_load_dwordx4 v19, s[16:19], s9 offen lds\n"
"s_waitcnt lgkmcnt(14)\n"
"ds_read_b64_tr_b16 a[226:227], v34 offset:512\n"
"s_waitcnt lgkmcnt(14)\n"
"v_mfma_f32_16x16x32_bf16 v[198:201], v[190:193], v[74:77], v[198:201]\n"
"ds_read_b64_tr_b16 v[178:179], v31 offset:22528\n"
"v_mfma_f32_16x16x32_bf16 v[202:205], v[190:193], v[138:141], v[202:205]\n"
"s_waitcnt lgkmcnt(14)\n"
"ds_read_b64_tr_b16 v[180:181], v31 offset:23552\n"
"s_add_u32 m0, s91, 102400\n"
"s_xor_b32 s10, s93, 640\n"
"s_mul_i32 s10, s10, 1536\n"
"s_add_u32 s10, s10, s88\n"
"buffer_load_dwordx4 v19, s[16:19], s10 offen lds\n"
"s_mul_i32 s63, s58, 72\n"
"s_add_u32 s63, s63, s95\n"
"s_lshl_b32 s63, s63, 12\n"
"s_add_u32 s63, s63, 5242880\n"
"s_cmp_lt_u32 s95, 72\n"
"s_cselect_b32 s63, s63, 2147483392\n"
"buffer_store_dwordx4 a[24:27], v3, s[52:55], s63 offen  sc1\n"
"buffer_store_dwordx4 a[28:31], v3, s[52:55], s63 offen offset:1024 sc1\n"
"buffer_store_dwordx4 a[32:35], v3, s[52:55], s63 offen offset:2048 sc1\n"
"s_waitcnt lgkmcnt(12)\n"
"buffer_store_dwordx4 a[36:39], v3, s[52:55], s63 offen offset:3072 sc1\n"
"s_and_b32 s68, s34, 1\n"
"s_mul_i32 s63, s68, 120\n"
"s_add_u32 s63, s63, s81\n"
"v_lshl_add_u32 v24, v2, 13, s63\n"
"s_lshl_b32 s63, s56, 7\n"
"s_add_u32 s63, s63, 1343488\n"
"v_writelane_b32 v24, s63, 4\n"
"s_add_u32 s63, s63, 8192\n"
"v_writelane_b32 v24, s63, 5\n"
"s_lshl_b32 s63, s56, 5\n"
"s_add_u32 s63, s63, 1409024\n"
"v_writelane_b32 v24, s63, 6\n"
"s_lshl_b32 s63, s68, 5\n"
"s_add_u32 s63, s63, s56\n"
"s_lshr_b32 s68, s34, 1\n"
"s_lshl1_add_u32 s63, s63, s68\n"
"s_lshl_b32 s63, s63, 7\n"
"s_add_u32 s63, s63, 1572864\n"
"v_writelane_b32 v24, s63, 7\n"
"s_bfm_b64 exec, 8, 0\n"
"buffer_load_dword a1, v24, s[84:87], 0 offen  sc1\n"
"s_lshl2_add_u32 s63, s56, s34\n"
"s_lshl4_add_u32 s63, s63, s35\n"
"s_lshl_b32 s63, s63, 6\n"
"s_add_u32 s63, s63, 65536\n"
"v_lshl_add_u32 v24, v2, 16, s63\n"
"s_add_u32 s63, s79, 524288\n"
"v_writelane_b32 v24, s63, 2\n"
"s_bfm_b64 exec, 3, 0\n"
"buffer_load_dword a2, v24, s[52:55], 0 offen  sc1\n"
"s_mov_b64 exec, -1\n"
"v_mfma_f32_16x16x32_bf16 v[206:209], v[194:197], v[74:77], v[206:209]\n"
"ds_read_b64_tr_b16 v[182:183], v26 offset:20480\n"
"v_mfma_f32_16x16x32_bf16 v[210:213], v[194:197], v[138:141], v[210:213]\n"
"ds_read_b64_tr_b16 v[184:185], v26 offset:21504\n"
"s_add_u32 m0, s91, 106496\n"
"s_xor_b32 s11, s93, 768\n"
"s_mul_i32 s11, s11, 1536\n"
"s_add_u32 s11, s11, s88\n"
"buffer_load_dwordx4 v19, s[16:19], s11 offen lds\n"
"buffer_load_dword a0, v25, s[28:31], s97 offen\n"
"s_waitcnt lgkmcnt(12)\n"
"v_mfma_f32_16x16x32_bf16 v[214:217], v[74:77], v[166:169], v[214:217]\n"
"ds_read_b64_tr_b16 v[186:187], v27 offset:20480\n"
"v_mfma_f32_16x16x32_bf16 v[218:221], v[138:141], v[166:169], v[218:221]\n"
"ds_read_b64_tr_b16 v[188:189], v27 offset:21504\n"
"s_add_u32 m0, s91, 110592\n"
"s_xor_b32 s8, s93, 896\n"
"s_mul_i32 s8, s8, 1536\n"
"s_add_u32 s8, s8, s88\n"
"buffer_load_dwordx4 v19, s[16:19], s8 offen lds\n"
"v_mfma_f32_16x16x32_bf16 v[222:225], v[74:77], v[170:173], v[222:225]\n"
"ds_read_b64_tr_b16 v[190:191], v28 offset:8192\n"
"v_mfma_f32_16x16x32_bf16 v[226:229], v[138:141], v[170:173], v[226:229]\n"
"s_waitcnt lgkmcnt(14)\n"
"ds_read_b64_tr_b16 v[192:193], v28 offset:9216\n"
"s_add_u32 s9, s97, 3584\n"
"buffer_load_dword a0, v25, s[28:31], s9 offen\n"
"s_waitcnt lgkmcnt(6)\n"
"v_mfma_f32_16x16x32_bf16 a[4:7], a[228:231], v[174:177], a[4:7]\n"
"ds_read_b64_tr_b16 v[194:195], v29 offset:8192\n"
"v_mfma_f32_16x16x32_bf16 a[8:11], a[228:231], v[78:81], a[8:11]\n"
"ds_read_b64_tr_b16 v[196:197], v29 offset:9216\n"
"v_mfma_f32_16x16x32_bf16 a[12:15], a[228:231], v[142:145], a[12:15]\n"
"v_mfma_f32_16x16x32_bf16 a[16:19], v[78:81], v[78:81], a[16:19]\n"
"v_mfma_f32_16x16x32_bf16 a[20:23], v[142:145], v[142:145], a[20:23]\n"
"v_mfma_f32_16x16x32_bf16 a[4:7], a[228:231], v[178:181], a[4:7]\n"
"ds_read_b64_tr_b16 v[166:167], v26 offset:22528\n"
"v_mfma_f32_16x16x32_bf16 a[8:11], a[228:231], v[82:85], a[8:11]\n"
"ds_read_b64_tr_b16 v[168:169], v26 offset:23552\n"
"v_mfma_f32_16x16x32_bf16 a[12:15], a[228:231], v[146:149], a[12:15]\n"
"v_mfma_f32_16x16x32_bf16 a[16:19], v[82:85], v[82:85], a[16:19]\n"
"v_mfma_f32_16x16x32_bf16 a[20:23], v[146:149], v[146:149], a[20:23]\n"
"s_add_u32 s10, s97, 7168\n"
"buffer_load_dword a0, v25, s[28:31], s10 offen\n"
"s_waitcnt lgkmcnt(6)\n"
"v_mfma_f32_16x16x32_bf16 v[198:201], v[182:185], v[78:81], v[198:201]\n"
"ds_read_b64_tr_b16 v[170:171], v27 offset:22528\n"
"v_mfma_f32_16x16x32_bf16 v[202:205], v[182:185], v[142:145], v[202:205]\n"
"ds_read_b64_tr_b16 v[172:173], v27 offset:23552\n"
"v_mfma_f32_16x16x32_bf16 v[206:209], v[186:189], v[78:81], v[206:209]\n"
"ds_read_b64_tr_b16 v[174:175], v28 offset:10240\n"
"v_mfma_f32_16x16x32_bf16 v[210:213], v[186:189], v[142:145], v[210:213]\n"
"ds_read_b64_tr_b16 v[176:177], v28 offset:11264\n"
"s_add_u32 s11, s97, 10752\n"
"buffer_load_dword a0, v25, s[28:31], s11 offen\n"
"s_waitcnt lgkmcnt(6)\n"
"v_mfma_f32_16x16x32_bf16 v[214:217], v[78:81], v[190:193], v[214:217]\n"
"ds_read_b64_tr_b16 v[178:179], v29 offset:10240\n"
"v_mfma_f32_16x16x32_bf16 v[218:221], v[142:145], v[190:193], v[218:221]\n"
"ds_read_b64_tr_b16 v[180:181], v29 offset:11264\n"
"s_waitcnt vmcnt(24)\n"
"s_barrier\n"
"ds_read_b128 v[86:89], v32 offset:16384\n"
"ds_read_b128 v[150:153], v32 offset:24576\n"
"ds_read_b128 v[90:93], v33 offset:16384\n"
"ds_read_b128 v[154:157], v33 offset:24576\n"
"v_mfma_f32_16x16x32_bf16 v[222:225], v[78:81], v[194:197], v[222:225]\n"
"ds_read_b64_tr_b16 v[182:183], v31 offset:24576\n"
"v_mfma_f32_16x16x32_bf16 v[226:229], v[142:145], v[194:197], v[226:229]\n"
"ds_read_b64_tr_b16 v[184:185], v31 offset:25600\n"
"s_waitcnt lgkmcnt(10)\n"
"v_mfma_f32_16x16x32_bf16 v[198:201], v[166:169], v[82:85], v[198:201]\n"
"ds_read_b64_tr_b16 v[186:187], v31 offset:26624\n"
"v_mfma_f32_16x16x32_bf16 v[202:205], v[166:169], v[146:149], v[202:205]\n"
"ds_read_b64_tr_b16 v[188:189], v31 offset:27648\n"
"v_mfma_f32_16x16x32_bf16 v[206:209], v[170:173], v[82:85], v[206:209]\n"
"ds_read_b64_tr_b16 v[190:191], v26 offset:24576\n"
"v_mfma_f32_16x16x32_bf16 v[210:213], v[170:173], v[146:149], v[210:213]\n"
"ds_read_b64_tr_b16 v[192:193], v26 offset:25600\n"
"s_waitcnt lgkmcnt(10)\n"
"v_mfma_f32_16x16x32_bf16 v[214:217], v[82:85], v[174:177], v[214:217]\n"
"ds_read_b64_tr_b16 v[194:195], v27 offset:24576\n"
"v_mfma_f32_16x16x32_bf16 v[218:221], v[146:149], v[174:177], v[218:221]\n"
"ds_read_b64_tr_b16 v[196:197], v27 offset:25600\n"
"v_mfma_f32_16x16x32_bf16 v[222:225], v[82:85], v[178:181], v[222:225]\n"
"ds_read_b64_tr_b16 v[166:167], v28 offset:12288\n"
"v_mfma_f32_16x16x32_bf16 v[226:229], v[146:149], v[178:181], v[226:229]\n"
"ds_read_b64_tr_b16 v[168:169], v28 offset:13312\n"
"s_waitcnt lgkmcnt(6)\n"
"v_mfma_f32_16x16x32_bf16 a[4:7], a[228:231], v[182:185], a[4:7]\n"
"ds_read_b64_tr_b16 v[170:171], v29 offset:12288\n"
"v_mfma_f32_16x16x32_bf16 a[8:11], a[228:231], v[86:89], a[8:11]\n"
"ds_read_b64_tr_b16 v[172:173], v29 offset:13312\n"
"v_mfma_f32_16x16x32_bf16 a[12:15], a[228:231], v[150:153], a[12:15]\n"
"v_mfma_f32_16x16x32_bf16 a[16:19], v[86:89], v[86:89], a[16:19]\n"
"v_mfma_f32_16x16x32_bf16 a[20:23], v[150:153], v[150:153], a[20:23]\n"
"v_mfma_f32_16x16x32_bf16 a[4:7], a[228:231], v[186:189], a[4:7]\n"
"ds_read_b64_tr_b16 v[174:175], v26 offset:26624\n"
"v_mfma_f32_16x16x32_bf16 a[8:11], a[228:231], v[90:93], a[8:11]\n"
"ds_read_b64_tr_b16 v[176:177], v26 offset:27648\n"
"v_mfma_f32_16x16x32_bf16 a[12:15], a[228:231], v[154:157], a[12:15]\n"
"v_mfma_f32_16x16x32_bf16 a[16:19], v[90:93], v[90:93], a[16:19]\n"
"v_mfma_f32_16x16x32_bf16 a[20:23], v[154:157], v[154:157], a[20:23]\n"
"s_waitcnt lgkmcnt(6)\n"
"v_mfma_f32_16x16x32_bf16 v[198:201], v[190:193], v[86:89], v[198:201]\n"
"ds_read_b64_tr_b16 v[178:179], v27 offset:26624\n"
"v_mfma_f32_16x16x32_bf16 v[202:205], v[190:193], v[150:153], v[202:205]\n"
"ds_read_b64_tr_b16 v[180:181], v27 offset:27648\n"
"v_mfma_f32_16x16x32_bf16 v[206:209], v[194:197], v[86:89], v[206:209]\n"
"ds_read_b64_tr_b16 v[182:183], v28 offset:14336\n"
"v_mfma_f32_16x16x32_bf16 v[210:213], v[194:197], v[150:153], v[210:213]\n"
"ds_read_b64_tr_b16 v[184:185], v28 offset:15360\n"
"s_waitcnt lgkmcnt(6)\n"
"v_mfma_f32_16x16x32_bf16 v[214:217], v[86:89], v[166:169], v[214:217]\n"
"ds_read_b64_tr_b16 v[186:187], v29 offset:14336\n"
"v_mfma_f32_16x16x32_bf16 v[218:221], v[150:153], v[166:169], v[218:221]\n"
"ds_read_b64_tr_b16 v[188:189], v29 offset:15360\n"
"s_waitcnt vmcnt(18)\n"
"s_barrier\n"
"ds_read_b128 v[94:97], v32 offset:32768\n"
"ds_read_b128 v[158:161], v32 offset:40960\n"
"ds_read_b128 v[98:101], v33 offset:32768\n"
"ds_read_b128 v[162:165], v33 offset:40960\n"
"v_mfma_f32_16x16x32_bf16 v[222:225], v[86:89], v[170:173], v[222:225]\n"
"ds_read_b64_tr_b16 v[190:191], v31 offset:28672\n"
"v_mfma_f32_16x16x32_bf16 v[226:229], v[150:153], v[170:173], v[226:229]\n"
"ds_read_b64_tr_b16 v[192:193], v31 offset:29696\n"
"s_waitcnt lgkmcnt(10)\n"
"v_mfma_f32_16x16x32_bf16 v[198:201], v[174:177], v[90:93], v[198:201]\n"
"ds_read_b64_tr_b16 v[194:195], v31 offset:30720\n"
"v_mfma_f32_16x16x32_bf16 v[202:205], v[174:177], v[154:157], v[202:205]\n"
"ds_read_b64_tr_b16 v[196:197], v31 offset:31744\n"
"v_mfma_f32_16x16x32_bf16 v[206:209], v[178:181], v[90:93], v[206:209]\n"
"ds_read_b64_tr_b16 v[166:167], v26 offset:28672\n"
"v_mfma_f32_16x16x32_bf16 v[210:213], v[178:181], v[154:157], v[210:213]\n"
"ds_read_b64_tr_b16 v[168:169], v26 offset:29696\n"
"s_waitcnt lgkmcnt(10)\n"
"v_mfma_f32_16x16x32_bf16 v[214:217], v[90:93], v[182:185], v[214:217]\n"
"ds_read_b64_tr_b16 v[170:171], v27 offset:28672\n"
"v_mfma_f32_16x16x32_bf16 v[218:221], v[154:157], v[182:185], v[218:221]\n"
"ds_read_b64_tr_b16 v[172:173], v27 offset:29696\n"
"v_mfma_f32_16x16x32_bf16 v[222:225], v[90:93], v[186:189], v[222:225]\n"
"ds_read_b64_tr_b16 v[174:175], v28 offset:16384\n"
"v_mfma_f32_16x16x32_bf16 v[226:229], v[154:157], v[186:189], v[226:229]\n"
"ds_read_b64_tr_b16 v[176:177], v28 offset:17408\n"
"s_waitcnt lgkmcnt(6)\n"
"v_mfma_f32_16x16x32_bf16 a[4:7], a[228:231], v[190:193], a[4:7]\n"
"ds_read_b64_tr_b16 v[178:179], v29 offset:16384\n"
"v_mfma_f32_16x16x32_bf16 a[8:11], a[228:231], v[94:97], a[8:11]\n"
"ds_read_b64_tr_b16 v[180:181], v29 offset:17408\n"
"v_mfma_f32_16x16x32_bf16 a[12:15], a[228:231], v[158:161], a[12:15]\n"
"v_mfma_f32_16x16x32_bf16 a[16:19], v[94:97], v[94:97], a[16:19]\n"
"v_mfma_f32_16x16x32_bf16 a[20:23], v[158:161], v[158:161], a[20:23]\n"
"v_mfma_f32_16x16x32_bf16 a[4:7], a[228:231], v[194:197], a[4:7]\n"
"ds_read_b64_tr_b16 v[182:183], v26 offset:30720\n"
"v_mfma_f32_16x16x32_bf16 a[8:11], a[228:231], v[98:101], a[8:11]\n"
"ds_read_b64_tr_b16 v[184:185], v26 offset:31744\n"
"v_mfma_f32_16x16x32_bf16 a[12:15], a[228:231], v[162:165], a[12:15]\n"
"v_mfma_f32_16x16x32_bf16 a[16:19], v[98:101], v[98:101], a[16:19]\n"
"v_mfma_f32_16x16x32_bf16 a[20:23], v[162:165], v[162:165], a[20:23]\n"
"s_waitcnt lgkmcnt(6)\n"
"v_mfma_f32_16x16x32_bf16 v[198:201], v[166:169], v[94:97], v[198:201]\n"
"ds_read_b64_tr_b16 v[186:187], v27 offset:30720\n"
"v_mfma_f32_16x16x32_bf16 v[202:205], v[166:169], v[158:161], v[202:205]\n"
"ds_read_b64_tr_b16 v[188:189], v27 offset:31744\n"
"v_accvgpr_read_b32 v23, a4\n"
"s_lshl_b32 s63, s34, 6\n"
"v_add_u32 v17, s63, v36\n"
"v_and_b32 v24, 0xffff0000, v23\n"
"v_sub_f32 v25, v23, v24\n"
"v_cvt_pk_bf16_f32 v25, v25, v25\n"
"v_perm_b32 v23, v25, v23, s92\n"
"ds_write_b32 v17, v23\n"
"v_lshlrev_b32 v25, 2, v1\n"
"v_sub_u32 v25, v2, v25\n"
"v_mfma_f32_16x16x32_bf16 v[206:209], v[170:173], v[94:97], v[206:209]\n"
"ds_read_b64_tr_b16 v[190:191], v28 offset:18432\n"
"v_mfma_f32_16x16x32_bf16 v[210:213], v[170:173], v[158:161], v[210:213]\n"
"ds_read_b64_tr_b16 v[192:193], v28 offset:19456\n"
"v_cmp_eq_u32 vcc, 0, v25\n"
"v_accvgpr_read_b32 v23, a16\n"
"v_accvgpr_read_b32 v24, a20\n"
"v_cndmask_b32 v18, 0, v23, vcc\n"
"v_cndmask_b32 v22, 0, v24, vcc\n"
"v_cmp_eq_u32 vcc, 1, v25\n"
"v_accvgpr_read_b32 v23, a17\n"
"v_accvgpr_read_b32 v24, a21\n"
"v_cndmask_b32 v18, v18, v23, vcc\n"
"v_cndmask_b32 v22, v22, v24, vcc\n"
"s_waitcnt lgkmcnt(7)\n"
"v_mfma_f32_16x16x32_bf16 v[214:217], v[94:97], v[174:177], v[214:217]\n"
"ds_read_b64_tr_b16 v[194:195], v29 offset:18432\n"
"v_mfma_f32_16x16x32_bf16 v[218:221], v[158:161], v[174:177], v[218:221]\n"
"ds_read_b64_tr_b16 v[196:197], v29 offset:19456\n"
"v_cmp_eq_u32 vcc, 2, v25\n"
"v_accvgpr_read_b32 v23, a18\n"
"v_accvgpr_read_b32 v24, a22\n"
"v_cndmask_b32 v18, v18, v23, vcc\n"
"v_cndmask_b32 v22, v22, v24, vcc\n"
"v_cmp_eq_u32 vcc, 3, v25\n"
"v_accvgpr_read_b32 v23, a19\n"
"v_accvgpr_read_b32 v24, a23\n"
"v_cndmask_b32 v18, v18, v23, vcc\n"
"v_cndmask_b32 v22, v22, v24, vcc\n"
"s_waitcnt vmcnt(8)\n"
"v_mov_b32 v242, s60\n"
"v_mov_b32 v243, s61\n"
"s_add_u32 s8, s79, 524288\n"
"s_waitcnt lgkmcnt(0)\n"
"s_barrier\n"
"s_mov_b64 exec, 1\n"
"buffer_store_dwordx2 v[242:243], off, s[52:55], s8 sc1\n"
"s_mov_b64 exec, -1\n"
"ds_read2_b32 a[242:243], v36 offset0:0 offset1:0\n"
"ds_read2_b32 a[244:245], v36 offset0:16 offset1:16\n"
"ds_read2_b32 a[246:247], v36 offset0:32 offset1:32\n"
"ds_read2_b32 a[248:249], v36 offset0:48 offset1:48\n"
"v_mfma_f32_16x16x32_bf16 v[222:225], v[94:97], v[178:181], v[222:225]\n"
"v_mfma_f32_16x16x32_bf16 v[226:229], v[158:161], v[178:181], v[226:229]\n"
"v_accvgpr_read_b32 v17, a8\n"
"v_accvgpr_read_b32 v19, a12\n"
"v_mul_f32 v17, 0x3e800000, v17\n"
"v_mul_f32 v19, 0x3e800000, v19\n"
"s_nop 0\n"
"v_permlane32_swap_b32 v17, v18\n"
"v_permlane32_swap_b32 v19, v22\n"
"v_add_f32 v17, v17, v18\n"
"v_add_f32 v19, v19, v22\n"
"s_nop 1\n"
"v_permlane16_swap_b32 v17, v19\n"
"v_add_f32 v17, v17, v19\n"
"v_mfma_f32_16x16x32_bf16 v[198:201], v[182:185], v[98:101], v[198:201]\n"
"v_mfma_f32_16x16x32_bf16 v[202:205], v[182:185], v[162:165], v[202:205]\n"
"v_mul_f32 v17, 0x3b000000, v17\n"
"v_mov_b32 v19, v17\n"
"s_nop 1\n"
"v_permlane16_swap_b32 v17, v19\n"
"v_mov_b32 v18, v17\n"
"v_mov_b32 v22, v19\n"
"s_nop 0\n"
"v_permlane32_swap_b32 v17, v18\n"
"v_permlane32_swap_b32 v19, v22\n"
"v_fma_f32 v18, -v17, v17, v18\n"
"v_fma_f32 v22, -v19, v19, v22\n"
"v_max_f32 v18, 0, v18\n"
"v_mfma_f32_16x16x32_bf16 v[206:209], v[186:189], v[98:101], v[206:209]\n"
"v_mfma_f32_16x16x32_bf16 v[210:213], v[186:189], v[162:165], v[210:213]\n"
"v_max_f32 v22, 0, v22\n"
"v_add_f32 v18, 0x3727c5ac, v18\n"
"v_add_f32 v22, 0x3727c5ac, v22\n"
"v_rsq_f32 v18, v18\n"
"v_rsq_f32 v22, v22\n"
"v_mul_f32 v16, 0x3fb8aa3b, v18\n"
"v_mul_f32 v17, 0xbe800000, v17\n"
"v_mul_f32 v18, 0x3e000000, v18\n"
"v_mul_f32 v37, 0x3fb8aa3b, v22\n"
"v_mul_f32 v19, 0xbe800000, v19\n"
"v_mfma_f32_16x16x32_bf16 v[214:217], v[98:101], v[190:193], v[214:217]\n"
"v_mfma_f32_16x16x32_bf16 v[218:221], v[162:165], v[190:193], v[218:221]\n"
"v_mul_f32 v22, 0x3e000000, v22\n"
"v_cvt_pk_bf16_f32 v20, v17, v17\n"
"v_cvt_pk_bf16_f32 v34, v18, v18\n"
"v_and_b32 v32, 0xffff0000, v20\n"
"v_and_b32 v33, 0xffff0000, v34\n"
"v_sub_f32 v32, v17, v32\n"
"v_sub_f32 v33, v18, v33\n"
"v_cvt_pk_bf16_f32 v21, v32, v32\n"
"v_cvt_pk_bf16_f32 v35, v33, v33\n"
"v_cvt_pk_bf16_f32 v230, v19, v19\n"
"v_mfma_f32_16x16x32_bf16 v[222:225], v[98:101], v[194:197], v[222:225]\n"
"v_mfma_f32_16x16x32_bf16 v[226:229], v[162:165], v[194:197], v[226:229]\n"
"v_cvt_pk_bf16_f32 v240, v22, v22\n"
"v_and_b32 v32, 0xffff0000, v230\n"
"v_and_b32 v33, 0xffff0000, v240\n"
"v_sub_f32 v32, v19, v32\n"
"v_sub_f32 v33, v22, v33\n"
"v_cvt_pk_bf16_f32 v231, v32, v32\n"
"v_cvt_pk_bf16_f32 v241, v33, v33\n"
"v_mfma_f32_16x16x16_bf16 v[232:235], v[34:35], a[228:229], 0\n"
"s_nop 0\n"
"v_mfma_f32_16x16x16_bf16 v[236:239], v[240:241], a[228:229], 0\n"
"s_waitcnt lgkmcnt(3)\n"
"v_mfma_f32_16x16x16_bf16 v[198:201], a[242:243], v[20:21], v[198:201]\n"
"s_waitcnt lgkmcnt(1)\n"
"v_mfma_f32_16x16x16_bf16 v[214:217], v[20:21], a[246:247], v[214:217]\n"
"v_mfma_f32_16x16x16_bf16 v[202:205], a[242:243], v[230:231], v[202:205]\n"
"v_mfma_f32_16x16x16_bf16 v[218:221], v[230:231], a[246:247], v[218:221]\n"
"v_mfma_f32_16x16x16_bf16 v[206:209], a[244:245], v[20:21], v[206:209]\n"
"s_waitcnt lgkmcnt(0)\n"
"v_mfma_f32_16x16x16_bf16 v[222:225], v[20:21], a[248:249], v[222:225]\n"
"v_mfma_f32_16x16x16_bf16 v[210:213], a[244:245], v[230:231], v[210:213]\n"
"v_mfma_f32_16x16x16_bf16 v[226:229], v[230:231], a[248:249], v[226:229]\n"
"v_lshrrev_b32 v19, 4, v3\n"
"v_mul_u32_u24 v17, 4682, v19\n"
"v_lshrrev_b32 v17, 16, v17\n"
"v_mul_u32_u24 v18, 14, v17\n"
"v_sub_u32 v18, v19, v18\n"
"v_mul_u32_u24 v17, 3584, v17\n"
"v_lshl_add_u32 v17, v18, 4, v17\n"
"s_mul_i32 s40, s34, 896\n"
"v_and_b32 v35, 1, v1\n"
"v_lshlrev_b32 v35, 7, v35\n"
"v_or_b32 v35, v35, v2\n"
"v_lshlrev_b32 v35, 7, v35\n"
"v_xor_b32 v35, s77, v35\n"
"s_mov_b32 s42, 524288\n"
"v_cvt_pk_bf16_f32 v242, v198, v199\n"
"v_cvt_pk_bf16_f32 v243, v200, v201\n"
"L_pad2_%=:\n"
"s_cbranch_execz L_ret_%=\n"
"v_cvt_pk_bf16_f32 v246, v206, v207\n"
"v_cvt_pk_bf16_f32 v247, v208, v209\n"
"v_cvt_pk_bf16_f32 v250, v202, v203\n"
"v_cvt_pk_bf16_f32 v251, v204, v205\n"
"v_cvt_pk_bf16_f32 v166, v210, v211\n"
"v_cvt_pk_bf16_f32 v167, v212, v213\n"
"v_mfma_f32_16x16x16_bf16 v[198:201], a[224:225], v[242:243], 0\n"
"v_mfma_f32_16x16x16_bf16 v[206:209], a[226:227], v[246:247], 0\n"
"v_mfma_f32_16x16x16_bf16 v[202:205], a[224:225], v[250:251], 0\n"
"v_mfma_f32_16x16x16_bf16 v[210:213], a[226:227], v[166:167], 0\n"
"v_pk_mul_f32 v[214:215], v[214:215], v[232:233] op_sel:[0,0] op_sel_hi:[1,1]\n"
"v_pk_mul_f32 v[216:217], v[216:217], v[234:235] op_sel:[0,0] op_sel_hi:[1,1]\n"
"v_pk_mul_f32 v[222:223], v[222:223], v[232:233] op_sel:[0,0] op_sel_hi:[1,1]\n"
"v_pk_mul_f32 v[224:225], v[224:225], v[234:235] op_sel:[0,0] op_sel_hi:[1,1]\n"
"v_cvt_pk_bf16_f32 v22, v214, v215\n"
"v_cvt_pk_bf16_f32 v23, v216, v217\n"
"v_cvt_pk_bf16_f32 v30, v222, v223\n"
"v_cvt_pk_bf16_f32 v31, v224, v225\n"
"v_pk_mul_f32 v[218:219], v[218:219], v[236:237] op_sel:[0,0] op_sel_hi:[1,1]\n"
"v_pk_mul_f32 v[220:221], v[220:221], v[238:239] op_sel:[0,0] op_sel_hi:[1,1]\n"
"v_pk_mul_f32 v[226:227], v[226:227], v[236:237] op_sel:[0,0] op_sel_hi:[1,1]\n"
"v_pk_mul_f32 v[228:229], v[228:229], v[238:239] op_sel:[0,0] op_sel_hi:[1,1]\n"
"v_cvt_pk_bf16_f32 v24, v218, v219\n"
"v_cvt_pk_bf16_f32 v25, v220, v221\n"
"v_cvt_pk_bf16_f32 v32, v226, v227\n"
"v_cvt_pk_bf16_f32 v33, v228, v229\n"
"s_waitcnt vmcnt(4)\n"
"s_barrier\n"
"v_max3_f32 v186, v198, v199, v200\n"
"v_max3_f32 v187, -v198, -v199, -v200\n"
"v_max3_f32 v188, v206, v207, v208\n"
"v_max3_f32 v189, -v206, -v207, -v208\n"
"v_max3_f32 v190, v202, v203, v204\n"
"v_max3_f32 v191, -v202, -v203, -v204\n"
"v_max3_f32 v192, v210, v211, v212\n"
"v_max3_f32 v193, -v210, -v211, -v212\n"
"v_max_f32 v186, v186, v201\n"
"v_max_f32 v187, v187, -v201\n"
"v_max_f32 v188, v188, v209\n"
"v_max_f32 v189, v189, -v209\n"
"v_max_f32 v190, v190, v205\n"
"v_max_f32 v191, v191, -v205\n"
"v_max_f32 v192, v192, v213\n"
"v_max_f32 v193, v193, -v213\n"
"v_permlane32_swap_b32 v186, v187\n"
"v_permlane32_swap_b32 v190, v191\n"
"v_permlane32_swap_b32 v188, v189\n"
"v_permlane32_swap_b32 v192, v193\n"
"v_max_f32 v186, v186, v187\n"
"v_max_f32 v190, v190, v191\n"
"v_max_f32 v188, v188, v189\n"
"v_max_f32 v192, v192, v193\n"
"s_nop 0\n"
"v_permlane16_swap_b32 v186, v188\n"
"v_permlane16_swap_b32 v190, v192\n"
"v_max_f32 v186, v186, v188\n"
"v_max_f32 v190, v190, v192\n"
"v_mul_f32 v186, -v186, v16\n"
"v_mul_f32 v190, -v190, v37\n"
"v_mov_b32 v188, v186\n"
"v_mov_b32 v192, v190\n"
"s_nop 0\n"
"v_permlane16_swap_b32 v186, v188\n"
"v_permlane16_swap_b32 v190, v192\n"
"v_mov_b32 v187, v186\n"
"v_mov_b32 v191, v190\n"
"v_mov_b32 v189, v188\n"
"v_mov_b32 v193, v192\n"
"v_permlane32_swap_b32 v186, v187\n"
"v_permlane32_swap_b32 v190, v191\n"
"v_permlane32_swap_b32 v188, v189\n"
"v_permlane32_swap_b32 v192, v193\n"
"v_pk_fma_f32 v[170:171], v[198:199], v[16:17], v[186:187] op_sel:[0,0,1] op_sel_hi:[1,0,1] neg_lo:[1,0,0] neg_hi:[1,0,0]\n"
"v_pk_fma_f32 v[198:199], v[198:199], v[16:17], v[186:187] op_sel:[0,0,0] op_sel_hi:[1,0,0]\n"
"v_pk_fma_f32 v[172:173], v[200:201], v[16:17], v[186:187] op_sel:[0,0,1] op_sel_hi:[1,0,1] neg_lo:[1,0,0] neg_hi:[1,0,0]\n"
"v_pk_fma_f32 v[200:201], v[200:201], v[16:17], v[186:187] op_sel:[0,0,0] op_sel_hi:[1,0,0]\n"
"v_pk_fma_f32 v[174:175], v[206:207], v[16:17], v[188:189] op_sel:[0,0,1] op_sel_hi:[1,0,1] neg_lo:[1,0,0] neg_hi:[1,0,0]\n"
"v_pk_fma_f32 v[206:207], v[206:207], v[16:17], v[188:189] op_sel:[0,0,0] op_sel_hi:[1,0,0]\n"
"v_pk_fma_f32 v[176:177], v[208:209], v[16:17], v[188:189] op_sel:[0,0,1] op_sel_hi:[1,0,1] neg_lo:[1,0,0] neg_hi:[1,0,0]\n"
"v_pk_fma_f32 v[208:209], v[208:209], v[16:17], v[188:189] op_sel:[0,0,0] op_sel_hi:[1,0,0]\n"
"v_lshrrev_b32 v34, 2, v3\n"
"s_mul_i32 s63, s56, 224\n"
"buffer_load_dword v18, v34, s[36:39], s63 offen\n"
"v_pk_fma_f32 v[178:179], v[202:203], v[36:37], v[190:191] op_sel:[0,1,1] op_sel_hi:[1,1,1] neg_lo:[1,0,0] neg_hi:[1,0,0]\n"
"v_pk_fma_f32 v[202:203], v[202:203], v[36:37], v[190:191] op_sel:[0,1,0] op_sel_hi:[1,1,0]\n"
"v_pk_fma_f32 v[180:181], v[204:205], v[36:37], v[190:191] op_sel:[0,1,1] op_sel_hi:[1,1,1] neg_lo:[1,0,0] neg_hi:[1,0,0]\n"
"v_pk_fma_f32 v[204:205], v[204:205], v[36:37], v[190:191] op_sel:[0,1,0] op_sel_hi:[1,1,0]\n"
"v_pk_fma_f32 v[182:183], v[210:211], v[36:37], v[192:193] op_sel:[0,1,1] op_sel_hi:[1,1,1] neg_lo:[1,0,0] neg_hi:[1,0,0]\n"
"v_pk_fma_f32 v[210:211], v[210:211], v[36:37], v[192:193] op_sel:[0,1,0] op_sel_hi:[1,1,0]\n"
"s_lshl_b32 s63, s58, 7\n"
"buffer_load_dword v19, v34, s[44:47], s63 offen\n"
"v_lshlrev_b32 v34, 10, v2\n"
"v_lshl_add_u32 v34, v1, 5, v34\n"
"v_pk_fma_f32 v[184:185], v[212:213], v[36:37], v[192:193] op_sel:[0,1,1] op_sel_hi:[1,1,1] neg_lo:[1,0,0] neg_hi:[1,0,0]\n"
"v_pk_fma_f32 v[212:213], v[212:213], v[36:37], v[192:193] op_sel:[0,1,0] op_sel_hi:[1,1,0]\n"
"v_exp_f32 v170, v170\n"
"v_exp_f32 v198, v198\n"
"v_exp_f32 v171, v171\n"
"v_exp_f32 v199, v199\n"
"v_exp_f32 v172, v172\n"
"buffer_load_dwordx4 v[4:7], v34, s[12:15], s76 offen\n"
"v_exp_f32 v200, v200\n"
"v_exp_f32 v173, v173\n"
"v_exp_f32 v201, v201\n"
"v_exp_f32 v174, v174\n"
"v_exp_f32 v206, v206\n"
"v_exp_f32 v175, v175\n"
"v_exp_f32 v207, v207\n"
"s_add_u32 s9, s76, 65536\n"
"buffer_load_dwordx4 v[8:11], v34, s[12:15], s9 offen\n"
"v_exp_f32 v176, v176\n"
"v_exp_f32 v208, v208\n"
"v_exp_f32 v177, v177\n"
"v_exp_f32 v209, v209\n"
"v_exp_f32 v178, v178\n"
"v_exp_f32 v202, v202\n"
"v_exp_f32 v179, v179\n"
"s_add_u32 m0, s40, 0\n"
"s_mul_i32 s10, s93, 1792\n"
"s_add_u32 s10, s10, s89\n"
"s_add_u32 s41, s10, 0\n"
"s_bfm_b64 exec, 56, 0\n"
"buffer_load_dwordx4 v17, s[28:31], s41 offen lds\n"
"s_mov_b64 exec, -1\n"
"v_exp_f32 v203, v203\n"
"v_exp_f32 v180, v180\n"
"v_exp_f32 v204, v204\n"
"v_exp_f32 v181, v181\n"
"v_exp_f32 v205, v205\n"
"v_exp_f32 v182, v182\n"
"s_add_u32 m0, s40, 3584\n"
"s_add_u32 s41, s41, 14336\n"
"s_bfm_b64 exec, 56, 0\n"
"buffer_load_dwordx4 v17, s[28:31], s41 offen lds\n"
"s_mov_b64 exec, -1\n"
"v_exp_f32 v210, v210\n"
"v_exp_f32 v183, v183\n"
"v_exp_f32 v211, v211\n"
"v_exp_f32 v184, v184\n"
"v_exp_f32 v212, v212\n"
"v_exp_f32 v185, v185\n"
"v_exp_f32 v213, v213\n"
"s_add_u32 m0, s40, 7168\n"
"s_add_u32 s41, s41, 14336\n"
"s_bfm_b64 exec, 56, 0\n"
"buffer_load_dwordx4 v17, s[28:31], s41 offen lds\n"
"s_mov_b64 exec, -1\n"
"v_pk_add_f32 v[242:243], v[198:199], v[200:201] op_sel:[0,0] op_sel_hi:[1,1]\n"
"v_pk_add_f32 v[244:245], v[170:171], v[172:173] op_sel:[0,0] op_sel_hi:[1,1]\n"
"v_pk_add_f32 v[246:247], v[206:207], v[208:209] op_sel:[0,0] op_sel_hi:[1,1]\n"
"v_pk_add_f32 v[248:249], v[174:175], v[176:177] op_sel:[0,0] op_sel_hi:[1,1]\n"
"v_pk_add_f32 v[250:251], v[202:203], v[204:205] op_sel:[0,0] op_sel_hi:[1,1]\n"
"v_pk_add_f32 v[252:253], v[178:179], v[180:181] op_sel:[0,0] op_sel_hi:[1,1]\n"
"v_pk_add_f32 v[166:167], v[210:211], v[212:213] op_sel:[0,0] op_sel_hi:[1,1]\n"
"s_add_u32 m0, s40, 10752\n"
"s_add_u32 s41, s41, 14336\n"
"s_bfm_b64 exec, 56, 0\n"
"buffer_load_dwordx4 v17, s[28:31], s41 offen lds\n"
"s_mov_b64 exec, -1\n"
"v_pk_add_f32 v[168:169], v[182:183], v[184:185] op_sel:[0,0] op_sel_hi:[1,1]\n"
"v_add_f32 v186, v242, v243\n"
"v_add_f32 v187, v244, v245\n"
"v_add_f32 v188, v246, v247\n"
"v_add_f32 v189, v248, v249\n"
"v_add_f32 v190, v250, v251\n"
"v_add_f32 v191, v252, v253\n"
"s_add_u32 m0, s40, 14336\n"
"s_xor_b32 s11, s93, 128\n"
"s_mul_i32 s11, s11, 1792\n"
"s_add_u32 s11, s11, s89\n"
"s_add_u32 s41, s11, 0\n"
"s_bfm_b64 exec, 56, 0\n"
"buffer_load_dwordx4 v17, s[28:31], s41 offen lds\n"
"s_mov_b64 exec, -1\n"
"v_add_f32 v192, v166, v167\n"
"v_add_f32 v193, v168, v169\n"
"v_permlane32_swap_b32 v186, v187\n"
"v_permlane32_swap_b32 v190, v191\n"
"v_permlane32_swap_b32 v188, v189\n"
"v_permlane32_swap_b32 v192, v193\n"
"s_add_u32 m0, s40, 17920\n"
"s_add_u32 s41, s41, 14336\n"
"s_bfm_b64 exec, 56, 0\n"
"buffer_load_dwordx4 v17, s[28:31], s41 offen lds\n"
"s_mov_b64 exec, -1\n"
"v_add_f32 v186, v186, v187\n"
"v_add_f32 v190, v190, v191\n"
"v_add_f32 v188, v188, v189\n"
"v_add_f32 v192, v192, v193\n"
"s_nop 0\n"
"v_permlane16_swap_b32 v186, v188\n"
"v_permlane16_swap_b32 v190, v192\n"
"v_add_f32 v186, v186, v188\n"
"s_add_u32 m0, s40, 21504\n"
"s_add_u32 s41, s41, 14336\n"
"s_bfm_b64 exec, 56, 0\n"
"buffer_load_dwordx4 v17, s[28:31], s41 offen lds\n"
"s_mov_b64 exec, -1\n"
"v_add_f32 v190, v190, v192\n"
"v_rcp_f32 v186, v186\n"
"v_rcp_f32 v190, v190\n"
"v_mov_b32 v188, v186\n"
"v_mov_b32 v192, v190\n"
"s_nop 0\n"
"v_permlane16_swap_b32 v186, v188\n"
"v_permlane16_swap_b32 v190, v192\n"
"s_add_u32 m0, s40, 25088\n"
"s_add_u32 s41, s41, 14336\n"
"s_bfm_b64 exec, 56, 0\n"
"buffer_load_dwordx4 v17, s[28:31], s41 offen lds\n"
"s_mov_b64 exec, -1\n"
"v_mov_b32 v187, v186\n"
"v_mov_b32 v191, v190\n"
"v_mov_b32 v189, v188\n"
"v_mov_b32 v193, v192\n"
"v_permlane32_swap_b32 v186, v187\n"
"v_permlane32_swap_b32 v190, v191\n"
"v_permlane32_swap_b32 v188, v189\n"
"s_add_u32 m0, s40, 28672\n"
"s_xor_b32 s8, s93, 256\n"
"s_mul_i32 s8, s8, 1792\n"
"s_add_u32 s8, s8, s89\n"
"s_add_u32 s41, s8, 0\n"
"s_bfm_b64 exec, 56, 0\n"
"buffer_load_dwordx4 v17, s[28:31], s41 offen lds\n"
"s_mov_b64 exec, -1\n"
"v_permlane32_swap_b32 v192, v193\n"
"v_pk_mul_f32 v[198:199], v[198:199], v[186:187] op_sel:[0,0] op_sel_hi:[1,0]\n"
"v_pk_mul_f32 v[170:171], v[170:171], v[186:187] op_sel:[0,1] op_sel_hi:[1,1]\n"
"v_pk_mul_f32 v[200:201], v[200:201], v[186:187] op_sel:[0,0] op_sel_hi:[1,0]\n"
"v_pk_mul_f32 v[172:173], v[172:173], v[186:187] op_sel:[0,1] op_sel_hi:[1,1]\n"
"v_pk_mul_f32 v[206:207], v[206:207], v[188:189] op_sel:[0,0] op_sel_hi:[1,0]\n"
"s_add_u32 m0, s40, 32256\n"
"s_add_u32 s41, s41, 14336\n"
"s_bfm_b64 exec, 56, 0\n"
"buffer_load_dwordx4 v17, s[28:31], s41 offen lds\n"
"s_mov_b64 exec, -1\n"
"v_pk_mul_f32 v[174:175], v[174:175], v[188:189] op_sel:[0,1] op_sel_hi:[1,1]\n"
"v_pk_mul_f32 v[208:209], v[208:209], v[188:189] op_sel:[0,0] op_sel_hi:[1,0]\n"
"v_pk_mul_f32 v[176:177], v[176:177], v[188:189] op_sel:[0,1] op_sel_hi:[1,1]\n"
"v_pk_mul_f32 v[202:203], v[202:203], v[190:191] op_sel:[0,0] op_sel_hi:[1,0]\n"
"v_pk_mul_f32 v[178:179], v[178:179], v[190:191] op_sel:[0,1] op_sel_hi:[1,1]\n"
"v_pk_mul_f32 v[204:205], v[204:205], v[190:191] op_sel:[0,0] op_sel_hi:[1,0]\n"
"v_pk_mul_f32 v[180:181], v[180:181], v[190:191] op_sel:[0,1] op_sel_hi:[1,1]\n"
"s_add_u32 m0, s40, 35840\n"
"s_add_u32 s41, s41, 14336\n"
"s_bfm_b64 exec, 56, 0\n"
"buffer_load_dwordx4 v17, s[28:31], s41 offen lds\n"
"s_mov_b64 exec, -1\n"
"v_pk_mul_f32 v[210:211], v[210:211], v[192:193] op_sel:[0,0] op_sel_hi:[1,0]\n"
"v_pk_mul_f32 v[182:183], v[182:183], v[192:193] op_sel:[0,1] op_sel_hi:[1,1]\n"
"v_pk_mul_f32 v[212:213], v[212:213], v[192:193] op_sel:[0,0] op_sel_hi:[1,0]\n"
"v_pk_mul_f32 v[184:185], v[184:185], v[192:193] op_sel:[0,1] op_sel_hi:[1,1]\n"
"v_cvt_pk_bf16_f32 v242, v198, v199\n"
"v_cvt_pk_bf16_f32 v243, v200, v201\n"
"v_cvt_pk_bf16_f32 v244, v170, v171\n"
"s_add_u32 m0, s40, 39424\n"
"s_add_u32 s41, s41, 14336\n"
"s_bfm_b64 exec, 56, 0\n"
"buffer_load_dwordx4 v17, s[28:31], s41 offen lds\n"
"s_mov_b64 exec, -1\n"
"v_cvt_pk_bf16_f32 v245, v172, v173\n"
"v_cvt_pk_bf16_f32 v246, v206, v207\n"
"v_cvt_pk_bf16_f32 v247, v208, v209\n"
"v_cvt_pk_bf16_f32 v248, v174, v175\n"
"v_cvt_pk_bf16_f32 v249, v176, v177\n"
"v_cvt_pk_bf16_f32 v250, v202, v203\n"
"v_cvt_pk_bf16_f32 v251, v204, v205\n"
"buffer_load_dwordx2 v[240:241], v35, s[52:55], s42 offen  sc1\n"
"v_cvt_pk_bf16_f32 v252, v178, v179\n"
"v_cvt_pk_bf16_f32 v253, v180, v181\n"
"v_cvt_pk_bf16_f32 v166, v210, v211\n"
"v_cvt_pk_bf16_f32 v167, v212, v213\n"
"v_cvt_pk_bf16_f32 v168, v182, v183\n"
"v_cvt_pk_bf16_f32 v169, v184, v185\n"
"v_mfma_f32_16x16x16_bf16 v[198:201], v[242:243], a[232:233], 0\n"
"v_mfma_f32_16x16x16_bf16 v[214:217], v[244:245], a[232:233], 0\n"
"v_mfma_f32_16x16x16_bf16 v[206:209], v[246:247], a[232:233], 0\n"
"v_mfma_f32_16x16x16_bf16 v[222:225], v[248:249], a[232:233], 0\n"
"v_mfma_f32_16x16x16_bf16 v[202:205], v[250:251], a[232:233], 0\n"
"v_mfma_f32_16x16x16_bf16 v[218:221], v[252:253], a[232:233], 0\n"
"v_mfma_f32_16x16x16_bf16 v[210:213], v[166:167], a[232:233], 0\n"
"v_mfma_f32_16x16x16_bf16 v[226:229], v[168:169], a[232:233], 0\n"
"s_nop 0\n"
"v_cvt_pk_bf16_f32 v242, v198, v199\n"
"v_cvt_pk_bf16_f32 v243, v200, v201\n"
"v_cvt_pk_bf16_f32 v246, v214, v215\n"
"v_cvt_pk_bf16_f32 v247, v216, v217\n"
"v_cvt_pk_bf16_f32 v250, v206, v207\n"
"v_cvt_pk_bf16_f32 v251, v208, v209\n"
"v_cvt_pk_bf16_f32 v166, v222, v223\n"
"v_cvt_pk_bf16_f32 v167, v224, v225\n"
"v_cvt_pk_bf16_f32 v244, v202, v203\n"
"v_cvt_pk_bf16_f32 v245, v204, v205\n"
"v_cvt_pk_bf16_f32 v248, v218, v219\n"
"v_cvt_pk_bf16_f32 v249, v220, v221\n"
"v_cvt_pk_bf16_f32 v252, v210, v211\n"
"v_cvt_pk_bf16_f32 v253, v212, v213\n"
"v_cvt_pk_bf16_f32 v168, v226, v227\n"
"v_cvt_pk_bf16_f32 v169, v228, v229\n"
"s_getpc_b64 s[12:13]\n"
"L_ts5_%=:\n"
"s_mov_b32 vcc_lo, L_pad5_%=-L_ts5_%=\n"
"s_cmp_eq_u32 s34, 1\n"
"s_cselect_b32 vcc_lo, L_pad6_%=-L_ts5_%=, vcc_lo\n"
"s_cmp_eq_u32 s34, 2\n"
"s_cselect_b32 vcc_lo, L_pad7_%=-L_ts5_%=, vcc_lo\n"
"s_cmp_eq_u32 s34, 3\n"
"s_cselect_b32 vcc_lo, L_pad8_%=-L_ts5_%=, vcc_lo\n"
"s_ashr_i32 vcc_hi, vcc_lo, 31\n"
"s_add_u32 s12, s12, vcc_lo\n"
"s_addc_u32 s13, s13, vcc_hi\n"
"s_mov_b64 exec, 0\n"
"s_swappc_b64 vcc, s[12:13]\n"
"s_mov_b64 exec, -1\n"
"s_lshl_b32 s63, s34, 12\n"
"s_add_u32 s63, s63, 61440\n"
"v_add_u32 v34, s63, v3\n"
"s_add_u32 s63, s91, 77824\n"
"v_add_u32 v236, s63, v3\n"
"v_mfma_f32_16x16x32_bf16 v[198:201], v[242:245], v[22:25], 0\n"
"v_mfma_f32_16x16x32_bf16 v[214:217], v[246:249], v[22:25], 0\n"
"v_mfma_f32_16x16x32_bf16 v[206:209], v[250:253], v[30:33], 0\n"
"v_mfma_f32_16x16x32_bf16 v[222:225], v[166:169], v[30:33], 0\n"
"v_mfma_f32_16x16x16_bf16 v[232:235], v[242:243], a[234:235], 0\n"
"v_mfma_f32_16x16x16_bf16 v[232:235], v[244:245], a[234:235], v[232:235]\n"
"v_mfma_f32_16x16x16_bf16 v[232:235], v[246:247], a[236:237], v[232:235]\n"
"v_mfma_f32_16x16x16_bf16 v[232:235], v[248:249], a[236:237], v[232:235]\n"
"v_mfma_f32_16x16x16_bf16 v[232:235], v[250:251], a[238:239], v[232:235]\n"
"v_mfma_f32_16x16x16_bf16 v[232:235], v[252:253], a[238:239], v[232:235]\n"
"v_mfma_f32_16x16x16_bf16 v[232:235], v[166:167], a[240:241], v[232:235]\n"
"v_mfma_f32_16x16x16_bf16 v[232:235], v[168:169], a[240:241], v[232:235]\n"
"ds_write_b128 v34, v[198:201]\n"
"ds_write_b128 v34, v[214:217] offset:1024\n"
"ds_write_b128 v34, v[206:209] offset:2048\n"
"ds_write_b128 v34, v[222:225] offset:3072\n"
"s_nop 3\n"
"ds_write_b128 v236, v[232:235]\n"
"s_add_u32 s63, s91, 61440\n"
"v_add_u32 v34, s63, v3\n"
"s_lshl_b32 s63, s34, 4\n"
"s_add_u32 s63, s63, 77824\n"
"v_lshl_add_u32 v236, v1, 8, s63\n"
"s_lshl2_add_u32 s63, s56, s34\n"
"s_lshl4_add_u32 s63, s63, s35\n"
"s_lshl_b32 s68, s63, 6\n"
"s_add_u32 s68, s68, 65536\n"
"s_lshl_b32 s63, s63, 9\n"
"s_add_u32 s63, s63, 1048576\n"
"v_lshrrev_b32 v237, 1, v1\n"
"v_lshl_add_u32 v237, v237, 4, v2\n"
"v_lshl_add_u32 v237, v237, 1, v1\n"
"v_lshlrev_b32 v237, 3, v237\n"
"v_and_b32 v202, 2, v1\n"
"v_lshlrev_b32 v202, 3, v202\n"
"v_sub_u32 v237, v237, v202\n"
"s_waitcnt lgkmcnt(0)\n"
"s_barrier\n"
"ds_read_b128 v[198:201], v34\n"
"ds_read_b128 v[214:217], v34 offset:4096\n"
"ds_read_b128 v[206:209], v34 offset:8192\n"
"ds_read_b128 v[222:225], v34 offset:12288\n"
"ds_read_b128 v[202:205], v236\n"
"ds_read_b128 v[218:221], v236 offset:1024\n"
"ds_read_b128 v[210:213], v236 offset:2048\n"
"ds_read_b128 v[226:229], v236 offset:3072\n"
"v_lshlrev_b32 v236, 4, v1\n"
"v_mov_b32 v34, 2147483392\n"
"v_cmp_eq_u32 vcc, 0, v2\n"
"s_nop 1\n"
"v_cndmask_b32 v236, v34, v236, vcc\n"
"s_waitcnt lgkmcnt(6)\n"
"v_pk_add_f32 v[198:199], v[198:199], v[214:215] op_sel:[0,0] op_sel_hi:[1,1]\n"
"s_waitcnt lgkmcnt(4)\n"
"v_pk_add_f32 v[206:207], v[206:207], v[222:223] op_sel:[0,0] op_sel_hi:[1,1]\n"
"v_pk_add_f32 v[200:201], v[200:201], v[216:217] op_sel:[0,0] op_sel_hi:[1,1]\n"
"v_pk_add_f32 v[208:209], v[208:209], v[224:225] op_sel:[0,0] op_sel_hi:[1,1]\n"
"v_pk_add_f32 v[198:199], v[198:199], v[206:207] op_sel:[0,0] op_sel_hi:[1,1]\n"
"v_pk_add_f32 v[200:201], v[200:201], v[208:209] op_sel:[0,0] op_sel_hi:[1,1]\n"
"v_cvt_pk_bf16_f32 v214, v198, v199\n"
"v_cvt_pk_bf16_f32 v215, v200, v201\n"
"s_and_b32 s13, s34, 1\n"
"s_lshl_b32 s13, s13, 5\n"
"s_add_u32 s13, s13, s56\n"
"s_lshl_b32 s13, s13, 8\n"
"s_lshr_b32 s43, s34, 1\n"
"s_lshl_b32 s43, s43, 7\n"
"s_add_u32 s13, s13, s43\n"
"s_add_u32 s13, s13, 1572864\n"
"s_mov_b32 s12, 0\n"
"s_mov_b64 exec, 1\n"
"buffer_store_dword v2, off, s[84:87], s13 sc1\n"
"s_mov_b64 exec, -1\n"
"buffer_store_dwordx2 v[214:215], v237, s[52:55], s63 offen  sc1\n"
"s_waitcnt lgkmcnt(2)\n"
"v_pk_add_f32 v[202:203], v[202:203], v[218:219] op_sel:[0,0] op_sel_hi:[1,1]\n"
"s_waitcnt lgkmcnt(0)\n"
"v_pk_add_f32 v[210:211], v[210:211], v[226:227] op_sel:[0,0] op_sel_hi:[1,1]\n"
"v_pk_add_f32 v[204:205], v[204:205], v[220:221] op_sel:[0,0] op_sel_hi:[1,1]\n"
"v_pk_add_f32 v[212:213], v[212:213], v[228:229] op_sel:[0,0] op_sel_hi:[1,1]\n"
"v_pk_add_f32 v[202:203], v[202:203], v[210:211] op_sel:[0,0] op_sel_hi:[1,1]\n"
"v_pk_add_f32 v[204:205], v[204:205], v[212:213] op_sel:[0,0] op_sel_hi:[1,1]\n"
"buffer_store_dwordx4 v[202:205], v236, s[52:55], s68 offen  sc1\n"
"v_lshrrev_b32 v237, 1, v3\n"
"s_mul_i32 s63, s58, 72\n"
"s_mul_i32 s68, s34, 14\n"
"s_add_u32 s63, s63, s68\n"
"s_lshl_b32 s63, s63, 12\n"
"s_add_u32 s63, s63, 5308416\n"
"s_add_u32 s14, s63, s82\n"
"s_add_u32 s15, s63, s83\n"
"ds_read_b64_tr_b16 v[22:23], v28 offset:20480\n"
"ds_read_b64_tr_b16 v[24:25], v28 offset:21504\n"
"ds_read_b64_tr_b16 v[30:31], v29 offset:20480\n"
"ds_read_b64_tr_b16 v[32:33], v29 offset:21504\n"
"ds_read_b64_tr_b16 v[232:233], v28 offset:22528\n"
"ds_read_b64_tr_b16 v[234:235], v28 offset:23552\n"
"ds_read_b64_tr_b16 v[242:243], v29 offset:22528\n"
"ds_read_b64_tr_b16 v[244:245], v29 offset:23552\n"
"ds_read_b64_tr_b16 v[246:247], v28 offset:24576\n"
"ds_read_b64_tr_b16 v[248:249], v28 offset:25600\n"
"s_getreg_b32 s43, hwreg(HW_REG_IB_STS, 0, 4)\n"
"s_cmp_gt_u32 s43, 2\n"
"s_addc_u32 s12, s12, 0\n"
"s_waitcnt lgkmcnt(6)\n"
"v_mfma_f32_16x16x32_bf16 v[198:201], v[22:25], v[38:41], 0\n"
"ds_read_b64_tr_b16 v[250:251], v29 offset:24576\n"
"v_mfma_f32_16x16x32_bf16 v[202:205], v[22:25], v[102:105], 0\n"
"ds_read_b64_tr_b16 v[252:253], v29 offset:25600\n"
"v_mfma_f32_16x16x32_bf16 v[214:217], a[228:231], v[22:25], 0\n"
"s_getreg_b32 s43, hwreg(HW_REG_IB_STS, 0, 4)\n"
"s_cmp_gt_u32 s43, 2\n"
"s_addc_u32 s12, s12, 0\n"
"v_mfma_f32_16x16x32_bf16 v[206:209], v[30:33], v[38:41], 0\n"
"ds_read_b64_tr_b16 v[166:167], v28 offset:26624\n"
"v_mfma_f32_16x16x32_bf16 v[210:213], v[30:33], v[102:105], 0\n"
"ds_read_b64_tr_b16 v[168:169], v28 offset:27648\n"
"v_mfma_f32_16x16x32_bf16 v[218:221], a[228:231], v[30:33], 0\n"
"s_getreg_b32 s43, hwreg(HW_REG_IB_STS, 0, 4)\n"
"s_cmp_gt_u32 s43, 2\n"
"s_addc_u32 s12, s12, 0\n"
"s_waitcnt lgkmcnt(6)\n"
"v_mfma_f32_16x16x32_bf16 v[198:201], v[232:235], v[42:45], v[198:201]\n"
"ds_read_b64_tr_b16 v[22:23], v29 offset:26624\n"
"v_mfma_f32_16x16x32_bf16 v[202:205], v[232:235], v[106:109], v[202:205]\n"
"ds_read_b64_tr_b16 v[24:25], v29 offset:27648\n"
"v_mfma_f32_16x16x32_bf16 v[214:217], a[228:231], v[232:235], v[214:217]\n"
"s_getreg_b32 s43, hwreg(HW_REG_IB_STS, 0, 4)\n"
"s_cmp_gt_u32 s43, 2\n"
"s_addc_u32 s12, s12, 0\n"
"v_mfma_f32_16x16x32_bf16 v[206:209], v[242:245], v[42:45], v[206:209]\n"
"ds_read_b64_tr_b16 v[30:31], v28 offset:28672\n"
"v_mfma_f32_16x16x32_bf16 v[210:213], v[242:245], v[106:109], v[210:213]\n"
"ds_read_b64_tr_b16 v[32:33], v28 offset:29696\n"
"v_mfma_f32_16x16x32_bf16 v[218:221], a[228:231], v[242:245], v[218:221]\n"
"s_getreg_b32 s43, hwreg(HW_REG_IB_STS, 0, 4)\n"
"s_cmp_gt_u32 s43, 2\n"
"s_addc_u32 s12, s12, 0\n"
"s_waitcnt lgkmcnt(6)\n"
"v_mfma_f32_16x16x32_bf16 v[198:201], v[246:249], v[46:49], v[198:201]\n"
"ds_read_b64_tr_b16 v[232:233], v29 offset:28672\n"
"v_mfma_f32_16x16x32_bf16 v[202:205], v[246:249], v[110:113], v[202:205]\n"
"ds_read_b64_tr_b16 v[234:235], v29 offset:29696\n"
"v_mfma_f32_16x16x32_bf16 v[214:217], a[228:231], v[246:249], v[214:217]\n"
"s_getreg_b32 s43, hwreg(HW_REG_IB_STS, 0, 4)\n"
"s_cmp_gt_u32 s43, 2\n"
"s_addc_u32 s12, s12, 0\n"
"v_mfma_f32_16x16x32_bf16 v[206:209], v[250:253], v[46:49], v[206:209]\n"
"ds_read_b64_tr_b16 v[242:243], v28 offset:30720\n"
"v_mfma_f32_16x16x32_bf16 v[210:213], v[250:253], v[110:113], v[210:213]\n"
"ds_read_b64_tr_b16 v[244:245], v28 offset:31744\n"
"v_mfma_f32_16x16x32_bf16 v[218:221], a[228:231], v[250:253], v[218:221]\n"
"s_getreg_b32 s43, hwreg(HW_REG_IB_STS, 0, 4)\n"
"s_cmp_gt_u32 s43, 2\n"
"s_addc_u32 s12, s12, 0\n"
"s_waitcnt lgkmcnt(6)\n"
"v_mfma_f32_16x16x32_bf16 v[198:201], v[166:169], v[50:53], v[198:201]\n"
"ds_read_b64_tr_b16 v[246:247], v29 offset:30720\n"
"v_mfma_f32_16x16x32_bf16 v[202:205], v[166:169], v[114:117], v[202:205]\n"
"ds_read_b64_tr_b16 v[248:249], v29 offset:31744\n"
"v_mfma_f32_16x16x32_bf16 v[214:217], a[228:231], v[166:169], v[214:217]\n"
"s_getreg_b32 s43, hwreg(HW_REG_IB_STS, 0, 4)\n"
"s_cmp_gt_u32 s43, 2\n"
"s_addc_u32 s12, s12, 0\n"
"v_mfma_f32_16x16x32_bf16 v[206:209], v[22:25], v[50:53], v[206:209]\n"
"ds_read_b64_tr_b16 v[250:251], v28 offset:32768\n"
"v_mfma_f32_16x16x32_bf16 v[210:213], v[22:25], v[114:117], v[210:213]\n"
"ds_read_b64_tr_b16 v[252:253], v28 offset:33792\n"
"v_mfma_f32_16x16x32_bf16 v[218:221], a[228:231], v[22:25], v[218:221]\n"
"s_getreg_b32 s43, hwreg(HW_REG_IB_STS, 0, 4)\n"
"s_cmp_gt_u32 s43, 2\n"
"s_addc_u32 s12, s12, 0\n"
"s_waitcnt lgkmcnt(6)\n"
"v_mfma_f32_16x16x32_bf16 v[198:201], v[30:33], v[54:57], v[198:201]\n"
"ds_read_b64_tr_b16 v[166:167], v29 offset:32768\n"
"v_mfma_f32_16x16x32_bf16 v[202:205], v[30:33], v[118:121], v[202:205]\n"
"ds_read_b64_tr_b16 v[168:169], v29 offset:33792\n"
"v_mfma_f32_16x16x32_bf16 v[214:217], a[228:231], v[30:33], v[214:217]\n"
"s_getreg_b32 s43, hwreg(HW_REG_IB_STS, 0, 4)\n"
"s_cmp_gt_u32 s43, 2\n"
"s_addc_u32 s12, s12, 0\n"
"v_mfma_f32_16x16x32_bf16 v[206:209], v[232:235], v[54:57], v[206:209]\n"
"ds_read_b64_tr_b16 v[22:23], v28 offset:34816\n"
"v_mfma_f32_16x16x32_bf16 v[210:213], v[232:235], v[118:121], v[210:213]\n"
"ds_read_b64_tr_b16 v[24:25], v28 offset:35840\n"
"v_mfma_f32_16x16x32_bf16 v[218:221], a[228:231], v[232:235], v[218:221]\n"
"s_getreg_b32 s43, hwreg(HW_REG_IB_STS, 0, 4)\n"
"s_cmp_gt_u32 s43, 2\n"
"s_addc_u32 s12, s12, 0\n"
"s_waitcnt lgkmcnt(6)\n"
"v_mfma_f32_16x16x32_bf16 v[198:201], v[242:245], v[58:61], v[198:201]\n"
"ds_read_b64_tr_b16 v[30:31], v29 offset:34816\n"
"v_mfma_f32_16x16x32_bf16 v[202:205], v[242:245], v[122:125], v[202:205]\n"
"ds_read_b64_tr_b16 v[32:33], v29 offset:35840\n"
"v_mfma_f32_16x16x32_bf16 v[214:217], a[228:231], v[242:245], v[214:217]\n"
"s_getreg_b32 s43, hwreg(HW_REG_IB_STS, 0, 4)\n"
"s_cmp_gt_u32 s43, 2\n"
"s_addc_u32 s12, s12, 0\n"
"v_mfma_f32_16x16x32_bf16 v[206:209], v[246:249], v[58:61], v[206:209]\n"
"ds_read_b64_tr_b16 v[232:233], v28 offset:36864\n"
"v_mfma_f32_16x16x32_bf16 v[210:213], v[246:249], v[122:125], v[210:213]\n"
"ds_read_b64_tr_b16 v[234:235], v28 offset:37888\n"
"v_mfma_f32_16x16x32_bf16 v[218:221], a[228:231], v[246:249], v[218:221]\n"
"s_waitcnt vmcnt(0)\n"
"v_mov_b32 v238, s60\n"
"v_mov_b32 v239, s61\n"
"s_add_u32 s9, s79, 524296\n"
"s_barrier\n"
"s_mov_b64 exec, 1\n"
"buffer_store_dwordx2 v[238:239], off, s[52:55], s9 sc1\n"
"s_mov_b64 exec, -1\n"
"s_andn2_b32 s13, s60, 4095\n"
"s_or_b32 s13, s13, s12\n"
"v_mov_b32 v238, s13\n"
"v_mov_b32 v239, s61\n"
"s_lshl2_add_u32 s13, s56, s34\n"
"s_lshl_b32 s13, s13, 3\n"
"s_add_u32 s13, s13, 1409024\n"
"s_mov_b64 exec, 1\n"
"buffer_store_dwordx2 v[238:239], off, s[84:87], s13 sc1\n"
"s_mov_b64 exec, -1\n"
"v_cmp_ne_u64 vcc, s[60:61], v[240:241]\n"
"s_nop 4\n"
"s_cbranch_vccz L_1_%=\n"
"s_mov_b32 s62, 40000\n"
"buffer_load_dwordx2 v[12:13], v35, s[52:55], s42 offen  sc1\n"
"s_sleep 4\n"
"buffer_load_dwordx2 v[14:15], v35, s[52:55], s42 offen  sc1\n"
"L_2_%=:\n"
"s_waitcnt vmcnt(1)\n"
"v_cmp_ne_u64 vcc, s[60:61], v[12:13]\n"
"s_nop 4\n"
"s_cbranch_vccz L_1_%=\n"
"buffer_load_dwordx2 v[12:13], v35, s[52:55], s42 offen  sc1\n"
"s_waitcnt vmcnt(1)\n"
"v_cmp_ne_u64 vcc, s[60:61], v[14:15]\n"
"s_nop 4\n"
"s_cbranch_vccz L_1_%=\n"
"buffer_load_dwordx2 v[14:15], v35, s[52:55], s42 offen  sc1\n"
"s_sub_u32 s62, s62, 1\n"
"s_cbranch_scc0 L_2_%=\n"
"L_1_%=:\n"
"s_waitcnt lgkmcnt(6)\n"
"v_mfma_f32_16x16x32_bf16 v[198:201], v[250:253], v[62:65], v[198:201]\n"
"ds_read_b64_tr_b16 v[242:243], v29 offset:36864\n"
"v_mfma_f32_16x16x32_bf16 v[202:205], v[250:253], v[126:129], v[202:205]\n"
"ds_read_b64_tr_b16 v[244:245], v29 offset:37888\n"
"v_mfma_f32_16x16x32_bf16 v[214:217], a[228:231], v[250:253], v[214:217]\n"
"s_add_u32 m0, s40, 43008\n"
"s_xor_b32 s10, s93, 384\n"
"s_mul_i32 s10, s10, 1792\n"
"s_add_u32 s10, s10, s89\n"
"s_add_u32 s41, s10, 0\n"
"s_bfm_b64 exec, 56, 0\n"
"buffer_load_dwordx4 v17, s[28:31], s41 offen lds\n"
"s_mov_b64 exec, -1\n"
"v_mfma_f32_16x16x32_bf16 v[206:209], v[166:169], v[62:65], v[206:209]\n"
"ds_read_b64_tr_b16 v[246:247], v28 offset:38912\n"
"v_mfma_f32_16x16x32_bf16 v[210:213], v[166:169], v[126:129], v[210:213]\n"
"ds_read_b64_tr_b16 v[248:249], v28 offset:39936\n"
"v_mfma_f32_16x16x32_bf16 v[218:221], a[228:231], v[166:169], v[218:221]\n"
"s_add_u32 m0, s40, 46592\n"
"s_add_u32 s41, s41, 14336\n"
"s_bfm_b64 exec, 56, 0\n"
"buffer_load_dwordx4 v17, s[28:31], s41 offen lds\n"
"s_mov_b64 exec, -1\n"
"s_waitcnt lgkmcnt(6)\n"
"v_mfma_f32_16x16x32_bf16 v[198:201], v[22:25], v[66:69], v[198:201]\n"
"ds_read_b64_tr_b16 v[250:251], v29 offset:38912\n"
"v_mfma_f32_16x16x32_bf16 v[202:205], v[22:25], v[130:133], v[202:205]\n"
"ds_read_b64_tr_b16 v[252:253], v29 offset:39936\n"
"v_mfma_f32_16x16x32_bf16 v[214:217], a[228:231], v[22:25], v[214:217]\n"
"s_add_u32 m0, s40, 50176\n"
"s_add_u32 s41, s41, 14336\n"
"s_bfm_b64 exec, 56, 0\n"
"buffer_load_dwordx4 v17, s[28:31], s41 offen lds\n"
"s_mov_b64 exec, -1\n"
"v_mfma_f32_16x16x32_bf16 v[206:209], v[30:33], v[66:69], v[206:209]\n"
"ds_read_b64_tr_b16 v[166:167], v28 offset:40960\n"
"v_mfma_f32_16x16x32_bf16 v[210:213], v[30:33], v[130:133], v[210:213]\n"
"ds_read_b64_tr_b16 v[168:169], v28 offset:41984\n"
"v_mfma_f32_16x16x32_bf16 v[218:221], a[228:231], v[30:33], v[218:221]\n"
"s_add_u32 m0, s40, 53760\n"
"s_add_u32 s41, s41, 14336\n"
"s_bfm_b64 exec, 56, 0\n"
"buffer_load_dwordx4 v17, s[28:31], s41 offen lds\n"
"s_mov_b64 exec, -1\n"
"s_waitcnt lgkmcnt(6)\n"
"v_mfma_f32_16x16x32_bf16 v[198:201], v[232:235], v[70:73], v[198:201]\n"
"ds_read_b64_tr_b16 v[22:23], v29 offset:40960\n"
"v_mfma_f32_16x16x32_bf16 v[202:205], v[232:235], v[134:137], v[202:205]\n"
"ds_read_b64_tr_b16 v[24:25], v29 offset:41984\n"
"v_mfma_f32_16x16x32_bf16 v[214:217], a[228:231], v[232:235], v[214:217]\n"
"s_add_u32 m0, s40, 57344\n"
"s_xor_b32 s11, s93, 512\n"
"s_mul_i32 s11, s11, 1792\n"
"s_add_u32 s11, s11, s89\n"
"s_add_u32 s41, s11, 0\n"
"s_bfm_b64 exec, 56, 0\n"
"buffer_load_dwordx4 v17, s[28:31], s41 offen lds\n"
"s_mov_b64 exec, -1\n"
"v_mfma_f32_16x16x32_bf16 v[206:209], v[242:245], v[70:73], v[206:209]\n"
"ds_read_b64_tr_b16 v[30:31], v28 offset:43008\n"
"v_mfma_f32_16x16x32_bf16 v[210:213], v[242:245], v[134:137], v[210:213]\n"
"ds_read_b64_tr_b16 v[32:33], v28 offset:44032\n"
"v_mfma_f32_16x16x32_bf16 v[218:221], a[228:231], v[242:245], v[218:221]\n"
"s_add_u32 m0, s40, 60928\n"
"s_add_u32 s41, s41, 14336\n"
"s_bfm_b64 exec, 56, 0\n"
"buffer_load_dwordx4 v17, s[28:31], s41 offen lds\n"
"s_mov_b64 exec, -1\n"
"s_waitcnt lgkmcnt(6)\n"
"v_mfma_f32_16x16x32_bf16 v[198:201], v[246:249], v[74:77], v[198:201]\n"
"ds_read_b64_tr_b16 v[232:233], v29 offset:43008\n"
"v_mfma_f32_16x16x32_bf16 v[202:205], v[246:249], v[138:141], v[202:205]\n"
"ds_read_b64_tr_b16 v[234:235], v29 offset:44032\n"
"v_mfma_f32_16x16x32_bf16 v[214:217], a[228:231], v[246:249], v[214:217]\n"
"s_add_u32 m0, s40, 64512\n"
"s_add_u32 s41, s41, 14336\n"
"s_bfm_b64 exec, 56, 0\n"
"buffer_load_dwordx4 v17, s[28:31], s41 offen lds\n"
"s_mov_b64 exec, -1\n"
"v_mfma_f32_16x16x32_bf16 v[206:209], v[250:253], v[74:77], v[206:209]\n"
"ds_read_b64_tr_b16 v[242:243], v28 offset:45056\n"
"v_mfma_f32_16x16x32_bf16 v[210:213], v[250:253], v[138:141], v[210:213]\n"
"ds_read_b64_tr_b16 v[244:245], v28 offset:46080\n"
"v_mfma_f32_16x16x32_bf16 v[218:221], a[228:231], v[250:253], v[218:221]\n"
"s_add_u32 m0, s40, 68096\n"
"s_add_u32 s41, s41, 14336\n"
"s_bfm_b64 exec, 56, 0\n"
"buffer_load_dwordx4 v17, s[28:31], s41 offen lds\n"
"s_mov_b64 exec, -1\n"
"s_waitcnt lgkmcnt(6)\n"
"v_mfma_f32_16x16x32_bf16 v[198:201], v[166:169], v[78:81], v[198:201]\n"
"ds_read_b64_tr_b16 v[246:247], v29 offset:45056\n"
"L_pad3_%=:\n"
"s_cbranch_execz L_ret_%=\n"
"v_mfma_f32_16x16x32_bf16 v[202:205], v[166:169], v[142:145], v[202:205]\n"
"ds_read_b64_tr_b16 v[248:249], v29 offset:46080\n"
"v_mfma_f32_16x16x32_bf16 v[214:217], a[228:231], v[166:169], v[214:217]\n"
"s_add_u32 m0, s40, 71680\n"
"s_xor_b32 s8, s93, 640\n"
"s_mul_i32 s8, s8, 1792\n"
"s_add_u32 s8, s8, s89\n"
"s_add_u32 s41, s8, 0\n"
"s_bfm_b64 exec, 56, 0\n"
"buffer_load_dwordx4 v17, s[28:31], s41 offen lds\n"
"s_mov_b64 exec, -1\n"
"v_mfma_f32_16x16x32_bf16 v[206:209], v[22:25], v[78:81], v[206:209]\n"
"ds_read_b64_tr_b16 v[250:251], v28 offset:47104\n"
"v_mfma_f32_16x16x32_bf16 v[210:213], v[22:25], v[142:145], v[210:213]\n"
"ds_read_b64_tr_b16 v[252:253], v28 offset:48128\n"
"v_mfma_f32_16x16x32_bf16 v[218:221], a[228:231], v[22:25], v[218:221]\n"
"s_add_u32 m0, s40, 75264\n"
"s_add_u32 s41, s41, 14336\n"
"s_bfm_b64 exec, 56, 0\n"
"buffer_load_dwordx4 v17, s[28:31], s41 offen lds\n"
"s_mov_b64 exec, -1\n"
"s_waitcnt lgkmcnt(6)\n"
"v_mfma_f32_16x16x32_bf16 v[198:201], v[30:33], v[82:85], v[198:201]\n"
"ds_read_b64_tr_b16 v[166:167], v29 offset:47104\n"
"v_mfma_f32_16x16x32_bf16 v[202:205], v[30:33], v[146:149], v[202:205]\n"
"ds_read_b64_tr_b16 v[168:169], v29 offset:48128\n"
"v_mfma_f32_16x16x32_bf16 v[214:217], a[228:231], v[30:33], v[214:217]\n"
"buffer_load_dwordx4 a[0:3], v3, s[52:55], s14 offen  sc1\n"
"v_mfma_f32_16x16x32_bf16 v[206:209], v[232:235], v[82:85], v[206:209]\n"
"ds_read_b64_tr_b16 v[22:23], v28 offset:49152\n"
"v_mfma_f32_16x16x32_bf16 v[210:213], v[232:235], v[146:149], v[210:213]\n"
"ds_read_b64_tr_b16 v[24:25], v28 offset:50176\n"
"v_mfma_f32_16x16x32_bf16 v[218:221], a[228:231], v[232:235], v[218:221]\n"
"buffer_load_dwordx4 a[4:7], v3, s[52:55], s14 offen offset:1024 sc1\n"
"s_waitcnt lgkmcnt(6)\n"
"v_mfma_f32_16x16x32_bf16 v[198:201], v[242:245], v[86:89], v[198:201]\n"
"ds_read_b64_tr_b16 v[30:31], v29 offset:49152\n"
"v_mfma_f32_16x16x32_bf16 v[202:205], v[242:245], v[150:153], v[202:205]\n"
"ds_read_b64_tr_b16 v[32:33], v29 offset:50176\n"
"v_mfma_f32_16x16x32_bf16 v[214:217], a[228:231], v[242:245], v[214:217]\n"
"buffer_load_dwordx4 a[8:11], v3, s[52:55], s15 offen  sc1\n"
"v_mfma_f32_16x16x32_bf16 v[206:209], v[246:249], v[86:89], v[206:209]\n"
"ds_read_b64_tr_b16 v[232:233], v28 offset:51200\n"
"v_mfma_f32_16x16x32_bf16 v[210:213], v[246:249], v[150:153], v[210:213]\n"
"ds_read_b64_tr_b16 v[234:235], v28 offset:52224\n"
"v_mfma_f32_16x16x32_bf16 v[218:221], a[228:231], v[246:249], v[218:221]\n"
"buffer_load_dwordx4 a[12:15], v3, s[52:55], s15 offen offset:1024 sc1\n"
"s_waitcnt lgkmcnt(6)\n"
"v_mfma_f32_16x16x32_bf16 v[198:201], v[250:253], v[90:93], v[198:201]\n"
"ds_read_b64_tr_b16 v[242:243], v29 offset:51200\n"
"v_mfma_f32_16x16x32_bf16 v[202:205], v[250:253], v[154:157], v[202:205]\n"
"ds_read_b64_tr_b16 v[244:245], v29 offset:52224\n"
"v_mfma_f32_16x16x32_bf16 v[214:217], a[228:231], v[250:253], v[214:217]\n"
"s_add_u32 s9, s14, 4096\n"
"buffer_load_dwordx4 a[16:19], v3, s[52:55], s9 offen  sc1\n"
"v_mfma_f32_16x16x32_bf16 v[206:209], v[166:169], v[90:93], v[206:209]\n"
"v_mfma_f32_16x16x32_bf16 v[210:213], v[166:169], v[154:157], v[210:213]\n"
"v_mfma_f32_16x16x32_bf16 v[218:221], a[228:231], v[166:169], v[218:221]\n"
"s_add_u32 s10, s14, 4096\n"
"buffer_load_dwordx4 a[20:23], v3, s[52:55], s10 offen offset:1024 sc1\n"
"s_waitcnt lgkmcnt(4)\n"
"v_mfma_f32_16x16x32_bf16 v[198:201], v[22:25], v[94:97], v[198:201]\n"
"v_mfma_f32_16x16x32_bf16 v[202:205], v[22:25], v[158:161], v[202:205]\n"
"v_mfma_f32_16x16x32_bf16 v[214:217], a[228:231], v[22:25], v[214:217]\n"
"s_add_u32 s11, s15, 4096\n"
"buffer_load_dwordx4 a[24:27], v3, s[52:55], s11 offen  sc1\n"
"v_mfma_f32_16x16x32_bf16 v[206:209], v[30:33], v[94:97], v[206:209]\n"
"v_mfma_f32_16x16x32_bf16 v[210:213], v[30:33], v[158:161], v[210:213]\n"
"v_mfma_f32_16x16x32_bf16 v[218:221], a[228:231], v[30:33], v[218:221]\n"
"s_add_u32 s8, s15, 4096\n"
"buffer_load_dwordx4 a[28:31], v3, s[52:55], s8 offen offset:1024 sc1\n"
"s_waitcnt lgkmcnt(0)\n"
"v_mfma_f32_16x16x32_bf16 v[198:201], v[232:235], v[98:101], v[198:201]\n"
"v_mfma_f32_16x16x32_bf16 v[202:205], v[232:235], v[162:165], v[202:205]\n"
"v_mfma_f32_16x16x32_bf16 v[214:217], a[228:231], v[232:235], v[214:217]\n"
"s_add_u32 s9, s14, 8192\n"
"buffer_load_dwordx4 a[32:35], v3, s[52:55], s9 offen  sc1\n"
"v_mfma_f32_16x16x32_bf16 v[206:209], v[242:245], v[98:101], v[206:209]\n"
"v_mfma_f32_16x16x32_bf16 v[210:213], v[242:245], v[162:165], v[210:213]\n"
"v_mfma_f32_16x16x32_bf16 v[218:221], a[228:231], v[242:245], v[218:221]\n"
"s_add_u32 s10, s14, 8192\n"
"buffer_load_dwordx4 a[36:39], v3, s[52:55], s10 offen offset:1024 sc1\n"
"s_nop 0\n"
"v_and_b32 v17, 0xffff0000, v214\n"
"v_sub_f32 v236, v214, v17\n"
"v_cvt_pk_bf16_f32 v236, v236, v236\n"
"v_perm_b32 v214, v236, v214, s92\n"
"v_mov_b32 v215, v214\n"
"ds_write_b32 v36, v214 offset:256\n"
"v_and_b32 v17, 0xffff0000, v218\n"
"v_sub_f32 v236, v218, v17\n"
"v_cvt_pk_bf16_f32 v236, v236, v236\n"
"v_perm_b32 v218, v236, v218, s92\n"
"v_mov_b32 v219, v218\n"
"ds_write_b32 v36, v218 offset:320\n"
"v_mfma_f32_16x16x16_bf16 v[198:201], v[214:215], v[20:21], v[198:201]\n"
"v_mfma_f32_16x16x16_bf16 v[202:205], v[214:215], v[230:231], v[202:205]\n"
"v_mfma_f32_16x16x16_bf16 v[206:209], v[218:219], v[20:21], v[206:209]\n"
"v_mfma_f32_16x16x16_bf16 v[210:213], v[218:219], v[230:231], v[210:213]\n"
"v_lshrrev_b32 v232, 7, v3\n"
"v_subrev_u32 v232, s59, v232\n"
"v_min_u32 v232, 6, v232\n"
"v_lshlrev_b32 v232, 7, v232\n"
"v_and_b32 v233, 7, v2\n"
"v_lshl_add_u32 v232, v233, 3, v232\n"
"v_add_u32 v232, 160320, v232\n"
"s_mov_b32 s63, 0x01000100\n"
"v_perm_b32 v234, v18, v18, s63\n"
"s_mov_b32 s68, 0x03020302\n"
"v_perm_b32 v235, v18, v18, s68\n"
"s_bfm_b64 exec, 56, 0\n"
"ds_write_b64 v232, v[234:235]\n"
"s_bfm_b64 exec, 32, 0\n"
"v_lshlrev_b32 v22, 16, v19\n"
"v_and_b32 v23, 0xffff0000, v19\n"
"v_lshrrev_b32 v233, 1, v3\n"
"v_add_u32 v233, 161280, v233\n"
"ds_write_b64 v233, v[22:23]\n"
"s_mov_b64 exec, -1\n"
"s_mul_i32 s63, s58, 72\n"
"s_lshl2_add_u32 s63, s34, s63\n"
"s_lshl_b32 s63, s63, 12\n"
"s_add_u32 s63, s63, 5242880\n"
"s_add_u32 s12, s63, s82\n"
"s_add_u32 s13, s63, s83\n"
"s_lshl2_add_u32 s94, s56, s34\n"
"s_lshl_b32 s95, s94, 10\n"
"s_add_u32 s95, s95, 65536\n"
"s_lshl_b32 s94, s94, 13\n"
"s_add_u32 s94, s94, 1048576\n"
"v_lshlrev_b32 v17, 6, v2\n"
"v_lshl_add_u32 v17, v1, 4, v17\n"
"s_lshr_b32 s63, s34, 1\n"
"s_lshl_b32 s63, s63, 10\n"
"s_and_b32 s68, s34, 1\n"
"s_lshl3_add_u32 s63, s68, s63\n"
"s_add_u32 s63, s63, 161792\n"
"v_add_u32 v36, s63, v3\n"
"s_lshl_b32 s63, s34, 5\n"
"s_and_b32 s63, s63, 64\n"
"s_lshl3_add_u32 s63, s68, s63\n"
"s_add_u32 s63, s63, 160128\n"
"v_lshl_add_u32 v236, v1, 4, s63\n"
"v_and_b32 v214, 1, v2\n"
"v_lshlrev_b32 v214, 4, v214\n"
"v_mov_b32 v215, 0x3f80\n"
"v_lshlrev_b32 v214, v214, v215\n"
"v_and_b32 v215, 1, v1\n"
"v_lshlrev_b32 v215, 2, v215\n"
"v_lshrrev_b32 v218, 1, v2\n"
"v_sub_u32 v215, v218, v215\n"
"v_cmp_eq_u32 vcc, 0, v215\n"
"s_nop 1\n"
"v_cndmask_b32 v218, 0, v214, vcc\n"
"v_cmp_eq_u32 vcc, 1, v215\n"
"s_nop 1\n"
"v_cndmask_b32 v219, 0, v214, vcc\n"
"v_cmp_eq_u32 vcc, 2, v215\n"
"s_nop 1\n"
"v_cndmask_b32 v220, 0, v214, vcc\n"
"v_cmp_eq_u32 vcc, 3, v215\n"
"s_nop 1\n"
"v_cndmask_b32 v221, 0, v214, vcc\n"
"s_add_u32 s96, s94, 4096\n"
"v_lshlrev_b32 v34, 7, v2\n"
"s_add_u32 s97, s77, 524296\n"
"v_cvt_pk_bf16_f32 v226, v198, v199\n"
"v_cvt_pk_bf16_f32 v227, v200, v201\n"
"v_cvt_pk_bf16_f32 v230, v206, v207\n"
"v_cvt_pk_bf16_f32 v231, v208, v209\n"
"v_cvt_pk_bf16_f32 v102, v202, v203\n"
"v_cvt_pk_bf16_f32 v103, v204, v205\n"
"v_cvt_pk_bf16_f32 v106, v210, v211\n"
"v_cvt_pk_bf16_f32 v107, v212, v213\n"
"v_mfma_f32_16x16x16_bf16 v[198:201], a[224:225], v[226:227], 0\n"
"v_mfma_f32_16x16x16_bf16 v[206:209], a[226:227], v[230:231], 0\n"
"v_mfma_f32_16x16x16_bf16 v[202:205], a[224:225], v[102:103], 0\n"
"v_mfma_f32_16x16x16_bf16 v[210:213], a[226:227], v[106:107], 0\n"
"buffer_load_dwordx2 v[222:223], v34, s[52:55], s97 offen  sc1\n"
"s_nop 3\n"
"v_max3_f32 v35, v198, v199, v200\n"
"v_max3_f32 v234, -v198, -v199, -v200\n"
"v_max3_f32 v235, v206, v207, v208\n"
"v_max3_f32 v126, -v206, -v207, -v208\n"
"buffer_load_dwordx4 v[38:41], v3, s[52:55], s12 offen  sc1\n"
"v_max3_f32 v127, v202, v203, v204\n"
"v_max3_f32 v128, -v202, -v203, -v204\n"
"v_max3_f32 v129, v210, v211, v212\n"
"v_max3_f32 v130, -v210, -v211, -v212\n"
"v_max_f32 v35, v35, v201\n"
"v_max_f32 v234, v234, -v201\n"
"v_max_f32 v235, v235, v209\n"
"v_max_f32 v126, v126, -v209\n"
"v_max_f32 v127, v127, v205\n"
"v_max_f32 v128, v128, -v205\n"
"buffer_load_dwordx4 v[42:45], v3, s[52:55], s12 offen offset:1024 sc1\n"
"v_max_f32 v129, v129, v213\n"
"v_max_f32 v130, v130, -v213\n"
"v_permlane32_swap_b32 v35, v234\n"
"v_permlane32_swap_b32 v127, v128\n"
"v_permlane32_swap_b32 v235, v126\n"
"v_permlane32_swap_b32 v129, v130\n"
"v_max_f32 v35, v35, v234\n"
"v_max_f32 v127, v127, v128\n"
"v_max_f32 v235, v235, v126\n"
"v_max_f32 v129, v129, v130\n"
"buffer_load_dwordx4 v[46:49], v3, s[52:55], s13 offen  sc1\n"
"v_permlane16_swap_b32 v35, v235\n"
"v_permlane16_swap_b32 v127, v129\n"
"v_max_f32 v35, v35, v235\n"
"v_max_f32 v127, v127, v129\n"
"v_mul_f32 v35, -v35, v16\n"
"v_mul_f32 v127, -v127, v37\n"
"v_mov_b32 v235, v35\n"
"v_mov_b32 v129, v127\n"
"s_nop 0\n"
"v_permlane16_swap_b32 v35, v235\n"
"v_permlane16_swap_b32 v127, v129\n"
"buffer_load_dwordx4 v[50:53], v3, s[52:55], s13 offen offset:1024 sc1\n"
"v_mov_b32 v234, v35\n"
"v_mov_b32 v128, v127\n"
"v_mov_b32 v126, v235\n"
"v_mov_b32 v130, v129\n"
"v_permlane32_swap_b32 v35, v234\n"
"v_permlane32_swap_b32 v127, v128\n"
"v_permlane32_swap_b32 v235, v126\n"
"v_permlane32_swap_b32 v129, v130\n"
"v_pk_fma_f32 v[110:111], v[198:199], v[16:17], v[234:235] op_sel:[0,0,0] op_sel_hi:[1,0,0] neg_lo:[1,0,0] neg_hi:[1,0,0]\n"
"v_pk_fma_f32 v[198:199], v[198:199], v[16:17], v[34:35] op_sel:[0,0,1] op_sel_hi:[1,0,1]\n"
"s_add_u32 s11, s12, 4096\n"
"buffer_load_dwordx4 v[54:57], v3, s[52:55], s11 offen  sc1\n"
"v_pk_fma_f32 v[112:113], v[200:201], v[16:17], v[234:235] op_sel:[0,0,0] op_sel_hi:[1,0,0] neg_lo:[1,0,0] neg_hi:[1,0,0]\n"
"v_pk_fma_f32 v[200:201], v[200:201], v[16:17], v[34:35] op_sel:[0,0,1] op_sel_hi:[1,0,1]\n"
"v_pk_fma_f32 v[114:115], v[206:207], v[16:17], v[126:127] op_sel:[0,0,0] op_sel_hi:[1,0,0] neg_lo:[1,0,0] neg_hi:[1,0,0]\n"
"v_pk_fma_f32 v[206:207], v[206:207], v[16:17], v[234:235] op_sel:[0,0,1] op_sel_hi:[1,0,1]\n"
"v_pk_fma_f32 v[116:117], v[208:209], v[16:17], v[126:127] op_sel:[0,0,0] op_sel_hi:[1,0,0] neg_lo:[1,0,0] neg_hi:[1,0,0]\n"
"v_pk_fma_f32 v[208:209], v[208:209], v[16:17], v[234:235] op_sel:[0,0,1] op_sel_hi:[1,0,1]\n"
"v_pk_fma_f32 v[118:119], v[202:203], v[36:37], v[128:129] op_sel:[0,1,0] op_sel_hi:[1,1,0] neg_lo:[1,0,0] neg_hi:[1,0,0]\n"
"v_pk_fma_f32 v[202:203], v[202:203], v[36:37], v[126:127] op_sel:[0,1,1] op_sel_hi:[1,1,1]\n"
"v_pk_fma_f32 v[120:121], v[204:205], v[36:37], v[128:129] op_sel:[0,1,0] op_sel_hi:[1,1,0] neg_lo:[1,0,0] neg_hi:[1,0,0]\n"
"v_pk_fma_f32 v[204:205], v[204:205], v[36:37], v[126:127] op_sel:[0,1,1] op_sel_hi:[1,1,1]\n"
"s_add_u32 s8, s12, 4096\n"
"buffer_load_dwordx4 v[58:61], v3, s[52:55], s8 offen offset:1024 sc1\n"
"v_pk_fma_f32 v[122:123], v[210:211], v[36:37], v[130:131] op_sel:[0,1,0] op_sel_hi:[1,1,0] neg_lo:[1,0,0] neg_hi:[1,0,0]\n"
"v_pk_fma_f32 v[210:211], v[210:211], v[36:37], v[128:129] op_sel:[0,1,1] op_sel_hi:[1,1,1]\n"
"v_pk_fma_f32 v[124:125], v[212:213], v[36:37], v[130:131] op_sel:[0,1,0] op_sel_hi:[1,1,0] neg_lo:[1,0,0] neg_hi:[1,0,0]\n"
"v_pk_fma_f32 v[212:213], v[212:213], v[36:37], v[128:129] op_sel:[0,1,1] op_sel_hi:[1,1,1]\n"
"v_exp_f32 v110, v110\n"
"v_exp_f32 v198, v198\n"
"v_exp_f32 v111, v111\n"
"v_exp_f32 v199, v199\n"
"v_exp_f32 v112, v112\n"
"v_exp_f32 v200, v200\n"
"s_add_u32 s9, s13, 4096\n"
"buffer_load_dwordx4 v[62:65], v3, s[52:55], s9 offen  sc1\n"
"v_exp_f32 v113, v113\n"
"v_exp_f32 v201, v201\n"
"v_exp_f32 v114, v114\n"
"v_exp_f32 v206, v206\n"
"v_exp_f32 v115, v115\n"
"v_exp_f32 v207, v207\n"
"v_exp_f32 v116, v116\n"
"v_exp_f32 v208, v208\n"
"v_exp_f32 v117, v117\n"
"v_exp_f32 v209, v209\n"
"s_waitcnt vmcnt(7)\n"
"v_cmp_ne_u64 vcc, s[60:61], v[222:223]\n"
"s_nop 4\n"
"s_cbranch_vccz L_3_%=\n"
"s_mov_b32 s62, 40000\n"
"buffer_load_dwordx2 v[12:13], v34, s[52:55], s97 offen  sc1\n"
"s_sleep 4\n"
"buffer_load_dwordx2 v[14:15], v34, s[52:55], s97 offen  sc1\n"
"L_4_%=:\n"
"s_waitcnt vmcnt(1)\n"
"v_cmp_ne_u64 vcc, s[60:61], v[12:13]\n"
"s_nop 4\n"
"s_cbranch_vccz L_3_%=\n"
"buffer_load_dwordx2 v[12:13], v34, s[52:55], s97 offen  sc1\n"
"s_waitcnt vmcnt(1)\n"
"v_cmp_ne_u64 vcc, s[60:61], v[14:15]\n"
"s_nop 4\n"
"s_cbranch_vccz L_3_%=\n"
"buffer_load_dwordx2 v[14:15], v34, s[52:55], s97 offen  sc1\n"
"s_sub_u32 s62, s62, 1\n"
"s_cbranch_scc0 L_4_%=\n"
"L_3_%=:\n"
"buffer_load_dwordx4 v[214:217], v17, s[52:55], s95 offen  sc1\n"
"v_exp_f32 v118, v118\n"
"v_exp_f32 v202, v202\n"
"v_exp_f32 v119, v119\n"
"v_exp_f32 v203, v203\n"
"v_exp_f32 v120, v120\n"
"buffer_load_dwordx4 v[238:241], v3, s[52:55], s94 offen  sc1\n"
"v_exp_f32 v204, v204\n"
"v_exp_f32 v121, v121\n"
"v_exp_f32 v205, v205\n"
"v_exp_f32 v122, v122\n"
"buffer_load_dwordx4 v[242:245], v3, s[52:55], s94 offen offset:1024 sc1\n"
"v_exp_f32 v210, v210\n"
"v_exp_f32 v123, v123\n"
"v_exp_f32 v211, v211\n"
"v_exp_f32 v124, v124\n"
"buffer_load_dwordx4 v[246:249], v3, s[52:55], s94 offen offset:2048 sc1\n"
"v_exp_f32 v212, v212\n"
"v_exp_f32 v125, v125\n"
"v_exp_f32 v213, v213\n"
"v_pk_add_f32 v[226:227], v[198:199], v[200:201] op_sel:[0,0] op_sel_hi:[1,1]\n"
"v_pk_add_f32 v[228:229], v[110:111], v[112:113] op_sel:[0,0] op_sel_hi:[1,1]\n"
"buffer_load_dwordx4 v[250:253], v3, s[52:55], s94 offen offset:3072 sc1\n"
"v_pk_add_f32 v[230:231], v[206:207], v[208:209] op_sel:[0,0] op_sel_hi:[1,1]\n"
"v_pk_add_f32 v[232:233], v[114:115], v[116:117] op_sel:[0,0] op_sel_hi:[1,1]\n"
"v_pk_add_f32 v[102:103], v[202:203], v[204:205] op_sel:[0,0] op_sel_hi:[1,1]\n"
"v_pk_add_f32 v[104:105], v[118:119], v[120:121] op_sel:[0,0] op_sel_hi:[1,1]\n"
"buffer_load_dwordx4 v[18:21], v3, s[52:55], s96 offen  sc1\n"
"v_pk_add_f32 v[106:107], v[210:211], v[212:213] op_sel:[0,0] op_sel_hi:[1,1]\n"
"v_pk_add_f32 v[108:109], v[122:123], v[124:125] op_sel:[0,0] op_sel_hi:[1,1]\n"
"v_add_f32 v35, v226, v227\n"
"v_add_f32 v234, v228, v229\n"
"buffer_load_dwordx4 v[22:25], v3, s[52:55], s96 offen offset:1024 sc1\n"
"v_add_f32 v235, v230, v231\n"
"v_add_f32 v126, v232, v233\n"
"v_add_f32 v127, v102, v103\n"
"v_add_f32 v128, v104, v105\n"
"v_add_f32 v129, v106, v107\n"
"buffer_load_dwordx4 v[26:29], v3, s[52:55], s96 offen offset:2048 sc1\n"
"v_add_f32 v130, v108, v109\n"
"v_permlane32_swap_b32 v35, v234\n"
"v_permlane32_swap_b32 v127, v128\n"
"v_permlane32_swap_b32 v235, v126\n"
"buffer_load_dwordx4 v[30:33], v3, s[52:55], s96 offen offset:3072 sc1\n"
"v_permlane32_swap_b32 v129, v130\n"
"v_add_f32 v35, v35, v234\n"
"v_add_f32 v127, v127, v128\n"
"v_add_f32 v235, v235, v126\n"
"s_mov_b32 s97, 1409024\n"
"buffer_load_dwordx2 v[224:225], v237, s[84:87], s97 offen  sc1\n"
"v_add_f32 v129, v129, v130\n"
"v_permlane16_swap_b32 v35, v235\n"
"s_nop 0\n"
"v_permlane16_swap_b32 v127, v129\n"
"v_add_f32 v35, v35, v235\n"
"v_add_f32 v127, v127, v129\n"
"s_add_u32 s10, s13, 4096\n"
"buffer_load_dwordx4 v[66:69], v3, s[52:55], s10 offen offset:1024 sc1\n"
"v_rcp_f32 v35, v35\n"
"v_rcp_f32 v127, v127\n"
"v_mov_b32 v235, v35\n"
"v_mov_b32 v129, v127\n"
"s_add_u32 s11, s12, 8192\n"
"buffer_load_dwordx4 v[70:73], v3, s[52:55], s11 offen  sc1\n"
"v_permlane16_swap_b32 v35, v235\n"
"v_permlane16_swap_b32 v127, v129\n"
"v_mov_b32 v234, v35\n"
"v_mov_b32 v128, v127\n"
"s_add_u32 s8, s12, 8192\n"
"buffer_load_dwordx4 v[74:77], v3, s[52:55], s8 offen offset:1024 sc1\n"
"v_mov_b32 v126, v235\n"
"v_mov_b32 v130, v129\n"
"v_permlane32_swap_b32 v35, v234\n"
"v_permlane32_swap_b32 v127, v128\n"
"s_add_u32 s9, s13, 8192\n"
"buffer_load_dwordx4 v[78:81], v3, s[52:55], s9 offen  sc1\n"
"v_permlane32_swap_b32 v235, v126\n"
"v_permlane32_swap_b32 v129, v130\n"
"v_pk_mul_f32 v[198:199], v[198:199], v[34:35] op_sel:[0,1] op_sel_hi:[1,1]\n"
"v_pk_mul_f32 v[110:111], v[110:111], v[234:235] op_sel:[0,0] op_sel_hi:[1,0]\n"
"v_pk_mul_f32 v[200:201], v[200:201], v[34:35] op_sel:[0,1] op_sel_hi:[1,1]\n"
"s_add_u32 s10, s13, 8192\n"
"buffer_load_dwordx4 v[82:85], v3, s[52:55], s10 offen offset:1024 sc1\n"
"v_pk_mul_f32 v[112:113], v[112:113], v[234:235] op_sel:[0,0] op_sel_hi:[1,0]\n"
"v_pk_mul_f32 v[206:207], v[206:207], v[234:235] op_sel:[0,1] op_sel_hi:[1,1]\n"
"v_pk_mul_f32 v[114:115], v[114:115], v[126:127] op_sel:[0,0] op_sel_hi:[1,0]\n"
"v_pk_mul_f32 v[208:209], v[208:209], v[234:235] op_sel:[0,1] op_sel_hi:[1,1]\n"
"s_add_u32 s11, s12, 12288\n"
"buffer_load_dwordx4 v[86:89], v3, s[52:55], s11 offen  sc1\n"
"v_pk_mul_f32 v[116:117], v[116:117], v[126:127] op_sel:[0,0] op_sel_hi:[1,0]\n"
"v_pk_mul_f32 v[202:203], v[202:203], v[126:127] op_sel:[0,1] op_sel_hi:[1,1]\n"
"v_pk_mul_f32 v[118:119], v[118:119], v[128:129] op_sel:[0,0] op_sel_hi:[1,0]\n"
"v_pk_mul_f32 v[204:205], v[204:205], v[126:127] op_sel:[0,1] op_sel_hi:[1,1]\n"
"s_add_u32 s8, s12, 12288\n"
"buffer_load_dwordx4 v[90:93], v3, s[52:55], s8 offen offset:1024 sc1\n"
"v_pk_mul_f32 v[120:121], v[120:121], v[128:129] op_sel:[0,0] op_sel_hi:[1,0]\n"
"v_pk_mul_f32 v[210:211], v[210:211], v[128:129] op_sel:[0,1] op_sel_hi:[1,1]\n"
"v_pk_mul_f32 v[122:123], v[122:123], v[130:131] op_sel:[0,0] op_sel_hi:[1,0]\n"
"v_pk_mul_f32 v[212:213], v[212:213], v[128:129] op_sel:[0,1] op_sel_hi:[1,1]\n"
"v_pk_mul_f32 v[124:125], v[124:125], v[130:131] op_sel:[0,0] op_sel_hi:[1,0]\n"
"s_add_u32 s9, s13, 12288\n"
"buffer_load_dwordx4 v[94:97], v3, s[52:55], s9 offen  sc1\n"
"v_cvt_pk_bf16_f32 v226, v198, v199\n"
"v_cvt_pk_bf16_f32 v227, v200, v201\n"
"v_cvt_pk_bf16_f32 v228, v110, v111\n"
"v_cvt_pk_bf16_f32 v229, v112, v113\n"
"s_add_u32 s10, s13, 12288\n"
"buffer_load_dwordx4 v[98:101], v3, s[52:55], s10 offen offset:1024 sc1\n"
"v_cvt_pk_bf16_f32 v230, v206, v207\n"
"v_cvt_pk_bf16_f32 v231, v208, v209\n"
"v_cvt_pk_bf16_f32 v232, v114, v115\n"
"v_cvt_pk_bf16_f32 v233, v116, v117\n"
"v_cvt_pk_bf16_f32 v102, v202, v203\n"
"v_cvt_pk_bf16_f32 v103, v204, v205\n"
"v_cvt_pk_bf16_f32 v104, v118, v119\n"
"v_cvt_pk_bf16_f32 v105, v120, v121\n"
"v_cvt_pk_bf16_f32 v106, v210, v211\n"
"v_cvt_pk_bf16_f32 v107, v212, v213\n"
"v_cvt_pk_bf16_f32 v108, v122, v123\n"
"v_cvt_pk_bf16_f32 v109, v124, v125\n"
"s_waitcnt vmcnt(18)\n"
"v_add_f32_dpp v214, v214, v214 row_ror:8 row_mask:0xf bank_mask:0xf\n"
"v_add_f32_dpp v215, v215, v215 row_ror:8 row_mask:0xf bank_mask:0xf\n"
"v_add_f32_dpp v216, v216, v216 row_ror:8 row_mask:0xf bank_mask:0xf\n"
"v_add_f32_dpp v217, v217, v217 row_ror:8 row_mask:0xf bank_mask:0xf\n"
"v_add_f32_dpp v214, v214, v214 row_ror:4 row_mask:0xf bank_mask:0xf\n"
"v_add_f32_dpp v215, v215, v215 row_ror:4 row_mask:0xf bank_mask:0xf\n"
"v_add_f32_dpp v216, v216, v216 row_ror:4 row_mask:0xf bank_mask:0xf\n"
"v_add_f32_dpp v217, v217, v217 row_ror:4 row_mask:0xf bank_mask:0xf\n"
"v_add_f32_dpp v214, v214, v214 row_ror:2 row_mask:0xf bank_mask:0xf\n"
"v_add_f32_dpp v215, v215, v215 row_ror:2 row_mask:0xf bank_mask:0xf\n"
"v_add_f32_dpp v216, v216, v216 row_ror:2 row_mask:0xf bank_mask:0xf\n"
"v_add_f32_dpp v217, v217, v217 row_ror:2 row_mask:0xf bank_mask:0xf\n"
"v_add_f32_dpp v214, v214, v214 row_ror:1 row_mask:0xf bank_mask:0xf\n"
"v_add_f32_dpp v215, v215, v215 row_ror:1 row_mask:0xf bank_mask:0xf\n"
"v_add_f32_dpp v216, v216, v216 row_ror:1 row_mask:0xf bank_mask:0xf\n"
"v_add_f32_dpp v217, v217, v217 row_ror:1 row_mask:0xf bank_mask:0xf\n"
"v_cvt_pk_bf16_f32 v214, v214, v215\n"
"v_cvt_pk_bf16_f32 v215, v216, v217\n"
"ds_write_b64 v236, v[214:215]\n"
"v_add_u32 v17, 161792, v3\n"
"v_lshlrev_b32 v236, 4, v1\n"
"v_add_u32 v236, 160128, v236\n"
"s_lshr_b32 s68, s56, 1\n"
"s_lshl3_add_u32 s63, s34, s68\n"
"s_mul_i32 s68, s59, 3\n"
"s_lshl4_add_u32 s63, s63, s68\n"
"s_lshl_b32 s63, s63, 10\n"
"s_add_u32 s68, s63, 524288\n"
"s_waitcnt vmcnt(17)\n"
"v_mfma_f32_16x16x32_bf16 v[110:113], v[218:221], v[238:241], 0\n"
"s_waitcnt vmcnt(16)\n"
"v_mfma_f32_16x16x32_bf16 v[110:113], v[218:221], v[242:245], v[110:113]\n"
"s_waitcnt vmcnt(15)\n"
"v_mfma_f32_16x16x32_bf16 v[110:113], v[218:221], v[246:249], v[110:113]\n"
"s_waitcnt vmcnt(14)\n"
"v_mfma_f32_16x16x32_bf16 v[110:113], v[218:221], v[250:253], v[110:113]\n"
"s_waitcnt vmcnt(13)\n"
"v_mfma_f32_16x16x32_bf16 v[110:113], v[218:221], v[18:21], v[110:113]\n"
"s_waitcnt vmcnt(12)\n"
"v_mfma_f32_16x16x32_bf16 v[110:113], v[218:221], v[22:25], v[110:113]\n"
"s_waitcnt vmcnt(11)\n"
"v_mfma_f32_16x16x32_bf16 v[110:113], v[218:221], v[26:29], v[110:113]\n"
"s_waitcnt vmcnt(10)\n"
"v_mfma_f32_16x16x32_bf16 v[110:113], v[218:221], v[30:33], v[110:113]\n"
"s_andn2_b32 s98, s60, 4095\n"
"s_mov_b32 s99, s61\n"
"s_waitcnt vmcnt(9)\n"
"v_and_b32 v216, 0xfffff000, v224\n"
"v_mov_b32 v217, v225\n"
"v_cmp_ne_u64 vcc, s[98:99], v[216:217]\n"
"s_nop 4\n"
"s_cbranch_vccz L_5_%=\n"
"s_mov_b32 s62, 80000\n"
"L_6_%=:\n"
"buffer_load_dwordx2 v[224:225], v237, s[84:87], s97 offen  sc1\n"
"s_waitcnt vmcnt(0)\n"
"v_and_b32 v216, 0xfffff000, v224\n"
"v_mov_b32 v217, v225\n"
"v_cmp_ne_u64 vcc, s[98:99], v[216:217]\n"
"s_nop 4\n"
"s_cbranch_vccz L_5_%=\n"
"s_sub_u32 s62, s62, 1\n"
"s_cbranch_scc0 L_6_%=\n"
"L_5_%=:\n"
"v_min_u32_dpp v224, v224, v224 row_ror:2 row_mask:0xf bank_mask:0xf\n"
"s_nop 1\n"
"v_min_u32_dpp v224, v224, v224 row_ror:4 row_mask:0xf bank_mask:0xf\n"
"s_nop 1\n"
"v_min_u32_dpp v224, v224, v224 row_ror:8 row_mask:0xf bank_mask:0xf\n"
"v_mov_b32 v216, v224\n"
"s_nop 1\n"
"v_permlane16_swap_b32 v224, v216\n"
"v_min_u32 v224, v224, v216\n"
"v_mov_b32 v216, v224\n"
"s_nop 1\n"
"v_permlane32_swap_b32 v224, v216\n"
"v_min_u32 v224, v224, v216\n"
"s_nop 0\n"
"v_readlane_b32 s98, v224, 0\n"
"v_readlane_b32 s99, v224, 1\n"
"s_cmp_gt_u32 s98, s99\n"
"s_cselect_b32 s97, 8192, 0\n"
"s_add_u32 s84, s84, s97\n"
"v_cvt_pk_bf16_f32 v242, v110, v111\n"
"v_cvt_pk_bf16_f32 v243, v112, v113\n"
"ds_write_b64 v36, v[242:243]\n"
"s_waitcnt lgkmcnt(0)\n"
"s_barrier\n"
"ds_read_b128 v[18:21], v236\n"
"ds_read_b128 v[22:25], v236 offset:64\n"
"ds_read_b128 v[246:249], v17\n"
"ds_read_b128 v[250:253], v17 offset:1024\n"
"s_waitcnt lgkmcnt(3)\n"
"v_mfma_f32_16x16x32_bf16 v[126:129], v[18:21], v[226:229], 0\n"
"s_waitcnt lgkmcnt(2)\n"
"v_mfma_f32_16x16x32_bf16 v[134:137], v[22:25], v[230:233], 0\n"
"v_mfma_f32_16x16x32_bf16 v[130:133], v[18:21], v[102:105], 0\n"
"v_mfma_f32_16x16x32_bf16 v[138:141], v[22:25], v[106:109], 0\n"
"s_waitcnt lgkmcnt(1)\n"
"v_mfma_f32_16x16x32_bf16 v[110:113], v[246:249], v[226:229], 0\n"
"s_waitcnt lgkmcnt(0)\n"
"v_mfma_f32_16x16x32_bf16 v[118:121], v[250:253], v[230:233], 0\n"
"v_mfma_f32_16x16x32_bf16 v[114:117], v[246:249], v[102:105], 0\n"
"v_mfma_f32_16x16x32_bf16 v[122:125], v[250:253], v[106:109], 0\n"
"v_add_f32 v126, 0x358637bd, v126\n"
"v_add_f32 v134, 0x358637bd, v134\n"
"v_add_f32 v130, 0x358637bd, v130\n"
"v_add_f32 v138, 0x358637bd, v138\n"
"v_rcp_f32 v126, v126\n"
"v_rcp_f32 v134, v134\n"
"v_rcp_f32 v130, v130\n"
"v_rcp_f32 v138, v138\n"
"v_pk_mul_f32 v[110:111], v[110:111], v[126:127] op_sel:[0,0] op_sel_hi:[1,0]\n"
"v_pk_mul_f32 v[112:113], v[112:113], v[126:127] op_sel:[0,0] op_sel_hi:[1,0]\n"
"v_pk_mul_f32 v[118:119], v[118:119], v[134:135] op_sel:[0,0] op_sel_hi:[1,0]\n"
"v_pk_mul_f32 v[120:121], v[120:121], v[134:135] op_sel:[0,0] op_sel_hi:[1,0]\n"
"v_cvt_pk_bf16_f32 v26, v110, v111\n"
"v_cvt_pk_bf16_f32 v27, v112, v113\n"
"v_cvt_pk_bf16_f32 v28, v118, v119\n"
"v_cvt_pk_bf16_f32 v29, v120, v121\n"
"buffer_store_dwordx4 v[26:29], v3, s[84:87], s63 offen  sc1\n"
"v_pk_mul_f32 v[114:115], v[114:115], v[130:131] op_sel:[0,0] op_sel_hi:[1,0]\n"
"v_pk_mul_f32 v[116:117], v[116:117], v[130:131] op_sel:[0,0] op_sel_hi:[1,0]\n"
"v_pk_mul_f32 v[122:123], v[122:123], v[138:139] op_sel:[0,0] op_sel_hi:[1,0]\n"
"v_pk_mul_f32 v[124:125], v[124:125], v[138:139] op_sel:[0,0] op_sel_hi:[1,0]\n"
"v_cvt_pk_bf16_f32 v30, v114, v115\n"
"v_cvt_pk_bf16_f32 v31, v116, v117\n"
"v_cvt_pk_bf16_f32 v32, v122, v123\n"
"v_cvt_pk_bf16_f32 v33, v124, v125\n"
"buffer_store_dwordx4 v[30:33], v3, s[84:87], s68 offen  sc1\n"
"v_lshrrev_b32 v20, 2, v2\n"
"v_mul_u32_u24 v20, 224, v20\n"
"v_mul_u32_u24 v16, 896, v1\n"
"v_add_u32 v20, v20, v16\n"
"v_and_b32 v16, 3, v2\n"
"v_lshl_add_u32 v20, v16, 3, v20\n"
"s_lshl_b32 s63, s59, 5\n"
"v_add_u32 v16, s63, v20\n"
"v_add_u32 v17, 57344, v16\n"
"s_cmp_eq_u32 s59, 0\n"
"s_cselect_b32 s63, 192, 0\n"
"v_add_u32 v18, s63, v20\n"
"v_add_u32 v19, 57344, v18\n"
"v_lshlrev_b32 v20, 16, v4\n"
"v_and_b32 v21, 0xffff0000, v4\n"
"v_lshlrev_b32 v22, 16, v5\n"
"v_and_b32 v23, 0xffff0000, v5\n"
"v_lshlrev_b32 v24, 16, v6\n"
"v_and_b32 v25, 0xffff0000, v6\n"
"v_lshlrev_b32 v26, 16, v7\n"
"v_and_b32 v27, 0xffff0000, v7\n"
"v_add_u32 v36, s73, v3\n"
"v_add_u32 v37, s74, v3\n"
"v_add_u32 v118, s75, v3\n"
"s_mul_i32 s63, s34, 6144\n"
"s_add_u32 s63, s63, 90112\n"
"v_add_u32 v119, s63, v3\n"
"v_and_b32 v120, 3, v2\n"
"v_lshlrev_b32 v120, 7, v120\n"
"s_lshl_b32 s94, s34, 9\n"
"s_add_u32 s94, s94, 1343488\n"
"v_lshrrev_b32 v123, 4, v3\n"
"v_mul_u32_u24 v121, 4682, v123\n"
"v_lshrrev_b32 v121, 16, v121\n"
"v_mul_u32_u24 v122, 14, v121\n"
"v_sub_u32 v122, v123, v122\n"
"v_mul_u32_u24 v121, 3584, v121\n"
"v_lshl_add_u32 v121, v122, 4, v121\n"
"s_mul_i32 s95, s34, 896\n"
"s_getpc_b64 s[98:99]\n"
"L_ts9_%=:\n"
"s_mov_b32 vcc_lo, L_pad9_%=-L_ts9_%=\n"
"s_cmp_eq_u32 s34, 1\n"
"s_cselect_b32 vcc_lo, L_pad10_%=-L_ts9_%=, vcc_lo\n"
"s_cmp_eq_u32 s34, 2\n"
"s_cselect_b32 vcc_lo, L_pad11_%=-L_ts9_%=, vcc_lo\n"
"s_cmp_eq_u32 s34, 3\n"
"s_cselect_b32 vcc_lo, L_pad12_%=-L_ts9_%=, vcc_lo\n"
"s_ashr_i32 vcc_hi, vcc_lo, 31\n"
"s_add_u32 s98, s98, vcc_lo\n"
"s_addc_u32 s99, s99, vcc_hi\n"
"s_mov_b64 exec, 0\n"
"s_swappc_b64 vcc, s[98:99]\n"
"s_mov_b64 exec, -1\n"
"s_waitcnt vmcnt(0)\n"
"v_mov_b32 v122, s60\n"
"v_mov_b32 v123, s61\n"
"s_add_u32 s11, s78, 1343488\n"
"s_barrier\n"
"s_mov_b64 exec, 1\n"
"buffer_store_dwordx2 v[122:123], off, s[84:87], s11 sc1\n"
"s_mov_b64 exec, -1\n"
"s_add_u32 m0, s95, 78848\n"
"s_xor_b32 s8, s93, 640\n"
"s_mul_i32 s8, s8, 1792\n"
"s_add_u32 s8, s8, s89\n"
"s_add_u32 s96, s8, 28672\n"
"s_bfm_b64 exec, 56, 0\n"
"buffer_load_dwordx4 v121, s[28:31], s96 offen lds\n"
"s_mov_b64 exec, -1\n"
"s_add_u32 m0, s95, 82432\n"
"s_add_u32 s96, s96, 14336\n"
"s_bfm_b64 exec, 56, 0\n"
"buffer_load_dwordx4 v121, s[28:31], s96 offen lds\n"
"s_mov_b64 exec, -1\n"
"s_add_u32 m0, s95, 86016\n"
"s_xor_b32 s9, s93, 768\n"
"s_mul_i32 s9, s9, 1792\n"
"s_add_u32 s9, s9, s89\n"
"s_add_u32 s96, s9, 0\n"
"s_bfm_b64 exec, 56, 0\n"
"buffer_load_dwordx4 v121, s[28:31], s96 offen lds\n"
"s_mov_b64 exec, -1\n"
"s_mul_i32 s63, s34, 14336\n"
"v_add_u32 v121, s63, v16\n"
"v_add_u32 v154, s63, v18\n"
"ds_read_b64_tr_b16 v[122:123], v121\n"
"ds_read_b64_tr_b16 v[124:125], v121 offset:3584\n"
"ds_read_b64_tr_b16 v[126:127], v121 offset:32\n"
"ds_read_b64_tr_b16 v[128:129], v121 offset:3616\n"
"ds_read_b64_tr_b16 v[130:131], v121 offset:64\n"
"ds_read_b64_tr_b16 v[132:133], v121 offset:3648\n"
"ds_read_b64_tr_b16 v[134:135], v121 offset:96\n"
"ds_read_b64_tr_b16 v[136:137], v121 offset:3680\n"
"ds_read_b64_tr_b16 v[138:139], v121 offset:128\n"
"ds_read_b64_tr_b16 v[140:141], v121 offset:3712\n"
"ds_read_b64_tr_b16 v[142:143], v121 offset:160\n"
"ds_read_b64_tr_b16 v[144:145], v121 offset:3744\n"
"s_waitcnt lgkmcnt(8)\n"
"v_mfma_f32_16x16x32_bf16 a[196:199], a[228:231], v[122:125], 0\n"
"ds_read_b64_tr_b16 v[146:147], v154\n"
"ds_read_b64_tr_b16 v[148:149], v154 offset:3584\n"
"v_mfma_f32_16x16x32_bf16 a[200:203], a[228:231], v[126:129], 0\n"
"ds_read_b64_tr_b16 v[150:151], v121 offset:7168\n"
"ds_read_b64_tr_b16 v[152:153], v121 offset:10752\n"
"s_waitcnt lgkmcnt(8)\n"
"v_mfma_f32_16x16x32_bf16 a[204:207], a[228:231], v[130:133], 0\n"
"ds_read_b64_tr_b16 v[122:123], v121 offset:7200\n"
"ds_read_b64_tr_b16 v[124:125], v121 offset:10784\n"
"v_mfma_f32_16x16x32_bf16 a[208:211], a[228:231], v[134:137], 0\n"
"ds_read_b64_tr_b16 v[126:127], v121 offset:7232\n"
"ds_read_b64_tr_b16 v[128:129], v121 offset:10816\n"
"s_waitcnt lgkmcnt(8)\n"
"v_mfma_f32_16x16x32_bf16 a[212:215], a[228:231], v[138:141], 0\n"
"ds_read_b64_tr_b16 v[130:131], v121 offset:7264\n"
"ds_read_b64_tr_b16 v[132:133], v121 offset:10848\n"
"v_mfma_f32_16x16x32_bf16 a[216:219], a[228:231], v[142:145], 0\n"
"L_pad4_%=:\n"
"s_cbranch_execz L_ret_%=\n"
"ds_read_b64_tr_b16 v[134:135], v121 offset:7296\n"
"ds_read_b64_tr_b16 v[136:137], v121 offset:10880\n"
"s_waitcnt lgkmcnt(8)\n"
"v_mfma_f32_16x16x32_bf16 a[220:223], a[228:231], v[146:149], 0\n"
"ds_read_b64_tr_b16 v[138:139], v121 offset:7328\n"
"ds_read_b64_tr_b16 v[140:141], v121 offset:10912\n"
"v_mfma_f32_16x16x32_bf16 a[196:199], a[228:231], v[150:153], a[196:199]\n"
"ds_read_b64_tr_b16 v[142:143], v154 offset:7168\n"
"ds_read_b64_tr_b16 v[144:145], v154 offset:10752\n"
"s_waitcnt lgkmcnt(8)\n"
"v_mfma_f32_16x16x32_bf16 a[200:203], a[228:231], v[122:125], a[200:203]\n"
"v_mfma_f32_16x16x32_bf16 a[204:207], a[228:231], v[126:129], a[204:207]\n"
"s_waitcnt lgkmcnt(4)\n"
"v_mfma_f32_16x16x32_bf16 a[208:211], a[228:231], v[130:133], a[208:211]\n"
"v_mfma_f32_16x16x32_bf16 a[212:215], a[228:231], v[134:137], a[212:215]\n"
"s_waitcnt lgkmcnt(0)\n"
"v_mfma_f32_16x16x32_bf16 a[216:219], a[228:231], v[138:141], a[216:219]\n"
"v_mfma_f32_16x16x32_bf16 a[220:223], a[228:231], v[142:145], a[220:223]\n"
"s_mov_b32 s62, 40000\n"
"buffer_load_dwordx2 v[12:13], v120, s[84:87], s94 offen  sc1\n"
"L_7_%=:\n"
"s_waitcnt vmcnt(0)\n"
"v_cmp_ne_u64 vcc, s[60:61], v[12:13]\n"
"s_nop 4\n"
"s_cbranch_vccz L_8_%=\n"
"buffer_load_dwordx2 v[12:13], v120, s[84:87], s94 offen  sc1\n"
"s_sub_u32 s62, s62, 1\n"
"s_cbranch_scc0 L_7_%=\n"
"L_8_%=:\n"
"buffer_load_dwordx4 v[138:141], v3, s[84:87], s72 offen  sc1\n"
"buffer_load_dwordx4 v[142:145], v3, s[84:87], s72 offen offset:3072 sc1\n"
"s_add_u32 s10, s72, 16384\n"
"buffer_load_dwordx4 v[146:149], v3, s[84:87], s10 offen  sc1\n"
"s_add_u32 s11, s72, 16384\n"
"buffer_load_dwordx4 v[150:153], v3, s[84:87], s11 offen offset:3072 sc1\n"
"buffer_load_dwordx4 v[122:125], v3, s[84:87], s71 offen  sc1\n"
"buffer_load_dwordx4 v[126:129], v3, s[84:87], s71 offen offset:3072 sc1\n"
"s_add_u32 s8, s71, 16384\n"
"buffer_load_dwordx4 v[130:133], v3, s[84:87], s8 offen  sc1\n"
"s_add_u32 s9, s71, 16384\n"
"buffer_load_dwordx4 v[134:137], v3, s[84:87], s9 offen offset:3072 sc1\n"
"s_add_u32 s10, s72, 524288\n"
"buffer_load_dwordx4 v[170:173], v3, s[84:87], s10 offen  sc1\n"
"s_add_u32 s11, s72, 524288\n"
"buffer_load_dwordx4 v[174:177], v3, s[84:87], s11 offen offset:3072 sc1\n"
"s_add_u32 s8, s72, 540672\n"
"buffer_load_dwordx4 v[178:181], v3, s[84:87], s8 offen  sc1\n"
"s_add_u32 s9, s72, 540672\n"
"buffer_load_dwordx4 v[182:185], v3, s[84:87], s9 offen offset:3072 sc1\n"
"s_add_u32 s10, s71, 524288\n"
"buffer_load_dwordx4 v[154:157], v3, s[84:87], s10 offen  sc1\n"
"s_add_u32 s11, s71, 524288\n"
"buffer_load_dwordx4 v[158:161], v3, s[84:87], s11 offen offset:3072 sc1\n"
"s_add_u32 s8, s71, 540672\n"
"buffer_load_dwordx4 v[162:165], v3, s[84:87], s8 offen  sc1\n"
"s_add_u32 s9, s71, 540672\n"
"buffer_load_dwordx4 v[166:169], v3, s[84:87], s9 offen offset:3072 sc1\n"
"s_waitcnt vmcnt(15)\n"
"v_mfma_f32_16x16x32_bf16 v[102:105], v[38:41], v[138:141], 0\n"
"v_mfma_f32_16x16x32_bf16 v[106:109], v[42:45], v[138:141], 0\n"
"s_add_u32 s10, s15, 8192\n"
"buffer_load_dwordx4 a[40:43], v3, s[52:55], s10 offen  sc1\n"
"v_mfma_f32_16x16x32_bf16 v[110:113], v[46:49], v[138:141], 0\n"
"v_mfma_f32_16x16x32_bf16 v[114:117], v[50:53], v[138:141], 0\n"
"s_waitcnt vmcnt(15)\n"
"v_mfma_f32_16x16x32_bf16 v[102:105], v[54:57], v[142:145], v[102:105]\n"
"s_add_u32 s11, s15, 8192\n"
"buffer_load_dwordx4 a[44:47], v3, s[52:55], s11 offen offset:1024 sc1\n"
"v_mfma_f32_16x16x32_bf16 v[106:109], v[58:61], v[142:145], v[106:109]\n"
"v_mfma_f32_16x16x32_bf16 v[110:113], v[62:65], v[142:145], v[110:113]\n"
"v_mfma_f32_16x16x32_bf16 v[114:117], v[66:69], v[142:145], v[114:117]\n"
"s_add_u32 s8, s14, 12288\n"
"buffer_load_dwordx4 a[48:51], v3, s[52:55], s8 offen  sc1\n"
"s_waitcnt vmcnt(16)\n"
"v_mfma_f32_16x16x32_bf16 v[102:105], v[70:73], v[146:149], v[102:105]\n"
"v_mfma_f32_16x16x32_bf16 v[106:109], v[74:77], v[146:149], v[106:109]\n"
"v_mfma_f32_16x16x32_bf16 v[110:113], v[78:81], v[146:149], v[110:113]\n"
"s_add_u32 s9, s14, 12288\n"
"buffer_load_dwordx4 a[52:55], v3, s[52:55], s9 offen offset:1024 sc1\n"
"v_mfma_f32_16x16x32_bf16 v[114:117], v[82:85], v[146:149], v[114:117]\n"
"s_waitcnt vmcnt(16)\n"
"v_mfma_f32_16x16x32_bf16 v[102:105], v[86:89], v[150:153], v[102:105]\n"
"v_mfma_f32_16x16x32_bf16 v[106:109], v[90:93], v[150:153], v[106:109]\n"
"s_add_u32 s10, s15, 12288\n"
"buffer_load_dwordx4 a[56:59], v3, s[52:55], s10 offen  sc1\n"
"v_mfma_f32_16x16x32_bf16 v[110:113], v[94:97], v[150:153], v[110:113]\n"
"v_mfma_f32_16x16x32_bf16 v[114:117], v[98:101], v[150:153], v[114:117]\n"
"s_waitcnt vmcnt(16)\n"
"v_mfma_f32_16x16x32_bf16 v[28:31], v[46:49], v[122:125], 0\n"
"s_add_u32 s11, s15, 12288\n"
"buffer_load_dwordx4 a[60:63], v3, s[52:55], s11 offen offset:1024 sc1\n"
"v_mfma_f32_16x16x32_bf16 v[32:35], v[50:53], v[122:125], 0\n"
"ds_write_b128 v37, v[102:105]\n"
"s_waitcnt vmcnt(16)\n"
"v_mfma_f32_16x16x32_bf16 v[28:31], v[62:65], v[126:129], v[28:31]\n"
"v_mfma_f32_16x16x32_bf16 v[32:35], v[66:69], v[126:129], v[32:35]\n"
"ds_write_b128 v37, v[106:109] offset:1024\n"
"s_add_u32 s8, s14, 16384\n"
"buffer_load_dwordx4 a[64:67], v3, s[52:55], s8 offen  sc1\n"
"s_waitcnt vmcnt(16)\n"
"v_mfma_f32_16x16x32_bf16 v[28:31], v[78:81], v[130:133], v[28:31]\n"
"v_mfma_f32_16x16x32_bf16 v[32:35], v[82:85], v[130:133], v[32:35]\n"
"ds_write_b128 v118, v[110:113]\n"
"s_waitcnt vmcnt(15)\n"
"v_mfma_f32_16x16x32_bf16 v[28:31], v[94:97], v[134:137], v[28:31]\n"
"s_add_u32 s9, s14, 16384\n"
"buffer_load_dwordx4 a[68:71], v3, s[52:55], s9 offen offset:1024 sc1\n"
"v_mfma_f32_16x16x32_bf16 v[32:35], v[98:101], v[134:137], v[32:35]\n"
"ds_write_b128 v118, v[114:117] offset:1024\n"
"v_mfma_f32_16x16x32_bf16 v[20:23], v[38:41], v[122:125], v[20:23]\n"
"v_mfma_f32_16x16x32_bf16 v[24:27], v[42:45], v[122:125], v[24:27]\n"
"s_nop 1\n"
"ds_write_b128 v36, v[28:31]\n"
"s_add_u32 s10, s15, 16384\n"
"buffer_load_dwordx4 a[72:75], v3, s[52:55], s10 offen  sc1\n"
"v_mfma_f32_16x16x32_bf16 v[20:23], v[54:57], v[126:129], v[20:23]\n"
"ds_write_b128 v36, v[32:35] offset:1024\n"
"v_mfma_f32_16x16x32_bf16 v[24:27], v[58:61], v[126:129], v[24:27]\n"
"s_waitcnt lgkmcnt(0)\n"
"s_barrier\n"
"ds_read_b128 v[28:31], v119\n"
"ds_read_b128 v[32:35], v119 offset:1024\n"
"v_mfma_f32_16x16x32_bf16 v[20:23], v[70:73], v[130:133], v[20:23]\n"
"ds_read_b128 v[102:105], v119 offset:2048\n"
"ds_read_b128 v[106:109], v119 offset:3072\n"
"s_add_u32 s11, s15, 16384\n"
"buffer_load_dwordx4 a[76:79], v3, s[52:55], s11 offen offset:1024 sc1\n"
"v_mfma_f32_16x16x32_bf16 v[24:27], v[74:77], v[130:133], v[24:27]\n"
"ds_read_b128 v[110:113], v119 offset:4096\n"
"ds_read_b128 v[114:117], v119 offset:5120\n"
"v_mfma_f32_16x16x32_bf16 v[20:23], v[86:89], v[134:137], v[20:23]\n"
"v_mfma_f32_16x16x32_bf16 v[24:27], v[90:93], v[134:137], v[24:27]\n"
"s_waitcnt lgkmcnt(5)\n"
"s_nop 5\n"
"v_pk_add_f32 v[28:29], v[28:29], v[20:21] op_sel:[0,0] op_sel_hi:[1,1]\n"
"s_waitcnt lgkmcnt(1)\n"
"v_pk_add_f32 v[102:103], v[102:103], v[110:111] op_sel:[0,0] op_sel_hi:[1,1]\n"
"v_pk_add_f32 v[30:31], v[30:31], v[22:23] op_sel:[0,0] op_sel_hi:[1,1]\n"
"v_pk_add_f32 v[104:105], v[104:105], v[112:113] op_sel:[0,0] op_sel_hi:[1,1]\n"
"v_pk_add_f32 v[32:33], v[32:33], v[24:25] op_sel:[0,0] op_sel_hi:[1,1]\n"
"s_waitcnt lgkmcnt(0)\n"
"v_pk_add_f32 v[106:107], v[106:107], v[114:115] op_sel:[0,0] op_sel_hi:[1,1]\n"
"v_pk_add_f32 v[34:35], v[34:35], v[26:27] op_sel:[0,0] op_sel_hi:[1,1]\n"
"v_pk_add_f32 v[108:109], v[108:109], v[116:117] op_sel:[0,0] op_sel_hi:[1,1]\n"
"v_pk_add_f32 v[28:29], v[28:29], v[102:103] op_sel:[0,0] op_sel_hi:[1,1]\n"
"v_pk_add_f32 v[30:31], v[30:31], v[104:105] op_sel:[0,0] op_sel_hi:[1,1]\n"
"v_pk_add_f32 v[32:33], v[32:33], v[106:107] op_sel:[0,0] op_sel_hi:[1,1]\n"
"v_pk_add_f32 v[34:35], v[34:35], v[108:109] op_sel:[0,0] op_sel_hi:[1,1]\n"
"v_cvt_pk_bf16_f32 v4, v28, v29\n"
"v_cvt_pk_bf16_f32 v5, v30, v31\n"
"v_cvt_pk_bf16_f32 v6, v32, v33\n"
"v_cvt_pk_bf16_f32 v7, v34, v35\n"
"buffer_store_dwordx4 v[4:7], v3, s[84:87], s80 offen  sc1\n"
"v_mov_b32 v120, 0\n"
"v_mov_b32 v121, 0\n"
"v_lshlrev_b32 v186, 4, v2\n"
"v_mov_b32 v187, 2147483392\n"
"v_cmp_eq_u32 vcc, 0, v1\n"
"v_dot2c_f32_bf16 v120, s90, v4\n"
"v_dot2c_f32_bf16 v121, v4, v4\n"
"v_dot2c_f32_bf16 v120, s90, v5\n"
"v_dot2c_f32_bf16 v121, v5, v5\n"
"v_dot2c_f32_bf16 v120, s90, v6\n"
"v_dot2c_f32_bf16 v121, v6, v6\n"
"v_dot2c_f32_bf16 v120, s90, v7\n"
"v_dot2c_f32_bf16 v121, v7, v7\n"
"v_cndmask_b32 v186, v187, v186, vcc\n"
"s_nop 1\n"
"v_permlane32_swap_b32 v120, v121\n"
"v_add_f32 v120, v120, v121\n"
"v_mov_b32 v121, v120\n"
"s_nop 1\n"
"v_permlane16_swap_b32 v120, v121\n"
"v_add_f32 v120, v120, v121\n"
"v_mov_b32 v121, v120\n"
"s_nop 1\n"
"v_permlane32_swap_b32 v120, v121\n"
"buffer_store_dwordx2 v[120:121], v186, s[84:87], s81 offen  sc1\n"
"v_lshlrev_b32 v20, 16, v8\n"
"v_and_b32 v21, 0xffff0000, v8\n"
"v_lshlrev_b32 v22, 16, v9\n"
"v_and_b32 v23, 0xffff0000, v9\n"
"v_lshlrev_b32 v24, 16, v10\n"
"v_and_b32 v25, 0xffff0000, v10\n"
"v_lshlrev_b32 v26, 16, v11\n"
"v_and_b32 v27, 0xffff0000, v11\n"
"s_add_u32 s8, s14, 20480\n"
"buffer_load_dwordx4 a[80:83], v3, s[52:55], s8 offen  sc1\n"
"s_waitcnt vmcnt(20)\n"
"v_mfma_f32_16x16x32_bf16 v[102:105], v[38:41], v[170:173], 0\n"
"v_mfma_f32_16x16x32_bf16 v[106:109], v[42:45], v[170:173], 0\n"
"v_mfma_f32_16x16x32_bf16 v[110:113], v[46:49], v[170:173], 0\n"
"s_add_u32 s9, s14, 20480\n"
"buffer_load_dwordx4 a[84:87], v3, s[52:55], s9 offen offset:1024 sc1\n"
"v_mfma_f32_16x16x32_bf16 v[114:117], v[50:53], v[170:173], 0\n"
"s_waitcnt vmcnt(20)\n"
"v_mfma_f32_16x16x32_bf16 v[102:105], v[54:57], v[174:177], v[102:105]\n"
"v_mfma_f32_16x16x32_bf16 v[106:109], v[58:61], v[174:177], v[106:109]\n"
"s_add_u32 s10, s15, 20480\n"
"buffer_load_dwordx4 a[88:91], v3, s[52:55], s10 offen  sc1\n"
"v_mfma_f32_16x16x32_bf16 v[110:113], v[62:65], v[174:177], v[110:113]\n"
"v_mfma_f32_16x16x32_bf16 v[114:117], v[66:69], v[174:177], v[114:117]\n"
"s_waitcnt vmcnt(20)\n"
"v_mfma_f32_16x16x32_bf16 v[102:105], v[70:73], v[178:181], v[102:105]\n"
"s_add_u32 s11, s15, 20480\n"
"buffer_load_dwordx4 a[92:95], v3, s[52:55], s11 offen offset:1024 sc1\n"
"v_mfma_f32_16x16x32_bf16 v[106:109], v[74:77], v[178:181], v[106:109]\n"
"v_mfma_f32_16x16x32_bf16 v[110:113], v[78:81], v[178:181], v[110:113]\n"
"v_mfma_f32_16x16x32_bf16 v[114:117], v[82:85], v[178:181], v[114:117]\n"
"s_add_u32 s8, s14, 24576\n"
"buffer_load_dwordx4 a[96:99], v3, s[52:55], s8 offen  sc1\n"
"s_waitcnt vmcnt(21)\n"
"v_mfma_f32_16x16x32_bf16 v[102:105], v[86:89], v[182:185], v[102:105]\n"
"v_mfma_f32_16x16x32_bf16 v[106:109], v[90:93], v[182:185], v[106:109]\n"
"v_mfma_f32_16x16x32_bf16 v[110:113], v[94:97], v[182:185], v[110:113]\n"
"s_add_u32 s9, s14, 24576\n"
"buffer_load_dwordx4 a[100:103], v3, s[52:55], s9 offen offset:1024 sc1\n"
"v_mfma_f32_16x16x32_bf16 v[114:117], v[98:101], v[182:185], v[114:117]\n"
"s_waitcnt vmcnt(21)\n"
"v_mfma_f32_16x16x32_bf16 v[28:31], v[46:49], v[154:157], 0\n"
"v_mfma_f32_16x16x32_bf16 v[32:35], v[50:53], v[154:157], 0\n"
"s_add_u32 s10, s15, 24576\n"
"buffer_load_dwordx4 a[104:107], v3, s[52:55], s10 offen  sc1\n"
"s_waitcnt vmcnt(21)\n"
"v_mfma_f32_16x16x32_bf16 v[28:31], v[62:65], v[158:161], v[28:31]\n"
"v_mfma_f32_16x16x32_bf16 v[32:35], v[66:69], v[158:161], v[32:35]\n"
"s_waitcnt vmcnt(20)\n"
"v_mfma_f32_16x16x32_bf16 v[28:31], v[78:81], v[162:165], v[28:31]\n"
"s_add_u32 s11, s15, 24576\n"
"buffer_load_dwordx4 a[108:111], v3, s[52:55], s11 offen offset:1024 sc1\n"
"v_mfma_f32_16x16x32_bf16 v[32:35], v[82:85], v[162:165], v[32:35]\n"
"s_waitcnt vmcnt(20)\n"
"v_mfma_f32_16x16x32_bf16 v[28:31], v[94:97], v[166:169], v[28:31]\n"
"v_mfma_f32_16x16x32_bf16 v[32:35], v[98:101], v[166:169], v[32:35]\n"
"s_add_u32 s8, s14, 28672\n"
"buffer_load_dwordx4 a[112:115], v3, s[52:55], s8 offen  sc1\n"
"v_mfma_f32_16x16x32_bf16 v[20:23], v[38:41], v[154:157], v[20:23]\n"
"v_mfma_f32_16x16x32_bf16 v[24:27], v[42:45], v[154:157], v[24:27]\n"
"v_mfma_f32_16x16x32_bf16 v[20:23], v[54:57], v[158:161], v[20:23]\n"
"v_mfma_f32_16x16x32_bf16 v[24:27], v[58:61], v[158:161], v[24:27]\n"
"v_mfma_f32_16x16x32_bf16 v[20:23], v[70:73], v[162:165], v[20:23]\n"
"v_mfma_f32_16x16x32_bf16 v[24:27], v[74:77], v[162:165], v[24:27]\n"
"v_mfma_f32_16x16x32_bf16 v[20:23], v[86:89], v[166:169], v[20:23]\n"
"v_mfma_f32_16x16x32_bf16 v[24:27], v[90:93], v[166:169], v[24:27]\n"
"v_lshlrev_b32 v124, 4, v2\n"
"v_lshl_add_u32 v124, v1, 9, v124\n"
"s_lshl_b32 s12, s34, 11\n"
"s_add_u32 s12, s12, 1310720\n"
"s_lshl_b32 s13, s34, 15\n"
"s_add_u32 s13, s13, 1048576\n"
"s_and_b32 s63, s93, 512\n"
"s_add_u32 s63, s63, s93\n"
"s_lshl4_add_u32 s13, s63, s13\n"
"v_and_b32 v128, 7, v2\n"
"v_lshlrev_b32 v128, 7, v128\n"
"s_lshr_b32 s95, s34, 1\n"
"s_lshl_b32 s95, s95, 10\n"
"s_add_u32 s95, s95, 1343496\n"
"s_waitcnt vmcnt(9)\n"
"v_mov_b32 v130, s60\n"
"v_mov_b32 v131, s61\n"
"s_add_u32 s9, s78, 1343496\n"
"s_barrier\n"
"s_mov_b64 exec, 1\n"
"buffer_store_dwordx2 v[130:131], off, s[84:87], s9 sc1\n"
"s_mov_b64 exec, -1\n"
"ds_write_b128 v37, v[102:105]\n"
"ds_write_b128 v37, v[106:109] offset:1024\n"
"ds_write_b128 v118, v[110:113]\n"
"ds_write_b128 v118, v[114:117] offset:1024\n"
"ds_write_b128 v36, v[28:31]\n"
"ds_write_b128 v36, v[32:35] offset:1024\n"
"s_waitcnt lgkmcnt(0)\n"
"s_barrier\n"
"ds_read_b128 v[28:31], v119\n"
"ds_read_b128 v[32:35], v119 offset:1024\n"
"ds_read_b128 v[102:105], v119 offset:2048\n"
"ds_read_b128 v[106:109], v119 offset:3072\n"
"ds_read_b128 v[110:113], v119 offset:4096\n"
"ds_read_b128 v[114:117], v119 offset:5120\n"
"s_waitcnt lgkmcnt(5)\n"
"v_pk_add_f32 v[28:29], v[28:29], v[20:21] op_sel:[0,0] op_sel_hi:[1,1]\n"
"s_waitcnt lgkmcnt(1)\n"
"v_pk_add_f32 v[102:103], v[102:103], v[110:111] op_sel:[0,0] op_sel_hi:[1,1]\n"
"v_pk_add_f32 v[30:31], v[30:31], v[22:23] op_sel:[0,0] op_sel_hi:[1,1]\n"
"v_pk_add_f32 v[104:105], v[104:105], v[112:113] op_sel:[0,0] op_sel_hi:[1,1]\n"
"v_pk_add_f32 v[32:33], v[32:33], v[24:25] op_sel:[0,0] op_sel_hi:[1,1]\n"
"s_waitcnt lgkmcnt(0)\n"
"v_pk_add_f32 v[106:107], v[106:107], v[114:115] op_sel:[0,0] op_sel_hi:[1,1]\n"
"v_pk_add_f32 v[34:35], v[34:35], v[26:27] op_sel:[0,0] op_sel_hi:[1,1]\n"
"v_pk_add_f32 v[108:109], v[108:109], v[116:117] op_sel:[0,0] op_sel_hi:[1,1]\n"
"v_pk_add_f32 v[28:29], v[28:29], v[102:103] op_sel:[0,0] op_sel_hi:[1,1]\n"
"v_pk_add_f32 v[30:31], v[30:31], v[104:105] op_sel:[0,0] op_sel_hi:[1,1]\n"
"v_pk_add_f32 v[32:33], v[32:33], v[106:107] op_sel:[0,0] op_sel_hi:[1,1]\n"
"v_pk_add_f32 v[34:35], v[34:35], v[108:109] op_sel:[0,0] op_sel_hi:[1,1]\n"
"v_cvt_pk_bf16_f32 v8, v28, v29\n"
"v_cvt_pk_bf16_f32 v9, v30, v31\n"
"v_cvt_pk_bf16_f32 v10, v32, v33\n"
"v_cvt_pk_bf16_f32 v11, v34, v35\n"
"s_add_u32 s10, s80, 131072\n"
"buffer_store_dwordx4 v[8:11], v3, s[84:87], s10 offen  sc1\n"
"v_mov_b32 v130, 0\n"
"v_mov_b32 v131, 0\n"
"v_lshlrev_b32 v129, 4, v2\n"
"v_mov_b32 v132, 2147483392\n"
"v_cmp_eq_u32 vcc, 0, v1\n"
"v_dot2c_f32_bf16 v130, s90, v8\n"
"v_dot2c_f32_bf16 v131, v8, v8\n"
"v_dot2c_f32_bf16 v130, s90, v9\n"
"v_dot2c_f32_bf16 v131, v9, v9\n"
"v_dot2c_f32_bf16 v130, s90, v10\n"
"v_dot2c_f32_bf16 v131, v10, v10\n"
"v_dot2c_f32_bf16 v130, s90, v11\n"
"v_dot2c_f32_bf16 v131, v11, v11\n"
"v_cndmask_b32 v129, v132, v129, vcc\n"
"s_nop 1\n"
"v_permlane32_swap_b32 v130, v131\n"
"v_add_f32 v130, v130, v131\n"
"v_mov_b32 v131, v130\n"
"s_nop 1\n"
"v_permlane16_swap_b32 v130, v131\n"
"v_add_f32 v130, v130, v131\n"
"v_mov_b32 v131, v130\n"
"s_nop 1\n"
"v_permlane32_swap_b32 v130, v131\n"
"s_add_u32 s11, s81, 16384\n"
"buffer_store_dwordx2 v[130:131], v129, s[84:87], s11 offen  sc1\n"
"v_lshrrev_b32 v21, 4, v3\n"
"v_mul_u32_u24 v129, 4682, v21\n"
"v_lshrrev_b32 v129, 16, v129\n"
"v_mul_u32_u24 v20, 14, v129\n"
"v_sub_u32 v20, v21, v20\n"
"v_mul_u32_u24 v129, 3584, v129\n"
"v_lshl_add_u32 v129, v20, 4, v129\n"
"s_mul_i32 s96, s34, 896\n"
"v_and_b32 v20, 1, v1\n"
"v_lshlrev_b32 v20, 6, v20\n"
"v_lshl_add_u32 v20, v2, 2, v20\n"
"v_add_u32 v20, 160256, v20\n"
"s_lshr_b32 s63, s56, 1\n"
"s_lshl3_add_u32 s63, s34, s63\n"
"s_lshl_b32 s63, s63, 14\n"
"s_lshl_b32 s68, s59, 12\n"
"s_add_u32 s98, s63, s68\n"
"s_lshl3_add_u32 s99, s59, s63\n"
"s_getpc_b64 s[20:21]\n"
"L_ts13_%=:\n"
"s_mov_b32 vcc_lo, L_pad13_%=-L_ts13_%=\n"
"s_cmp_eq_u32 s34, 1\n"
"s_cselect_b32 vcc_lo, L_pad14_%=-L_ts13_%=, vcc_lo\n"
"s_cmp_eq_u32 s34, 2\n"
"s_cselect_b32 vcc_lo, L_pad15_%=-L_ts13_%=, vcc_lo\n"
"s_cmp_eq_u32 s34, 3\n"
"s_cselect_b32 vcc_lo, L_pad16_%=-L_ts13_%=, vcc_lo\n"
"s_ashr_i32 vcc_hi, vcc_lo, 31\n"
"s_add_u32 s20, s20, vcc_lo\n"
"s_addc_u32 s21, s21, vcc_hi\n"
"s_mov_b64 exec, 0\n"
"s_swappc_b64 vcc, s[20:21]\n"
"s_mov_b64 exec, -1\n"
"s_mul_i32 s63, s34, 7168\n"
"v_add_u32 v21, s63, v17\n"
"v_add_u32 v118, s63, v19\n"
"ds_read_b64_tr_b16 v[22:23], v21\n"
"ds_read_b64_tr_b16 v[24:25], v21 offset:3584\n"
"ds_read_b64_tr_b16 v[26:27], v21 offset:32\n"
"ds_read_b64_tr_b16 v[28:29], v21 offset:3616\n"
"ds_read_b64_tr_b16 v[30:31], v21 offset:64\n"
"ds_read_b64_tr_b16 v[32:33], v21 offset:3648\n"
"ds_read_b64_tr_b16 v[34:35], v21 offset:96\n"
"ds_read_b64_tr_b16 v[36:37], v21 offset:3680\n"
"ds_read_b64_tr_b16 v[102:103], v21 offset:128\n"
"ds_read_b64_tr_b16 v[104:105], v21 offset:3712\n"
"ds_read_b64_tr_b16 v[106:107], v21 offset:160\n"
"ds_read_b64_tr_b16 v[108:109], v21 offset:3744\n"
"s_waitcnt lgkmcnt(8)\n"
"v_mfma_f32_16x16x32_bf16 a[196:199], a[228:231], v[22:25], a[196:199]\n"
"ds_read_b64_tr_b16 v[110:111], v118\n"
"ds_read_b64_tr_b16 v[112:113], v118 offset:3584\n"
"v_mfma_f32_16x16x32_bf16 a[200:203], a[228:231], v[26:29], a[200:203]\n"
"s_waitcnt lgkmcnt(6)\n"
"v_mfma_f32_16x16x32_bf16 a[204:207], a[228:231], v[30:33], a[204:207]\n"
"v_mfma_f32_16x16x32_bf16 a[208:211], a[228:231], v[34:37], a[208:211]\n"
"s_waitcnt lgkmcnt(2)\n"
"v_mfma_f32_16x16x32_bf16 a[212:215], a[228:231], v[102:105], a[212:215]\n"
"v_mfma_f32_16x16x32_bf16 a[216:219], a[228:231], v[106:109], a[216:219]\n"
"s_waitcnt lgkmcnt(0)\n"
"v_mfma_f32_16x16x32_bf16 a[220:223], a[228:231], v[110:113], a[220:223]\n"
"v_lshlrev_b32 v236, 4, v2\n"
"v_lshl_add_u32 v236, v1, 9, v236\n"
"s_lshl_b32 s20, s34, 11\n"
"s_add_u32 s20, s20, 1327104\n"
"s_lshl_b32 s21, s34, 15\n"
"s_add_u32 s21, s21, 1179648\n"
"s_and_b32 s63, s93, 512\n"
"s_add_u32 s63, s63, s93\n"
"s_lshl4_add_u32 s21, s63, s21\n"
"v_and_b32 v240, 7, v2\n"
"v_lshlrev_b32 v240, 7, v240\n"
"s_lshr_b32 s23, s34, 1\n"
"s_lshl_b32 s23, s23, 10\n"
"s_add_u32 s23, s23, 1343504\n"
"s_mul_i32 s63, s34, 7168\n"
"v_add_u32 v241, s63, v17\n"
"v_add_u32 v242, s63, v19\n"
"ds_read_b64_tr_b16 v[22:23], v16\n"
"ds_read_b64_tr_b16 v[24:25], v16 offset:3584\n"
"ds_read_b64_tr_b16 v[26:27], v16 offset:32\n"
"ds_read_b64_tr_b16 v[28:29], v16 offset:3616\n"
"ds_read_b64_tr_b16 v[30:31], v16 offset:64\n"
"ds_read_b64_tr_b16 v[32:33], v16 offset:3648\n"
"ds_read_b64_tr_b16 v[34:35], v16 offset:96\n"
"ds_read_b64_tr_b16 v[36:37], v16 offset:3680\n"
"ds_read_b64_tr_b16 v[102:103], v16 offset:128\n"
"ds_read_b64_tr_b16 v[104:105], v16 offset:3712\n"
"ds_read_b64_tr_b16 v[106:107], v16 offset:160\n"
"ds_read_b64_tr_b16 v[108:109], v16 offset:3744\n"
"s_mov_b32 s62, 40000\n"
"buffer_load_dwordx2 v[12:13], v128, s[84:87], s95 offen  sc1\n"
"L_9_%=:\n"
"s_waitcnt vmcnt(0)\n"
"v_cmp_ne_u64 vcc, s[60:61], v[12:13]\n"
"s_nop 4\n"
"s_cbranch_vccz L_10_%=\n"
"buffer_load_dwordx2 v[12:13], v128, s[84:87], s95 offen  sc1\n"
"s_sub_u32 s62, s62, 1\n"
"s_cbranch_scc0 L_9_%=\n"
"L_10_%=:\n"
"buffer_load_dwordx4 v[38:41], v3, s[84:87], s13 offen  sc1\n"
"buffer_load_dwordx4 v[42:45], v3, s[84:87], s13 offen offset:1024 sc1\n"
"buffer_load_dwordx4 v[120:123], v124, s[84:87], s12 offen  sc1\n"
"buffer_load_dwordx4 v[124:127], v124, s[84:87], s12 offen offset:256 sc1\n"
"s_xor_b32 s94, s13, 2048\n"
"buffer_load_dwordx4 v[46:49], v3, s[84:87], s94 offen  sc1\n"
"buffer_load_dwordx4 v[50:53], v3, s[84:87], s94 offen offset:1024 sc1\n"
"s_waitcnt vmcnt(6)\n"
"v_mov_b32 v244, s60\n"
"v_mov_b32 v245, s61\n"
"s_add_u32 s8, s78, 1343504\n"
"s_barrier\n"
"s_mov_b64 exec, 1\n"
"buffer_store_dwordx2 v[244:245], off, s[84:87], s8 sc1\n"
"s_mov_b64 exec, -1\n"
"s_xor_b32 s94, s13, 4096\n"
"buffer_load_dwordx4 v[54:57], v3, s[84:87], s94 offen  sc1\n"
"buffer_load_dwordx4 v[58:61], v3, s[84:87], s94 offen offset:1024 sc1\n"
"s_xor_b32 s94, s13, 6144\n"
"buffer_load_dwordx4 v[62:65], v3, s[84:87], s94 offen  sc1\n"
"buffer_load_dwordx4 v[66:69], v3, s[84:87], s94 offen offset:1024 sc1\n"
"s_xor_b32 s94, s13, 16384\n"
"buffer_load_dwordx4 v[70:73], v3, s[84:87], s94 offen  sc1\n"
"buffer_load_dwordx4 v[74:77], v3, s[84:87], s94 offen offset:1024 sc1\n"
"s_xor_b32 s94, s13, 18432\n"
"buffer_load_dwordx4 v[78:81], v3, s[84:87], s94 offen  sc1\n"
"s_waitcnt lgkmcnt(8) vmcnt(13)\n"
"v_mfma_f32_16x16x32_bf16 v[136:139], v[22:25], v[38:41], 0\n"
"ds_read_b64_tr_b16 v[110:111], v18\n"
"ds_read_b64_tr_b16 v[112:113], v18 offset:3584\n"
"buffer_load_dwordx4 v[82:85], v3, s[84:87], s94 offen offset:1024 sc1\n"
"v_mfma_f32_16x16x32_bf16 v[140:143], v[26:29], v[38:41], 0\n"
"ds_read_b64_tr_b16 v[114:115], v16 offset:7168\n"
"ds_read_b64_tr_b16 v[116:117], v16 offset:10752\n"
"s_waitcnt lgkmcnt(8)\n"
"v_mfma_f32_16x16x32_bf16 v[144:147], v[30:33], v[38:41], 0\n"
"ds_read_b64_tr_b16 v[22:23], v16 offset:7200\n"
"ds_read_b64_tr_b16 v[24:25], v16 offset:10784\n"
"v_mfma_f32_16x16x32_bf16 v[148:151], v[34:37], v[38:41], 0\n"
"ds_read_b64_tr_b16 v[26:27], v16 offset:7232\n"
"ds_read_b64_tr_b16 v[28:29], v16 offset:10816\n"
"s_xor_b32 s94, s13, 20480\n"
"buffer_load_dwordx4 v[86:89], v3, s[84:87], s94 offen  sc1\n"
"s_waitcnt lgkmcnt(8)\n"
"v_mfma_f32_16x16x32_bf16 v[152:155], v[102:105], v[38:41], 0\n"
"ds_read_b64_tr_b16 v[30:31], v16 offset:7264\n"
"ds_read_b64_tr_b16 v[32:33], v16 offset:10848\n"
"s_waitcnt vmcnt(13)\n"
"v_add_f32 v120, v120, v122\n"
"v_mfma_f32_16x16x32_bf16 v[156:159], v[106:109], v[38:41], 0\n"
"ds_read_b64_tr_b16 v[34:35], v16 offset:7296\n"
"ds_read_b64_tr_b16 v[36:37], v16 offset:10880\n"
"v_add_f32 v121, v121, v123\n"
"s_waitcnt lgkmcnt(8)\n"
"v_mfma_f32_16x16x32_bf16 v[160:163], v[110:113], v[38:41], 0\n"
"ds_read_b64_tr_b16 v[102:103], v16 offset:7328\n"
"ds_read_b64_tr_b16 v[104:105], v16 offset:10912\n"
"buffer_load_dwordx4 v[90:93], v3, s[84:87], s94 offen offset:1024 sc1\n"
"s_waitcnt vmcnt(13)\n"
"v_add_f32 v124, v124, v126\n"
"v_mfma_f32_16x16x32_bf16 v[136:139], v[114:117], v[42:45], v[136:139]\n"
"ds_read_b64_tr_b16 v[106:107], v18 offset:7168\n"
"ds_read_b64_tr_b16 v[108:109], v18 offset:10752\n"
"v_add_f32 v125, v125, v127\n"
"s_waitcnt lgkmcnt(8)\n"
"v_mfma_f32_16x16x32_bf16 v[140:143], v[22:25], v[42:45], v[140:143]\n"
"ds_read_b64_tr_b16 v[110:111], v16 offset:14336\n"
"ds_read_b64_tr_b16 v[112:113], v16 offset:17920\n"
"v_add_f32 v120, v120, v124\n"
"v_mfma_f32_16x16x32_bf16 v[144:147], v[26:29], v[42:45], v[144:147]\n"
"ds_read_b64_tr_b16 v[114:115], v16 offset:14368\n"
"ds_read_b64_tr_b16 v[116:117], v16 offset:17952\n"
"s_xor_b32 s94, s13, 22528\n"
"buffer_load_dwordx4 v[94:97], v3, s[84:87], s94 offen  sc1\n"
"v_add_f32 v121, v121, v125\n"
"s_waitcnt lgkmcnt(8)\n"
"v_mfma_f32_16x16x32_bf16 v[148:151], v[30:33], v[42:45], v[148:151]\n"
"ds_read_b64_tr_b16 v[22:23], v16 offset:14400\n"
"ds_read_b64_tr_b16 v[24:25], v16 offset:17984\n"
"v_permlane32_swap_b32 v120, v121\n"
"v_mfma_f32_16x16x32_bf16 v[152:155], v[34:37], v[42:45], v[152:155]\n"
"ds_read_b64_tr_b16 v[26:27], v16 offset:14432\n"
"ds_read_b64_tr_b16 v[28:29], v16 offset:18016\n"
"v_add_f32 v120, v120, v121\n"
"s_waitcnt lgkmcnt(8)\n"
"v_mfma_f32_16x16x32_bf16 v[156:159], v[102:105], v[42:45], v[156:159]\n"
"ds_read_b64_tr_b16 v[30:31], v16 offset:14464\n"
"ds_read_b64_tr_b16 v[32:33], v16 offset:18048\n"
"buffer_load_dwordx4 v[98:101], v3, s[84:87], s94 offen offset:1024 sc1\n"
"v_mov_b32 v121, v120\n"
"v_mfma_f32_16x16x32_bf16 v[160:163], v[106:109], v[42:45], v[160:163]\n"
"ds_read_b64_tr_b16 v[34:35], v16 offset:14496\n"
"ds_read_b64_tr_b16 v[36:37], v16 offset:18080\n"
"v_permlane16_swap_b32 v120, v121\n"
"s_waitcnt lgkmcnt(8) vmcnt(14)\n"
"v_mfma_f32_16x16x32_bf16 v[136:139], v[110:113], v[46:49], v[136:139]\n"
"ds_read_b64_tr_b16 v[102:103], v18 offset:14336\n"
"ds_read_b64_tr_b16 v[104:105], v18 offset:17920\n"
"v_add_f32 v120, v120, v121\n"
"v_mfma_f32_16x16x32_bf16 v[140:143], v[114:117], v[46:49], v[140:143]\n"
"ds_read_b64_tr_b16 v[106:107], v16 offset:21504\n"
"ds_read_b64_tr_b16 v[108:109], v16 offset:25088\n"
"s_add_u32 m0, s96, 89600\n"
"s_xor_b32 s9, s93, 768\n"
"s_mul_i32 s9, s9, 1792\n"
"s_add_u32 s9, s9, s89\n"
"s_add_u32 s97, s9, 14336\n"
"s_bfm_b64 exec, 56, 0\n"
"buffer_load_dwordx4 v129, s[28:31], s97 offen lds\n"
"s_mov_b64 exec, -1\n"
"v_mov_b32 v121, v120\n"
"buffer_load_dwordx2 v[244:245], v240, s[84:87], s23 offen  sc1\n"
"s_waitcnt lgkmcnt(8)\n"
"v_mfma_f32_16x16x32_bf16 v[144:147], v[22:25], v[46:49], v[144:147]\n"
"ds_read_b64_tr_b16 v[110:111], v16 offset:21536\n"
"ds_read_b64_tr_b16 v[112:113], v16 offset:25120\n"
"v_permlane32_swap_b32 v120, v121\n"
"v_mfma_f32_16x16x32_bf16 v[148:151], v[26:29], v[46:49], v[148:151]\n"
"ds_read_b64_tr_b16 v[114:115], v16 offset:21568\n"
"ds_read_b64_tr_b16 v[116:117], v16 offset:25152\n"
"v_mul_f32 v120, 0x3b000000, v120\n"
"s_waitcnt lgkmcnt(8)\n"
"v_mfma_f32_16x16x32_bf16 v[152:155], v[30:33], v[46:49], v[152:155]\n"
"ds_read_b64_tr_b16 v[22:23], v16 offset:21600\n"
"ds_read_b64_tr_b16 v[24:25], v16 offset:25184\n"
"s_add_u32 m0, s96, 93184\n"
"s_add_u32 s97, s97, 14336\n"
"s_bfm_b64 exec, 56, 0\n"
"buffer_load_dwordx4 v129, s[28:31], s97 offen lds\n"
"s_mov_b64 exec, -1\n"
"v_mul_f32 v121, 0x3b000000, v121\n"
"v_mfma_f32_16x16x32_bf16 v[156:159], v[34:37], v[46:49], v[156:159]\n"
"ds_read_b64_tr_b16 v[26:27], v16 offset:21632\n"
"ds_read_b64_tr_b16 v[28:29], v16 offset:25216\n"
"v_fma_f32 v121, -v120, v120, v121\n"
"s_waitcnt lgkmcnt(8)\n"
"v_mfma_f32_16x16x32_bf16 v[160:163], v[102:105], v[46:49], v[160:163]\n"
"ds_read_b64_tr_b16 v[30:31], v16 offset:21664\n"
"ds_read_b64_tr_b16 v[32:33], v16 offset:25248\n"
"v_max_f32 v121, 0, v121\n"
"s_waitcnt vmcnt(16)\n"
"v_mfma_f32_16x16x32_bf16 v[136:139], v[106:109], v[50:53], v[136:139]\n"
"ds_read_b64_tr_b16 v[34:35], v18 offset:21504\n"
"ds_read_b64_tr_b16 v[36:37], v18 offset:25088\n"
"s_add_u32 m0, s96, 96768\n"
"s_add_u32 s97, s97, 14336\n"
"s_bfm_b64 exec, 56, 0\n"
"buffer_load_dwordx4 v129, s[28:31], s97 offen lds\n"
"s_mov_b64 exec, -1\n"
"v_add_f32 v133, 0x3727c5ac, v121\n"
"s_waitcnt lgkmcnt(8)\n"
"v_mfma_f32_16x16x32_bf16 v[140:143], v[110:113], v[50:53], v[140:143]\n"
"ds_read_b64_tr_b16 v[102:103], v16 offset:28672\n"
"ds_read_b64_tr_b16 v[104:105], v16 offset:32256\n"
"v_rsq_f32 v133, v133\n"
"v_mfma_f32_16x16x32_bf16 v[144:147], v[114:117], v[50:53], v[144:147]\n"
"ds_read_b64_tr_b16 v[106:107], v16 offset:28704\n"
"ds_read_b64_tr_b16 v[108:109], v16 offset:32288\n"
"v_mul_f32 v121, v121, v133\n"
"s_waitcnt lgkmcnt(8)\n"
"v_mfma_f32_16x16x32_bf16 v[148:151], v[22:25], v[50:53], v[148:151]\n"
"ds_read_b64_tr_b16 v[110:111], v16 offset:28736\n"
"ds_read_b64_tr_b16 v[112:113], v16 offset:32320\n"
"s_add_u32 m0, s96, 100352\n"
"s_xor_b32 s10, s93, 896\n"
"s_mul_i32 s10, s10, 1792\n"
"s_add_u32 s10, s10, s89\n"
"s_add_u32 s97, s10, 0\n"
"s_bfm_b64 exec, 56, 0\n"
"buffer_load_dwordx4 v129, s[28:31], s97 offen lds\n"
"s_mov_b64 exec, -1\n"
"v_fmaak_f32 v121, v121, v133, 0x3727c5ac\n"
"v_mfma_f32_16x16x32_bf16 v[152:155], v[26:29], v[50:53], v[152:155]\n"
"ds_read_b64_tr_b16 v[114:115], v16 offset:28768\n"
"ds_read_b64_tr_b16 v[116:117], v16 offset:32352\n"
"v_rsq_f32 v121, v121\n"
"s_waitcnt lgkmcnt(8)\n"
"v_mfma_f32_16x16x32_bf16 v[156:159], v[30:33], v[50:53], v[156:159]\n"
"ds_read_b64_tr_b16 v[22:23], v16 offset:28800\n"
"ds_read_b64_tr_b16 v[24:25], v16 offset:32384\n"
"v_mul_f32 v121, v121, v133\n"
"v_mfma_f32_16x16x32_bf16 v[160:163], v[34:37], v[50:53], v[160:163]\n"
"ds_read_b64_tr_b16 v[26:27], v16 offset:28832\n"
"ds_read_b64_tr_b16 v[28:29], v16 offset:32416\n"
"s_add_u32 m0, s96, 103936\n"
"s_add_u32 s97, s97, 14336\n"
"s_bfm_b64 exec, 56, 0\n"
"buffer_load_dwordx4 v129, s[28:31], s97 offen lds\n"
"s_mov_b64 exec, -1\n"
"L_pad5_%=:\n"
"s_cbranch_execz L_ret_%=\n"
"v_rcp_f32 v21, v121\n"
"s_waitcnt lgkmcnt(8) vmcnt(17)\n"
"v_mfma_f32_16x16x32_bf16 v[136:139], v[102:105], v[54:57], v[136:139]\n"
"ds_read_b64_tr_b16 v[30:31], v18 offset:28672\n"
"ds_read_b64_tr_b16 v[32:33], v18 offset:32256\n"
"v_mul_f32 v120, -1.0, v120\n"
"v_mfma_f32_16x16x32_bf16 v[140:143], v[106:109], v[54:57], v[140:143]\n"
"ds_read_b64_tr_b16 v[34:35], v16 offset:35840\n"
"ds_read_b64_tr_b16 v[36:37], v16 offset:39424\n"
"v_mul_f32 v119, 0xc0135761, v121\n"
"s_waitcnt lgkmcnt(8)\n"
"v_mfma_f32_16x16x32_bf16 v[144:147], v[110:113], v[54:57], v[144:147]\n"
"ds_read_b64_tr_b16 v[102:103], v16 offset:35872\n"
"ds_read_b64_tr_b16 v[104:105], v16 offset:39456\n"
"s_add_u32 m0, s96, 107520\n"
"s_add_u32 s97, s97, 14336\n"
"s_bfm_b64 exec, 56, 0\n"
"buffer_load_dwordx4 v129, s[28:31], s97 offen lds\n"
"s_mov_b64 exec, -1\n"
"v_mul_f32 v165, v121, v121\n"
"v_mfma_f32_16x16x32_bf16 v[148:151], v[114:117], v[54:57], v[148:151]\n"
"ds_read_b64_tr_b16 v[106:107], v16 offset:35904\n"
"ds_read_b64_tr_b16 v[108:109], v16 offset:39488\n"
"v_cvt_pk_bf16_f32 v134, v120, v120\n"
"s_waitcnt lgkmcnt(8)\n"
"v_mfma_f32_16x16x32_bf16 v[152:155], v[22:25], v[54:57], v[152:155]\n"
"ds_read_b64_tr_b16 v[110:111], v16 offset:35936\n"
"ds_read_b64_tr_b16 v[112:113], v16 offset:39520\n"
"v_and_b32 v167, 0xffff0000, v134\n"
"v_mfma_f32_16x16x32_bf16 v[156:159], v[26:29], v[54:57], v[156:159]\n"
"ds_read_b64_tr_b16 v[114:115], v16 offset:35968\n"
"ds_read_b64_tr_b16 v[116:117], v16 offset:39552\n"
"s_add_u32 m0, s96, 111104\n"
"s_add_u32 s97, s97, 14336\n"
"s_bfm_b64 exec, 56, 0\n"
"buffer_load_dwordx4 v129, s[28:31], s97 offen lds\n"
"s_mov_b64 exec, -1\n"
"v_sub_f32 v167, v120, v167\n"
"s_waitcnt lgkmcnt(8)\n"
"v_mfma_f32_16x16x32_bf16 v[160:163], v[30:33], v[54:57], v[160:163]\n"
"ds_read_b64_tr_b16 v[22:23], v16 offset:36000\n"
"ds_read_b64_tr_b16 v[24:25], v16 offset:39584\n"
"v_cvt_pk_bf16_f32 v135, v167, v167\n"
"s_waitcnt vmcnt(18)\n"
"v_mfma_f32_16x16x32_bf16 v[136:139], v[34:37], v[58:61], v[136:139]\n"
"ds_read_b64_tr_b16 v[26:27], v18 offset:35840\n"
"ds_read_b64_tr_b16 v[28:29], v18 offset:39424\n"
"v_mul_f32 v165, 0x3d372713, v165\n"
"s_waitcnt lgkmcnt(8)\n"
"v_mfma_f32_16x16x32_bf16 v[140:143], v[102:105], v[58:61], v[140:143]\n"
"ds_read_b64_tr_b16 v[30:31], v16 offset:43008\n"
"ds_read_b64_tr_b16 v[32:33], v16 offset:46592\n"
"v_mul_f32 v118, v165, v119\n"
"v_mfma_f32_16x16x32_bf16 v[144:147], v[106:109], v[58:61], v[144:147]\n"
"ds_read_b64_tr_b16 v[34:35], v16 offset:43040\n"
"ds_read_b64_tr_b16 v[36:37], v16 offset:46624\n"
"v_and_b32 v165, 0xffff0000, v21\n"
"s_waitcnt lgkmcnt(8)\n"
"v_mfma_f32_16x16x32_bf16 v[148:151], v[110:113], v[58:61], v[148:151]\n"
"ds_read_b64_tr_b16 v[102:103], v16 offset:43072\n"
"ds_read_b64_tr_b16 v[104:105], v16 offset:46656\n"
"v_sub_f32 v166, v21, v165\n"
"v_mfma_f32_16x16x32_bf16 v[152:155], v[114:117], v[58:61], v[152:155]\n"
"ds_read_b64_tr_b16 v[106:107], v16 offset:43104\n"
"ds_read_b64_tr_b16 v[108:109], v16 offset:46688\n"
"v_cvt_pk_bf16_f32 v166, v165, v166\n"
"s_waitcnt vmcnt(6)\n"
"v_cmp_ne_u64 vcc, s[60:61], v[244:245]\n"
"s_nop 4\n"
"s_cbranch_vccz L_11_%=\n"
"s_mov_b32 s62, 40000\n"
"buffer_load_dwordx2 v[12:13], v240, s[84:87], s23 offen  sc1\n"
"L_12_%=:\n"
"s_waitcnt vmcnt(0)\n"
"v_cmp_ne_u64 vcc, s[60:61], v[12:13]\n"
"s_nop 4\n"
"s_cbranch_vccz L_11_%=\n"
"buffer_load_dwordx2 v[12:13], v240, s[84:87], s23 offen  sc1\n"
"s_sub_u32 s62, s62, 1\n"
"s_cbranch_scc0 L_12_%=\n"
"L_11_%=:\n"
"s_waitcnt lgkmcnt(8)\n"
"v_mfma_f32_16x16x32_bf16 v[156:159], v[22:25], v[58:61], v[156:159]\n"
"ds_read_b64_tr_b16 v[110:111], v16 offset:43136\n"
"ds_read_b64_tr_b16 v[112:113], v16 offset:46720\n"
"v_cmp_eq_u32 vcc, 1, v1\n"
"s_nop 1\n"
"v_cndmask_b32 v166, 0, v166, vcc\n"
"v_cmp_eq_u32 vcc, 0, v1\n"
"s_nop 1\n"
"v_cndmask_b32 v134, v166, v134, vcc\n"
"v_cndmask_b32 v135, 0, v135, vcc\n"
"buffer_load_dwordx4 v[168:171], v3, s[84:87], s21 offen  sc1\n"
"v_mfma_f32_16x16x32_bf16 v[160:163], v[26:29], v[58:61], v[160:163]\n"
"ds_read_b64_tr_b16 v[114:115], v16 offset:43168\n"
"ds_read_b64_tr_b16 v[116:117], v16 offset:46752\n"
"s_waitcnt lgkmcnt(8)\n"
"v_mfma_f32_16x16x32_bf16 v[136:139], v[30:33], v[62:65], v[136:139]\n"
"ds_read_b64_tr_b16 v[22:23], v18 offset:43008\n"
"ds_read_b64_tr_b16 v[24:25], v18 offset:46592\n"
"v_mfma_f32_16x16x32_bf16 v[140:143], v[34:37], v[62:65], v[140:143]\n"
"ds_read_b64_tr_b16 v[26:27], v16 offset:50176\n"
"ds_read_b64_tr_b16 v[28:29], v16 offset:53760\n"
"buffer_load_dwordx4 v[172:175], v3, s[84:87], s21 offen offset:1024 sc1\n"
"s_waitcnt lgkmcnt(8)\n"
"v_mfma_f32_16x16x32_bf16 v[144:147], v[102:105], v[62:65], v[144:147]\n"
"ds_read_b64_tr_b16 v[30:31], v16 offset:50208\n"
"ds_read_b64_tr_b16 v[32:33], v16 offset:53792\n"
"v_mfma_f32_16x16x32_bf16 v[148:151], v[106:109], v[62:65], v[148:151]\n"
"ds_read_b64_tr_b16 v[34:35], v16 offset:50240\n"
"ds_read_b64_tr_b16 v[36:37], v16 offset:53824\n"
"s_waitcnt lgkmcnt(8)\n"
"v_mfma_f32_16x16x32_bf16 v[152:155], v[110:113], v[62:65], v[152:155]\n"
"ds_read_b64_tr_b16 v[102:103], v16 offset:50272\n"
"ds_read_b64_tr_b16 v[104:105], v16 offset:53856\n"
"buffer_load_dwordx4 v[232:235], v236, s[84:87], s20 offen  sc1\n"
"v_mfma_f32_16x16x32_bf16 v[156:159], v[114:117], v[62:65], v[156:159]\n"
"ds_read_b64_tr_b16 v[106:107], v16 offset:50304\n"
"ds_read_b64_tr_b16 v[108:109], v16 offset:53888\n"
"s_waitcnt lgkmcnt(8)\n"
"v_mfma_f32_16x16x32_bf16 v[160:163], v[22:25], v[62:65], v[160:163]\n"
"ds_read_b64_tr_b16 v[110:111], v16 offset:50336\n"
"ds_read_b64_tr_b16 v[112:113], v16 offset:53920\n"
"v_mfma_f32_16x16x32_bf16 v[136:139], v[26:29], v[66:69], v[136:139]\n"
"ds_read_b64_tr_b16 v[114:115], v18 offset:50176\n"
"ds_read_b64_tr_b16 v[116:117], v18 offset:53760\n"
"buffer_load_dwordx4 v[236:239], v236, s[84:87], s20 offen offset:256 sc1\n"
"s_waitcnt lgkmcnt(8)\n"
"v_mfma_f32_16x16x32_bf16 v[140:143], v[30:33], v[66:69], v[140:143]\n"
"ds_read_b64_tr_b16 v[22:23], v17\n"
"ds_read_b64_tr_b16 v[24:25], v17 offset:3584\n"
"v_mfma_f32_16x16x32_bf16 v[144:147], v[34:37], v[66:69], v[144:147]\n"
"ds_read_b64_tr_b16 v[26:27], v17 offset:32\n"
"ds_read_b64_tr_b16 v[28:29], v17 offset:3616\n"
"s_waitcnt lgkmcnt(8)\n"
"v_mfma_f32_16x16x32_bf16 v[148:151], v[102:105], v[66:69], v[148:151]\n"
"ds_read_b64_tr_b16 v[30:31], v17 offset:64\n"
"ds_read_b64_tr_b16 v[32:33], v17 offset:3648\n"
"s_xor_b32 s22, s21, 2048\n"
"buffer_load_dwordx4 v[176:179], v3, s[84:87], s22 offen  sc1\n"
"v_mfma_f32_16x16x32_bf16 v[152:155], v[106:109], v[66:69], v[152:155]\n"
"ds_read_b64_tr_b16 v[34:35], v17 offset:96\n"
"ds_read_b64_tr_b16 v[36:37], v17 offset:3680\n"
"s_waitcnt lgkmcnt(8)\n"
"v_mfma_f32_16x16x32_bf16 v[156:159], v[110:113], v[66:69], v[156:159]\n"
"ds_read_b64_tr_b16 v[102:103], v17 offset:128\n"
"ds_read_b64_tr_b16 v[104:105], v17 offset:3712\n"
"v_mfma_f32_16x16x32_bf16 v[160:163], v[114:117], v[66:69], v[160:163]\n"
"ds_read_b64_tr_b16 v[106:107], v17 offset:160\n"
"ds_read_b64_tr_b16 v[108:109], v17 offset:3744\n"
"buffer_load_dwordx4 v[180:183], v3, s[84:87], s22 offen offset:1024 sc1\n"
"s_waitcnt lgkmcnt(8)\n"
"v_mfma_f32_16x16x32_bf16 v[136:139], v[22:25], v[70:73], v[136:139]\n"
"ds_read_b64_tr_b16 v[110:111], v19\n"
"ds_read_b64_tr_b16 v[112:113], v19 offset:3584\n"
"v_mfma_f32_16x16x32_bf16 v[140:143], v[26:29], v[70:73], v[140:143]\n"
"ds_read_b64_tr_b16 v[114:115], v17 offset:7168\n"
"ds_read_b64_tr_b16 v[116:117], v17 offset:10752\n"
"s_waitcnt lgkmcnt(8)\n"
"v_mfma_f32_16x16x32_bf16 v[144:147], v[30:33], v[70:73], v[144:147]\n"
"ds_read_b64_tr_b16 v[22:23], v17 offset:7200\n"
"ds_read_b64_tr_b16 v[24:25], v17 offset:10784\n"
"s_xor_b32 s22, s21, 4096\n"
"buffer_load_dwordx4 v[184:187], v3, s[84:87], s22 offen  sc1\n"
"v_mfma_f32_16x16x32_bf16 v[148:151], v[34:37], v[70:73], v[148:151]\n"
"ds_read_b64_tr_b16 v[26:27], v17 offset:7232\n"
"ds_read_b64_tr_b16 v[28:29], v17 offset:10816\n"
"s_waitcnt lgkmcnt(8)\n"
"v_mfma_f32_16x16x32_bf16 v[152:155], v[102:105], v[70:73], v[152:155]\n"
"ds_read_b64_tr_b16 v[30:31], v17 offset:7264\n"
"ds_read_b64_tr_b16 v[32:33], v17 offset:10848\n"
"v_mfma_f32_16x16x32_bf16 v[156:159], v[106:109], v[70:73], v[156:159]\n"
"ds_read_b64_tr_b16 v[34:35], v17 offset:7296\n"
"ds_read_b64_tr_b16 v[36:37], v17 offset:10880\n"
"buffer_load_dwordx4 v[188:191], v3, s[84:87], s22 offen offset:1024 sc1\n"
"s_waitcnt lgkmcnt(8)\n"
"v_mfma_f32_16x16x32_bf16 v[160:163], v[110:113], v[70:73], v[160:163]\n"
"ds_read_b64_tr_b16 v[102:103], v17 offset:7328\n"
"ds_read_b64_tr_b16 v[104:105], v17 offset:10912\n"
"v_mfma_f32_16x16x32_bf16 v[136:139], v[114:117], v[74:77], v[136:139]\n"
"ds_read_b64_tr_b16 v[106:107], v19 offset:7168\n"
"ds_read_b64_tr_b16 v[108:109], v19 offset:10752\n"
"s_waitcnt lgkmcnt(8)\n"
"v_mfma_f32_16x16x32_bf16 v[140:143], v[22:25], v[74:77], v[140:143]\n"
"ds_read_b64_tr_b16 v[110:111], v17 offset:14336\n"
"ds_read_b64_tr_b16 v[112:113], v17 offset:17920\n"
"s_xor_b32 s22, s21, 6144\n"
"buffer_load_dwordx4 v[192:195], v3, s[84:87], s22 offen  sc1\n"
"v_mfma_f32_16x16x32_bf16 v[144:147], v[26:29], v[74:77], v[144:147]\n"
"ds_read_b64_tr_b16 v[114:115], v17 offset:14368\n"
"ds_read_b64_tr_b16 v[116:117], v17 offset:17952\n"
"s_waitcnt lgkmcnt(8)\n"
"v_mfma_f32_16x16x32_bf16 v[148:151], v[30:33], v[74:77], v[148:151]\n"
"ds_read_b64_tr_b16 v[22:23], v17 offset:14400\n"
"ds_read_b64_tr_b16 v[24:25], v17 offset:17984\n"
"v_mfma_f32_16x16x32_bf16 v[152:155], v[34:37], v[74:77], v[152:155]\n"
"ds_read_b64_tr_b16 v[26:27], v17 offset:14432\n"
"ds_read_b64_tr_b16 v[28:29], v17 offset:18016\n"
"buffer_load_dwordx4 v[196:199], v3, s[84:87], s22 offen offset:1024 sc1\n"
"s_waitcnt lgkmcnt(8)\n"
"v_mfma_f32_16x16x32_bf16 v[156:159], v[102:105], v[74:77], v[156:159]\n"
"ds_read_b64_tr_b16 v[30:31], v17 offset:14464\n"
"ds_read_b64_tr_b16 v[32:33], v17 offset:18048\n"
"v_mfma_f32_16x16x32_bf16 v[160:163], v[106:109], v[74:77], v[160:163]\n"
"ds_read_b64_tr_b16 v[34:35], v17 offset:14496\n"
"ds_read_b64_tr_b16 v[36:37], v17 offset:18080\n"
"s_waitcnt lgkmcnt(8)\n"
"v_mfma_f32_16x16x32_bf16 v[136:139], v[110:113], v[78:81], v[136:139]\n"
"ds_read_b64_tr_b16 v[102:103], v19 offset:14336\n"
"ds_read_b64_tr_b16 v[104:105], v19 offset:17920\n"
"s_xor_b32 s22, s21, 16384\n"
"buffer_load_dwordx4 v[200:203], v3, s[84:87], s22 offen  sc1\n"
"v_mfma_f32_16x16x32_bf16 v[140:143], v[114:117], v[78:81], v[140:143]\n"
"ds_read_b64_tr_b16 v[106:107], v17 offset:21504\n"
"ds_read_b64_tr_b16 v[108:109], v17 offset:25088\n"
"s_waitcnt lgkmcnt(8)\n"
"v_mfma_f32_16x16x32_bf16 v[144:147], v[22:25], v[78:81], v[144:147]\n"
"ds_read_b64_tr_b16 v[110:111], v17 offset:21536\n"
"ds_read_b64_tr_b16 v[112:113], v17 offset:25120\n"
"v_mfma_f32_16x16x32_bf16 v[148:151], v[26:29], v[78:81], v[148:151]\n"
"ds_read_b64_tr_b16 v[114:115], v17 offset:21568\n"
"ds_read_b64_tr_b16 v[116:117], v17 offset:25152\n"
"buffer_load_dwordx4 v[204:207], v3, s[84:87], s22 offen offset:1024 sc1\n"
"s_waitcnt lgkmcnt(8)\n"
"v_mfma_f32_16x16x32_bf16 v[152:155], v[30:33], v[78:81], v[152:155]\n"
"ds_read_b64_tr_b16 v[22:23], v17 offset:21600\n"
"ds_read_b64_tr_b16 v[24:25], v17 offset:25184\n"
"v_mfma_f32_16x16x32_bf16 v[156:159], v[34:37], v[78:81], v[156:159]\n"
"ds_read_b64_tr_b16 v[26:27], v17 offset:21632\n"
"ds_read_b64_tr_b16 v[28:29], v17 offset:25216\n"
"s_waitcnt lgkmcnt(8)\n"
"v_mfma_f32_16x16x32_bf16 v[160:163], v[102:105], v[78:81], v[160:163]\n"
"ds_read_b64_tr_b16 v[30:31], v17 offset:21664\n"
"ds_read_b64_tr_b16 v[32:33], v17 offset:25248\n"
"s_xor_b32 s22, s21, 18432\n"
"buffer_load_dwordx4 v[208:211], v3, s[84:87], s22 offen  sc1\n"
"v_mfma_f32_16x16x32_bf16 v[136:139], v[106:109], v[82:85], v[136:139]\n"
"ds_read_b64_tr_b16 v[34:35], v19 offset:21504\n"
"ds_read_b64_tr_b16 v[36:37], v19 offset:25088\n"
"s_waitcnt vmcnt(13)\n"
"s_barrier\n"
"s_waitcnt lgkmcnt(8)\n"
"v_mfma_f32_16x16x32_bf16 v[140:143], v[110:113], v[82:85], v[140:143]\n"
"ds_read_b64_tr_b16 v[102:103], v241 offset:28672\n"
"ds_read_b64_tr_b16 v[104:105], v241 offset:32256\n"
"v_mfma_f32_16x16x32_bf16 v[144:147], v[114:117], v[82:85], v[144:147]\n"
"ds_read_b64_tr_b16 v[106:107], v241 offset:28704\n"
"ds_read_b64_tr_b16 v[108:109], v241 offset:32288\n"
"buffer_load_dwordx4 v[212:215], v3, s[84:87], s22 offen offset:1024 sc1\n"
"s_waitcnt lgkmcnt(8)\n"
"v_mfma_f32_16x16x32_bf16 v[148:151], v[22:25], v[82:85], v[148:151]\n"
"ds_read_b64_tr_b16 v[110:111], v241 offset:28736\n"
"ds_read_b64_tr_b16 v[112:113], v241 offset:32320\n"
"v_mfma_f32_16x16x32_bf16 v[152:155], v[26:29], v[82:85], v[152:155]\n"
"ds_read_b64_tr_b16 v[114:115], v241 offset:28768\n"
"ds_read_b64_tr_b16 v[116:117], v241 offset:32352\n"
"s_waitcnt lgkmcnt(8)\n"
"v_mfma_f32_16x16x32_bf16 v[156:159], v[30:33], v[82:85], v[156:159]\n"
"ds_read_b64_tr_b16 v[22:23], v241 offset:28800\n"
"ds_read_b64_tr_b16 v[24:25], v241 offset:32384\n"
"s_xor_b32 s22, s21, 20480\n"
"buffer_load_dwordx4 v[216:219], v3, s[84:87], s22 offen  sc1\n"
"v_mfma_f32_16x16x32_bf16 v[160:163], v[34:37], v[82:85], v[160:163]\n"
"ds_read_b64_tr_b16 v[26:27], v241 offset:28832\n"
"ds_read_b64_tr_b16 v[28:29], v241 offset:32416\n"
"s_waitcnt lgkmcnt(8)\n"
"v_mfma_f32_16x16x32_bf16 a[196:199], a[228:231], v[102:105], a[196:199]\n"
"ds_read_b64_tr_b16 v[30:31], v242 offset:28672\n"
"ds_read_b64_tr_b16 v[32:33], v242 offset:32256\n"
"v_mfma_f32_16x16x32_bf16 a[200:203], a[228:231], v[106:109], a[200:203]\n"
"ds_read_b64_tr_b16 v[34:35], v17 offset:28672\n"
"ds_read_b64_tr_b16 v[36:37], v17 offset:32256\n"
"buffer_load_dwordx4 v[220:223], v3, s[84:87], s22 offen offset:1024 sc1\n"
"s_waitcnt lgkmcnt(8)\n"
"v_mfma_f32_16x16x32_bf16 a[204:207], a[228:231], v[110:113], a[204:207]\n"
"ds_read_b64_tr_b16 v[102:103], v17 offset:28704\n"
"ds_read_b64_tr_b16 v[104:105], v17 offset:32288\n"
"v_mfma_f32_16x16x32_bf16 a[208:211], a[228:231], v[114:117], a[208:211]\n"
"ds_read_b64_tr_b16 v[106:107], v17 offset:28736\n"
"ds_read_b64_tr_b16 v[108:109], v17 offset:32320\n"
"s_waitcnt lgkmcnt(8)\n"
"v_mfma_f32_16x16x32_bf16 a[212:215], a[228:231], v[22:25], a[212:215]\n"
"ds_read_b64_tr_b16 v[110:111], v17 offset:28768\n"
"ds_read_b64_tr_b16 v[112:113], v17 offset:32352\n"
"s_xor_b32 s22, s21, 22528\n"
"buffer_load_dwordx4 v[224:227], v3, s[84:87], s22 offen  sc1\n"
"v_mfma_f32_16x16x32_bf16 a[216:219], a[228:231], v[26:29], a[216:219]\n"
"ds_read_b64_tr_b16 v[114:115], v17 offset:28800\n"
"ds_read_b64_tr_b16 v[116:117], v17 offset:32384\n"
"s_waitcnt lgkmcnt(8)\n"
"v_mfma_f32_16x16x32_bf16 a[220:223], a[228:231], v[30:33], a[220:223]\n"
"ds_read_b64_tr_b16 v[22:23], v17 offset:28832\n"
"ds_read_b64_tr_b16 v[24:25], v17 offset:32416\n"
"v_mfma_f32_16x16x32_bf16 v[136:139], v[34:37], v[86:89], v[136:139]\n"
"ds_read_b64_tr_b16 v[26:27], v19 offset:28672\n"
"ds_read_b64_tr_b16 v[28:29], v19 offset:32256\n"
"buffer_load_dwordx4 v[228:231], v3, s[84:87], s22 offen offset:1024 sc1\n"
"s_waitcnt lgkmcnt(8)\n"
"v_mfma_f32_16x16x32_bf16 v[140:143], v[102:105], v[86:89], v[140:143]\n"
"ds_read_b64_tr_b16 v[30:31], v17 offset:35840\n"
"ds_read_b64_tr_b16 v[32:33], v17 offset:39424\n"
"s_lshl_b32 s63, s34, 2\n"
"s_add_u32 s63, s63, 161792\n"
"v_lshl_add_u32 v241, v2, 4, s63\n"
"ds_write_b32 v241, a196\n"
"ds_write_b32 v241, a200 offset:256\n"
"ds_write_b32 v241, a204 offset:512\n"
"ds_write_b32 v241, a208 offset:768\n"
"ds_write_b32 v241, a212 offset:1024\n"
"s_waitcnt lgkmcnt(14)\n"
"ds_write_b32 v241, a216 offset:1280\n"
"s_waitcnt lgkmcnt(14)\n"
"ds_write_b32 v241, a220 offset:1536\n"
"v_mfma_f32_16x16x32_bf16 v[144:147], v[106:109], v[86:89], v[144:147]\n"
"s_waitcnt lgkmcnt(14)\n"
"ds_read_b64_tr_b16 v[34:35], v17 offset:35872\n"
"s_waitcnt lgkmcnt(14)\n"
"ds_read_b64_tr_b16 v[36:37], v17 offset:39456\n"
"v_mfma_f32_16x16x32_bf16 v[148:151], v[110:113], v[86:89], v[148:151]\n"
"s_waitcnt lgkmcnt(14)\n"
"ds_read_b64_tr_b16 v[102:103], v17 offset:35904\n"
"s_waitcnt lgkmcnt(14)\n"
"ds_read_b64_tr_b16 v[104:105], v17 offset:39488\n"
"s_add_u32 s11, s14, 28672\n"
"buffer_load_dwordx4 a[116:119], v3, s[52:55], s11 offen offset:1024 sc1\n"
"v_mfma_f32_16x16x32_bf16 v[152:155], v[114:117], v[86:89], v[152:155]\n"
"s_waitcnt lgkmcnt(14)\n"
"ds_read_b64_tr_b16 v[106:107], v17 offset:35936\n"
"s_waitcnt lgkmcnt(14)\n"
"ds_read_b64_tr_b16 v[108:109], v17 offset:39520\n"
"s_waitcnt lgkmcnt(0)\n"
"s_barrier\n"
"v_add_u32 v128, 161792, v3\n"
"v_mul_u32_u24 v240, 128, v1\n"
"v_lshl_add_u32 v240, v2, 2, v240\n"
"v_add_u32 v240, 160256, v240\n"
"ds_read_b128 v[242:245], v128\n"
"ds_read_b128 v[246:249], v128 offset:1024\n"
"s_waitcnt lgkmcnt(1)\n"
"v_pk_add_f32 v[242:243], v[242:243], v[244:245] op_sel:[0,0] op_sel_hi:[1,1]\n"
"v_add_f32 v242, v242, v243\n"
"v_and_b32 v241, 0xffff0000, v242\n"
"v_sub_f32 v243, v242, v241\n"
"v_cvt_pk_bf16_f32 v243, v243, v243\n"
"v_perm_b32 v242, v243, v242, s92\n"
"ds_write_b32 v240, v242\n"
"s_waitcnt lgkmcnt(1)\n"
"v_pk_add_f32 v[246:247], v[246:247], v[248:249] op_sel:[0,0] op_sel_hi:[1,1]\n"
"v_add_f32 v246, v246, v247\n"
"v_and_b32 v241, 0xffff0000, v246\n"
"v_sub_f32 v247, v246, v241\n"
"v_cvt_pk_bf16_f32 v247, v247, v247\n"
"v_perm_b32 v246, v247, v246, s92\n"
"s_bfm_b64 exec, 48, 0\n"
"ds_write_b32 v240, v246 offset:512\n"
"s_mov_b64 exec, -1\n"
"v_mfma_f32_16x16x32_bf16 v[156:159], v[22:25], v[86:89], v[156:159]\n"
"ds_read_b64_tr_b16 v[110:111], v17 offset:35968\n"
"ds_read_b64_tr_b16 v[112:113], v17 offset:39552\n"
"v_mfma_f32_16x16x32_bf16 v[160:163], v[26:29], v[86:89], v[160:163]\n"
"ds_read_b64_tr_b16 v[114:115], v17 offset:36000\n"
"ds_read_b64_tr_b16 v[116:117], v17 offset:39584\n"
"s_add_u32 s8, s15, 28672\n"
"buffer_load_dwordx4 a[120:123], v3, s[52:55], s8 offen  sc1\n"
"v_mfma_f32_16x16x32_bf16 v[136:139], v[30:33], v[90:93], v[136:139]\n"
"ds_read_b64_tr_b16 v[22:23], v19 offset:35840\n"
"ds_read_b64_tr_b16 v[24:25], v19 offset:39424\n"
"v_mfma_f32_16x16x32_bf16 v[140:143], v[34:37], v[90:93], v[140:143]\n"
"ds_read_b64_tr_b16 v[26:27], v17 offset:43008\n"
"ds_read_b64_tr_b16 v[28:29], v17 offset:46592\n"
"v_mfma_f32_16x16x32_bf16 v[144:147], v[102:105], v[90:93], v[144:147]\n"
"ds_read_b64_tr_b16 v[30:31], v17 offset:43040\n"
"ds_read_b64_tr_b16 v[32:33], v17 offset:46624\n"
"s_add_u32 s9, s15, 28672\n"
"buffer_load_dwordx4 a[124:127], v3, s[52:55], s9 offen offset:1024 sc1\n"
"v_mfma_f32_16x16x32_bf16 v[148:151], v[106:109], v[90:93], v[148:151]\n"
"ds_read_b64_tr_b16 v[34:35], v17 offset:43072\n"
"ds_read_b64_tr_b16 v[36:37], v17 offset:46656\n"
"s_waitcnt lgkmcnt(8)\n"
"v_mfma_f32_16x16x32_bf16 v[152:155], v[110:113], v[90:93], v[152:155]\n"
"ds_read_b64_tr_b16 v[102:103], v17 offset:43104\n"
"ds_read_b64_tr_b16 v[104:105], v17 offset:46688\n"
"v_mfma_f32_16x16x32_bf16 v[156:159], v[114:117], v[90:93], v[156:159]\n"
"ds_read_b64_tr_b16 v[106:107], v17 offset:43136\n"
"ds_read_b64_tr_b16 v[108:109], v17 offset:46720\n"
"s_add_u32 s10, s14, 32768\n"
"buffer_load_dwordx4 a[128:131], v3, s[52:55], s10 offen  sc1\n"
"ds_read2_b32 a[242:243], v20 offset0:0 offset1:0\n"
"ds_read2_b32 a[244:245], v20 offset0:32 offset1:32\n"
"ds_read2_b32 a[246:247], v20 offset0:64 offset1:64\n"
"s_waitcnt lgkmcnt(14)\n"
"ds_read2_b32 a[248:249], v20 offset0:96 offset1:96\n"
"s_waitcnt lgkmcnt(14)\n"
"ds_read2_b32 a[250:251], v20 offset0:128 offset1:128\n"
"s_waitcnt lgkmcnt(14)\n"
"ds_read2_b32 a[252:253], v20 offset0:160 offset1:160\n"
"s_waitcnt lgkmcnt(14)\n"
"ds_read2_b32 a[254:255], v20 offset0:192 offset1:192\n"
"v_mfma_f32_16x16x32_bf16 v[160:163], v[22:25], v[90:93], v[160:163]\n"
"s_waitcnt lgkmcnt(14)\n"
"ds_read_b64_tr_b16 v[110:111], v17 offset:43168\n"
"s_waitcnt lgkmcnt(14)\n"
"ds_read_b64_tr_b16 v[112:113], v17 offset:46752\n"
"v_mfma_f32_16x16x32_bf16 v[136:139], v[26:29], v[94:97], v[136:139]\n"
"s_waitcnt lgkmcnt(14)\n"
"ds_read_b64_tr_b16 v[114:115], v19 offset:43008\n"
"s_waitcnt lgkmcnt(14)\n"
"ds_read_b64_tr_b16 v[116:117], v19 offset:46592\n"
"v_mfma_f32_16x16x32_bf16 v[140:143], v[30:33], v[94:97], v[140:143]\n"
"s_waitcnt lgkmcnt(14)\n"
"ds_read_b64_tr_b16 v[22:23], v17 offset:50176\n"
"s_waitcnt lgkmcnt(14)\n"
"ds_read_b64_tr_b16 v[24:25], v17 offset:53760\n"
"s_add_u32 s11, s14, 32768\n"
"buffer_load_dwordx4 a[132:135], v3, s[52:55], s11 offen offset:1024 sc1\n"
"v_mfma_f32_16x16x32_bf16 v[144:147], v[34:37], v[94:97], v[144:147]\n"
"s_waitcnt lgkmcnt(14)\n"
"ds_read_b64_tr_b16 v[26:27], v17 offset:50208\n"
"s_waitcnt lgkmcnt(14)\n"
"ds_read_b64_tr_b16 v[28:29], v17 offset:53792\n"
"v_mfma_f32_16x16x32_bf16 v[148:151], v[102:105], v[94:97], v[148:151]\n"
"s_waitcnt lgkmcnt(14)\n"
"ds_read_b64_tr_b16 v[30:31], v17 offset:50240\n"
"s_waitcnt lgkmcnt(14)\n"
"ds_read_b64_tr_b16 v[32:33], v17 offset:53824\n"
"v_mfma_f32_16x16x32_bf16 v[152:155], v[106:109], v[94:97], v[152:155]\n"
"s_waitcnt lgkmcnt(14)\n"
"ds_read_b64_tr_b16 v[34:35], v17 offset:50272\n"
"s_waitcnt lgkmcnt(14)\n"
"ds_read_b64_tr_b16 v[36:37], v17 offset:53856\n"
"s_add_u32 s8, s15, 32768\n"
"buffer_load_dwordx4 a[136:139], v3, s[52:55], s8 offen  sc1\n"
"s_waitcnt lgkmcnt(8)\n"
"v_mfma_f32_16x16x32_bf16 v[156:159], v[110:113], v[94:97], v[156:159]\n"
"ds_read_b64_tr_b16 v[102:103], v17 offset:50304\n"
"ds_read_b64_tr_b16 v[104:105], v17 offset:53888\n"
"v_mfma_f32_16x16x16_bf16 v[136:139], a[242:243], v[134:135], v[136:139]\n"
"v_mfma_f32_16x16x16_bf16 v[140:143], a[244:245], v[134:135], v[140:143]\n"
"v_mfma_f32_16x16x16_bf16 v[144:147], a[246:247], v[134:135], v[144:147]\n"
"v_mfma_f32_16x16x16_bf16 v[148:151], a[248:249], v[134:135], v[148:151]\n"
"v_mfma_f32_16x16x16_bf16 v[152:155], a[250:251], v[134:135], v[152:155]\n"
"v_mfma_f32_16x16x16_bf16 v[156:159], a[252:253], v[134:135], v[156:159]\n"
"v_mfma_f32_16x16x16_bf16 v[160:163], a[254:255], v[134:135], v[160:163]\n"
"s_nop 5\n"
"v_mfma_f32_16x16x32_bf16 v[160:163], v[114:117], v[94:97], v[160:163]\n"
"ds_read_b64_tr_b16 v[106:107], v17 offset:50336\n"
"ds_read_b64_tr_b16 v[108:109], v17 offset:53920\n"
"s_waitcnt lgkmcnt(8)\n"
"v_mfma_f32_16x16x32_bf16 v[136:139], v[22:25], v[98:101], v[136:139]\n"
"ds_read_b64_tr_b16 v[110:111], v19 offset:50176\n"
"ds_read_b64_tr_b16 v[112:113], v19 offset:53760\n"
"v_mfma_f32_16x16x32_bf16 v[140:143], v[26:29], v[98:101], v[140:143]\n"
"s_waitcnt lgkmcnt(6)\n"
"v_mfma_f32_16x16x32_bf16 v[144:147], v[30:33], v[98:101], v[144:147]\n"
"v_mfma_f32_16x16x32_bf16 v[148:151], v[34:37], v[98:101], v[148:151]\n"
"s_waitcnt lgkmcnt(2)\n"
"v_mfma_f32_16x16x32_bf16 v[152:155], v[102:105], v[98:101], v[152:155]\n"
"v_and_b32 v20, 48, v3\n"
"v_mul_u32_u24 v20, 192, v20\n"
"v_and_b32 v128, 64, v3\n"
"v_add_u32 v20, v128, v20\n"
"v_lshrrev_b32 v128, 7, v3\n"
"v_lshl_add_u32 v20, v128, 10, v20\n"
"s_sub_u32 s95, 384, s16\n"
"s_and_b32 s95, s95, 1020\n"
"s_lshr_b32 s23, %2, 3\n"
"s_lshl2_add_u32 s23, s23, s34\n"
"s_mul_i32 s23, s23, 12288\n"
"s_add_u32 s23, s23, s95\n"
"s_bfm_b64 exec, 24, 0\n"
"buffer_load_dword v12, v20, s[16:19], s23 offen offset:1024\n"
"s_mov_b64 exec, -1\n"
"v_mfma_f32_16x16x32_bf16 v[156:159], v[106:109], v[98:101], v[156:159]\n"
"s_waitcnt lgkmcnt(0)\n"
"v_mfma_f32_16x16x32_bf16 v[160:163], v[110:113], v[98:101], v[160:163]\n"
"ds_read_b64_tr_b16 v[22:23], v16\n"
"ds_read_b64_tr_b16 v[24:25], v16 offset:3584\n"
"ds_read_b64_tr_b16 v[26:27], v16 offset:32\n"
"ds_read_b64_tr_b16 v[28:29], v16 offset:3616\n"
"ds_read_b64_tr_b16 v[30:31], v16 offset:64\n"
"ds_read_b64_tr_b16 v[32:33], v16 offset:3648\n"
"ds_read_b64_tr_b16 v[34:35], v16 offset:96\n"
"ds_read_b64_tr_b16 v[36:37], v16 offset:3680\n"
"ds_read_b64_tr_b16 v[102:103], v16 offset:7168\n"
"ds_read_b64_tr_b16 v[104:105], v16 offset:10752\n"
"ds_read_b64_tr_b16 v[106:107], v16 offset:7200\n"
"ds_read_b64_tr_b16 v[108:109], v16 offset:10784\n"
"v_pk_mul_f32 v[164:165], v[136:137], v[136:137] op_sel:[0,0] op_sel_hi:[1,1]\n"
"v_pk_mul_f32 v[166:167], v[138:139], v[138:139] op_sel:[0,0] op_sel_hi:[1,1]\n"
"v_pk_mul_f32 v[120:121], v[140:141], v[140:141] op_sel:[0,0] op_sel_hi:[1,1]\n"
"v_pk_mul_f32 v[122:123], v[142:143], v[142:143] op_sel:[0,0] op_sel_hi:[1,1]\n"
"v_pk_mul_f32 v[124:125], v[144:145], v[144:145] op_sel:[0,0] op_sel_hi:[1,1]\n"
"v_pk_mul_f32 v[126:127], v[146:147], v[146:147] op_sel:[0,0] op_sel_hi:[1,1]\n"
"v_pk_mul_f32 v[240:241], v[148:149], v[148:149] op_sel:[0,0] op_sel_hi:[1,1]\n"
"v_pk_mul_f32 v[242:243], v[150:151], v[150:151] op_sel:[0,0] op_sel_hi:[1,1]\n"
"v_pk_fma_f32 v[164:165], v[164:165], v[118:119], v[118:119] op_sel:[0,0,1] op_sel_hi:[1,0,1]\n"
"v_pk_fma_f32 v[166:167], v[166:167], v[118:119], v[118:119] op_sel:[0,0,1] op_sel_hi:[1,0,1]\n"
"v_pk_fma_f32 v[120:121], v[120:121], v[118:119], v[118:119] op_sel:[0,0,1] op_sel_hi:[1,0,1]\n"
"v_pk_fma_f32 v[122:123], v[122:123], v[118:119], v[118:119] op_sel:[0,0,1] op_sel_hi:[1,0,1]\n"
"v_pk_fma_f32 v[124:125], v[124:125], v[118:119], v[118:119] op_sel:[0,0,1] op_sel_hi:[1,0,1]\n"
"v_pk_fma_f32 v[126:127], v[126:127], v[118:119], v[118:119] op_sel:[0,0,1] op_sel_hi:[1,0,1]\n"
"v_pk_fma_f32 v[240:241], v[240:241], v[118:119], v[118:119] op_sel:[0,0,1] op_sel_hi:[1,0,1]\n"
"v_pk_fma_f32 v[242:243], v[242:243], v[118:119], v[118:119] op_sel:[0,0,1] op_sel_hi:[1,0,1]\n"
"v_pk_mul_f32 v[164:165], v[164:165], v[136:137] op_sel:[0,0] op_sel_hi:[1,1]\n"
"v_pk_mul_f32 v[166:167], v[166:167], v[138:139] op_sel:[0,0] op_sel_hi:[1,1]\n"
"v_pk_mul_f32 v[120:121], v[120:121], v[140:141] op_sel:[0,0] op_sel_hi:[1,1]\n"
"v_pk_mul_f32 v[122:123], v[122:123], v[142:143] op_sel:[0,0] op_sel_hi:[1,1]\n"
"v_pk_mul_f32 v[124:125], v[124:125], v[144:145] op_sel:[0,0] op_sel_hi:[1,1]\n"
"v_pk_mul_f32 v[126:127], v[126:127], v[146:147] op_sel:[0,0] op_sel_hi:[1,1]\n"
"v_pk_mul_f32 v[240:241], v[240:241], v[148:149] op_sel:[0,0] op_sel_hi:[1,1]\n"
"v_pk_mul_f32 v[242:243], v[242:243], v[150:151] op_sel:[0,0] op_sel_hi:[1,1]\n"
"v_exp_f32 v164, v164\n"
"v_exp_f32 v165, v165\n"
"v_exp_f32 v166, v166\n"
"v_exp_f32 v167, v167\n"
"v_exp_f32 v120, v120\n"
"v_exp_f32 v121, v121\n"
"v_exp_f32 v122, v122\n"
"v_exp_f32 v123, v123\n"
"v_exp_f32 v124, v124\n"
"v_exp_f32 v125, v125\n"
"v_exp_f32 v126, v126\n"
"v_exp_f32 v127, v127\n"
"v_exp_f32 v240, v240\n"
"v_exp_f32 v241, v241\n"
"v_exp_f32 v242, v242\n"
"v_exp_f32 v243, v243\n"
"v_pk_fma_f32 v[164:165], v[164:165], v[20:21], v[20:21] op_sel:[0,1,1] op_sel_hi:[1,1,1]\n"
"v_pk_fma_f32 v[166:167], v[166:167], v[20:21], v[20:21] op_sel:[0,1,1] op_sel_hi:[1,1,1]\n"
"v_pk_fma_f32 v[120:121], v[120:121], v[20:21], v[20:21] op_sel:[0,1,1] op_sel_hi:[1,1,1]\n"
"v_pk_fma_f32 v[122:123], v[122:123], v[20:21], v[20:21] op_sel:[0,1,1] op_sel_hi:[1,1,1]\n"
"v_pk_fma_f32 v[124:125], v[124:125], v[20:21], v[20:21] op_sel:[0,1,1] op_sel_hi:[1,1,1]\n"
"v_pk_fma_f32 v[126:127], v[126:127], v[20:21], v[20:21] op_sel:[0,1,1] op_sel_hi:[1,1,1]\n"
"v_pk_fma_f32 v[240:241], v[240:241], v[20:21], v[20:21] op_sel:[0,1,1] op_sel_hi:[1,1,1]\n"
"v_pk_fma_f32 v[242:243], v[242:243], v[20:21], v[20:21] op_sel:[0,1,1] op_sel_hi:[1,1,1]\n"
"v_rcp_f32 v164, v164\n"
"v_rcp_f32 v165, v165\n"
"v_rcp_f32 v166, v166\n"
"v_rcp_f32 v167, v167\n"
"v_rcp_f32 v120, v120\n"
"v_rcp_f32 v121, v121\n"
"v_rcp_f32 v122, v122\n"
"v_rcp_f32 v123, v123\n"
"v_rcp_f32 v124, v124\n"
"v_rcp_f32 v125, v125\n"
"v_rcp_f32 v126, v126\n"
"v_rcp_f32 v127, v127\n"
"v_rcp_f32 v240, v240\n"
"v_rcp_f32 v241, v241\n"
"v_rcp_f32 v242, v242\n"
"v_rcp_f32 v243, v243\n"
"v_pk_mul_f32 v[136:137], v[136:137], v[164:165] op_sel:[0,0] op_sel_hi:[1,1]\n"
"v_pk_mul_f32 v[138:139], v[138:139], v[166:167] op_sel:[0,0] op_sel_hi:[1,1]\n"
"v_pk_mul_f32 v[140:141], v[140:141], v[120:121] op_sel:[0,0] op_sel_hi:[1,1]\n"
"v_pk_mul_f32 v[142:143], v[142:143], v[122:123] op_sel:[0,0] op_sel_hi:[1,1]\n"
"v_pk_mul_f32 v[144:145], v[144:145], v[124:125] op_sel:[0,0] op_sel_hi:[1,1]\n"
"v_pk_mul_f32 v[146:147], v[146:147], v[126:127] op_sel:[0,0] op_sel_hi:[1,1]\n"
"v_pk_mul_f32 v[148:149], v[148:149], v[240:241] op_sel:[0,0] op_sel_hi:[1,1]\n"
"v_pk_mul_f32 v[150:151], v[150:151], v[242:243] op_sel:[0,0] op_sel_hi:[1,1]\n"
"v_cvt_pk_bf16_f32 v136, v136, v137\n"
"v_cvt_pk_bf16_f32 v137, v138, v139\n"
"v_cvt_pk_bf16_f32 v138, v140, v141\n"
"v_cvt_pk_bf16_f32 v139, v142, v143\n"
"buffer_store_dwordx4 v[136:139], v3, s[84:87], s98 offen  sc1\n"
"v_cvt_pk_bf16_f32 v144, v144, v145\n"
"v_cvt_pk_bf16_f32 v145, v146, v147\n"
"v_cvt_pk_bf16_f32 v146, v148, v149\n"
"v_cvt_pk_bf16_f32 v147, v150, v151\n"
"buffer_store_dwordx4 v[144:147], v3, s[84:87], s98 offen offset:1024 sc1\n"
"v_pk_mul_f32 v[164:165], v[152:153], v[152:153] op_sel:[0,0] op_sel_hi:[1,1]\n"
"v_pk_mul_f32 v[166:167], v[154:155], v[154:155] op_sel:[0,0] op_sel_hi:[1,1]\n"
"v_pk_mul_f32 v[120:121], v[156:157], v[156:157] op_sel:[0,0] op_sel_hi:[1,1]\n"
"v_pk_mul_f32 v[122:123], v[158:159], v[158:159] op_sel:[0,0] op_sel_hi:[1,1]\n"
"v_pk_mul_f32 v[124:125], v[160:161], v[160:161] op_sel:[0,0] op_sel_hi:[1,1]\n"
"v_pk_mul_f32 v[126:127], v[162:163], v[162:163] op_sel:[0,0] op_sel_hi:[1,1]\n"
"v_pk_fma_f32 v[164:165], v[164:165], v[118:119], v[118:119] op_sel:[0,0,1] op_sel_hi:[1,0,1]\n"
"v_pk_fma_f32 v[166:167], v[166:167], v[118:119], v[118:119] op_sel:[0,0,1] op_sel_hi:[1,0,1]\n"
"v_pk_fma_f32 v[120:121], v[120:121], v[118:119], v[118:119] op_sel:[0,0,1] op_sel_hi:[1,0,1]\n"
"v_pk_fma_f32 v[122:123], v[122:123], v[118:119], v[118:119] op_sel:[0,0,1] op_sel_hi:[1,0,1]\n"
"v_pk_fma_f32 v[124:125], v[124:125], v[118:119], v[118:119] op_sel:[0,0,1] op_sel_hi:[1,0,1]\n"
"v_pk_fma_f32 v[126:127], v[126:127], v[118:119], v[118:119] op_sel:[0,0,1] op_sel_hi:[1,0,1]\n"
"v_pk_mul_f32 v[164:165], v[164:165], v[152:153] op_sel:[0,0] op_sel_hi:[1,1]\n"
"v_pk_mul_f32 v[166:167], v[166:167], v[154:155] op_sel:[0,0] op_sel_hi:[1,1]\n"
"v_pk_mul_f32 v[120:121], v[120:121], v[156:157] op_sel:[0,0] op_sel_hi:[1,1]\n"
"v_pk_mul_f32 v[122:123], v[122:123], v[158:159] op_sel:[0,0] op_sel_hi:[1,1]\n"
"v_pk_mul_f32 v[124:125], v[124:125], v[160:161] op_sel:[0,0] op_sel_hi:[1,1]\n"
"v_pk_mul_f32 v[126:127], v[126:127], v[162:163] op_sel:[0,0] op_sel_hi:[1,1]\n"
"L_pad6_%=:\n"
"s_cbranch_execz L_ret_%=\n"
"v_exp_f32 v164, v164\n"
"v_exp_f32 v165, v165\n"
"v_exp_f32 v166, v166\n"
"v_exp_f32 v167, v167\n"
"v_exp_f32 v120, v120\n"
"v_exp_f32 v121, v121\n"
"v_exp_f32 v122, v122\n"
"v_exp_f32 v123, v123\n"
"v_exp_f32 v124, v124\n"
"v_exp_f32 v125, v125\n"
"v_exp_f32 v126, v126\n"
"v_exp_f32 v127, v127\n"
"v_pk_fma_f32 v[164:165], v[164:165], v[20:21], v[20:21] op_sel:[0,1,1] op_sel_hi:[1,1,1]\n"
"v_pk_fma_f32 v[166:167], v[166:167], v[20:21], v[20:21] op_sel:[0,1,1] op_sel_hi:[1,1,1]\n"
"v_pk_fma_f32 v[120:121], v[120:121], v[20:21], v[20:21] op_sel:[0,1,1] op_sel_hi:[1,1,1]\n"
"v_pk_fma_f32 v[122:123], v[122:123], v[20:21], v[20:21] op_sel:[0,1,1] op_sel_hi:[1,1,1]\n"
"v_pk_fma_f32 v[124:125], v[124:125], v[20:21], v[20:21] op_sel:[0,1,1] op_sel_hi:[1,1,1]\n"
"v_pk_fma_f32 v[126:127], v[126:127], v[20:21], v[20:21] op_sel:[0,1,1] op_sel_hi:[1,1,1]\n"
"v_rcp_f32 v164, v164\n"
"v_rcp_f32 v165, v165\n"
"v_rcp_f32 v166, v166\n"
"v_rcp_f32 v167, v167\n"
"v_rcp_f32 v120, v120\n"
"v_rcp_f32 v121, v121\n"
"v_rcp_f32 v122, v122\n"
"v_rcp_f32 v123, v123\n"
"v_rcp_f32 v124, v124\n"
"v_rcp_f32 v125, v125\n"
"v_rcp_f32 v126, v126\n"
"v_rcp_f32 v127, v127\n"
"v_pk_mul_f32 v[152:153], v[152:153], v[164:165] op_sel:[0,0] op_sel_hi:[1,1]\n"
"v_pk_mul_f32 v[154:155], v[154:155], v[166:167] op_sel:[0,0] op_sel_hi:[1,1]\n"
"v_pk_mul_f32 v[156:157], v[156:157], v[120:121] op_sel:[0,0] op_sel_hi:[1,1]\n"
"v_pk_mul_f32 v[158:159], v[158:159], v[122:123] op_sel:[0,0] op_sel_hi:[1,1]\n"
"v_pk_mul_f32 v[160:161], v[160:161], v[124:125] op_sel:[0,0] op_sel_hi:[1,1]\n"
"v_pk_mul_f32 v[162:163], v[162:163], v[126:127] op_sel:[0,0] op_sel_hi:[1,1]\n"
"v_cvt_pk_bf16_f32 v152, v152, v153\n"
"v_cvt_pk_bf16_f32 v153, v154, v155\n"
"v_cvt_pk_bf16_f32 v154, v156, v157\n"
"v_cvt_pk_bf16_f32 v155, v158, v159\n"
"buffer_store_dwordx4 v[152:155], v3, s[84:87], s98 offen offset:2048 sc1\n"
"v_cvt_pk_bf16_f32 v160, v160, v161\n"
"v_cvt_pk_bf16_f32 v161, v162, v163\n"
"buffer_store_dwordx2 v[160:161], v3, s[84:87], s99 offen offset:3072 sc1\n"
"s_waitcnt lgkmcnt(8) vmcnt(28)\n"
"v_mfma_f32_16x16x32_bf16 v[164:167], v[22:25], v[168:171], 0\n"
"ds_read_b64_tr_b16 v[110:111], v16 offset:7232\n"
"ds_read_b64_tr_b16 v[112:113], v16 offset:10816\n"
"s_waitcnt vmcnt(26)\n"
"v_add_f32 v232, v232, v234\n"
"s_add_u32 s9, s15, 32768\n"
"buffer_load_dwordx4 a[140:143], v3, s[52:55], s9 offen offset:1024 sc1\n"
"v_mfma_f32_16x16x32_bf16 v[120:123], v[26:29], v[168:171], 0\n"
"ds_read_b64_tr_b16 v[114:115], v16 offset:7264\n"
"ds_read_b64_tr_b16 v[116:117], v16 offset:10848\n"
"v_add_f32 v233, v233, v235\n"
"s_waitcnt lgkmcnt(8)\n"
"v_mfma_f32_16x16x32_bf16 v[124:127], v[30:33], v[168:171], 0\n"
"ds_read_b64_tr_b16 v[22:23], v16 offset:14336\n"
"ds_read_b64_tr_b16 v[24:25], v16 offset:17920\n"
"s_waitcnt vmcnt(26)\n"
"v_add_f32 v236, v236, v238\n"
"v_mfma_f32_16x16x32_bf16 v[240:243], v[34:37], v[168:171], 0\n"
"ds_read_b64_tr_b16 v[26:27], v16 offset:14368\n"
"ds_read_b64_tr_b16 v[28:29], v16 offset:17952\n"
"v_add_f32 v237, v237, v239\n"
"s_add_u32 s10, s14, 36864\n"
"buffer_load_dwordx4 a[144:147], v3, s[52:55], s10 offen  sc1\n"
"s_waitcnt lgkmcnt(8)\n"
"v_mfma_f32_16x16x32_bf16 v[164:167], v[102:105], v[172:175], v[164:167]\n"
"ds_read_b64_tr_b16 v[30:31], v16 offset:14400\n"
"ds_read_b64_tr_b16 v[32:33], v16 offset:17984\n"
"v_add_f32 v232, v232, v236\n"
"v_mfma_f32_16x16x32_bf16 v[120:123], v[106:109], v[172:175], v[120:123]\n"
"ds_read_b64_tr_b16 v[34:35], v16 offset:14432\n"
"ds_read_b64_tr_b16 v[36:37], v16 offset:18016\n"
"v_add_f32 v233, v233, v237\n"
"s_waitcnt lgkmcnt(8)\n"
"v_mfma_f32_16x16x32_bf16 v[124:127], v[110:113], v[172:175], v[124:127]\n"
"ds_read_b64_tr_b16 v[102:103], v16 offset:21504\n"
"ds_read_b64_tr_b16 v[104:105], v16 offset:25088\n"
"v_permlane32_swap_b32 v232, v233\n"
"s_add_u32 s11, s14, 36864\n"
"buffer_load_dwordx4 a[148:151], v3, s[52:55], s11 offen offset:1024 sc1\n"
"v_mfma_f32_16x16x32_bf16 v[240:243], v[114:117], v[172:175], v[240:243]\n"
"ds_read_b64_tr_b16 v[106:107], v16 offset:21536\n"
"ds_read_b64_tr_b16 v[108:109], v16 offset:25120\n"
"v_add_f32 v232, v232, v233\n"
"s_waitcnt lgkmcnt(8) vmcnt(27)\n"
"v_mfma_f32_16x16x32_bf16 v[164:167], v[22:25], v[176:179], v[164:167]\n"
"ds_read_b64_tr_b16 v[110:111], v16 offset:21568\n"
"ds_read_b64_tr_b16 v[112:113], v16 offset:25152\n"
"v_mov_b32 v233, v232\n"
"v_mfma_f32_16x16x32_bf16 v[120:123], v[26:29], v[176:179], v[120:123]\n"
"ds_read_b64_tr_b16 v[114:115], v16 offset:21600\n"
"ds_read_b64_tr_b16 v[116:117], v16 offset:25184\n"
"v_permlane16_swap_b32 v232, v233\n"
"s_add_u32 s8, s15, 36864\n"
"buffer_load_dwordx4 a[152:155], v3, s[52:55], s8 offen  sc1\n"
"s_waitcnt lgkmcnt(8)\n"
"v_mfma_f32_16x16x32_bf16 v[124:127], v[30:33], v[176:179], v[124:127]\n"
"ds_read_b64_tr_b16 v[22:23], v16 offset:28672\n"
"ds_read_b64_tr_b16 v[24:25], v16 offset:32256\n"
"v_add_f32 v232, v232, v233\n"
"v_mfma_f32_16x16x32_bf16 v[240:243], v[34:37], v[176:179], v[240:243]\n"
"ds_read_b64_tr_b16 v[26:27], v16 offset:28704\n"
"ds_read_b64_tr_b16 v[28:29], v16 offset:32288\n"
"v_mov_b32 v233, v232\n"
"s_waitcnt lgkmcnt(8) vmcnt(27)\n"
"v_mfma_f32_16x16x32_bf16 v[164:167], v[102:105], v[180:183], v[164:167]\n"
"ds_read_b64_tr_b16 v[30:31], v16 offset:28736\n"
"ds_read_b64_tr_b16 v[32:33], v16 offset:32320\n"
"v_permlane32_swap_b32 v232, v233\n"
"s_add_u32 s9, s15, 36864\n"
"buffer_load_dwordx4 a[156:159], v3, s[52:55], s9 offen offset:1024 sc1\n"
"v_mfma_f32_16x16x32_bf16 v[120:123], v[106:109], v[180:183], v[120:123]\n"
"ds_read_b64_tr_b16 v[34:35], v16 offset:28768\n"
"ds_read_b64_tr_b16 v[36:37], v16 offset:32352\n"
"v_mul_f32 v232, 0x3b000000, v232\n"
"s_waitcnt lgkmcnt(8)\n"
"v_mfma_f32_16x16x32_bf16 v[124:127], v[110:113], v[180:183], v[124:127]\n"
"ds_read_b64_tr_b16 v[102:103], v16 offset:35840\n"
"ds_read_b64_tr_b16 v[104:105], v16 offset:39424\n"
"v_mul_f32 v233, 0x3b000000, v233\n"
"v_mfma_f32_16x16x32_bf16 v[240:243], v[114:117], v[180:183], v[240:243]\n"
"ds_read_b64_tr_b16 v[106:107], v16 offset:35872\n"
"ds_read_b64_tr_b16 v[108:109], v16 offset:39456\n"
"v_fma_f32 v233, -v232, v232, v233\n"
"s_add_u32 s10, s14, 40960\n"
"buffer_load_dwordx4 a[160:163], v3, s[52:55], s10 offen  sc1\n"
"s_waitcnt lgkmcnt(8) vmcnt(28)\n"
"v_mfma_f32_16x16x32_bf16 v[164:167], v[22:25], v[184:187], v[164:167]\n"
"ds_read_b64_tr_b16 v[110:111], v16 offset:35904\n"
"ds_read_b64_tr_b16 v[112:113], v16 offset:39488\n"
"v_max_f32 v233, 0, v233\n"
"v_mfma_f32_16x16x32_bf16 v[120:123], v[26:29], v[184:187], v[120:123]\n"
"ds_read_b64_tr_b16 v[114:115], v16 offset:35936\n"
"ds_read_b64_tr_b16 v[116:117], v16 offset:39520\n"
"v_add_f32 v20, 0x3727c5ac, v233\n"
"s_waitcnt lgkmcnt(8)\n"
"v_mfma_f32_16x16x32_bf16 v[124:127], v[30:33], v[184:187], v[124:127]\n"
"ds_read_b64_tr_b16 v[22:23], v16 offset:43008\n"
"ds_read_b64_tr_b16 v[24:25], v16 offset:46592\n"
"v_rsq_f32 v20, v20\n"
"s_add_u32 s11, s14, 40960\n"
"buffer_load_dwordx4 a[164:167], v3, s[52:55], s11 offen offset:1024 sc1\n"
"v_mfma_f32_16x16x32_bf16 v[240:243], v[34:37], v[184:187], v[240:243]\n"
"ds_read_b64_tr_b16 v[26:27], v16 offset:43040\n"
"ds_read_b64_tr_b16 v[28:29], v16 offset:46624\n"
"v_mul_f32 v233, v233, v20\n"
"s_waitcnt lgkmcnt(8) vmcnt(28)\n"
"v_mfma_f32_16x16x32_bf16 v[164:167], v[102:105], v[188:191], v[164:167]\n"
"ds_read_b64_tr_b16 v[30:31], v16 offset:43072\n"
"ds_read_b64_tr_b16 v[32:33], v16 offset:46656\n"
"v_fmaak_f32 v233, v233, v20, 0x3727c5ac\n"
"v_mfma_f32_16x16x32_bf16 v[120:123], v[106:109], v[188:191], v[120:123]\n"
"ds_read_b64_tr_b16 v[34:35], v16 offset:43104\n"
"ds_read_b64_tr_b16 v[36:37], v16 offset:46688\n"
"v_rsq_f32 v233, v233\n"
"s_add_u32 s8, s15, 40960\n"
"buffer_load_dwordx4 a[168:171], v3, s[52:55], s8 offen  sc1\n"
"s_waitcnt lgkmcnt(8)\n"
"v_mfma_f32_16x16x32_bf16 v[124:127], v[110:113], v[188:191], v[124:127]\n"
"ds_read_b64_tr_b16 v[102:103], v16 offset:50176\n"
"ds_read_b64_tr_b16 v[104:105], v16 offset:53760\n"
"v_mul_f32 v233, v233, v20\n"
"v_mfma_f32_16x16x32_bf16 v[240:243], v[114:117], v[188:191], v[240:243]\n"
"ds_read_b64_tr_b16 v[106:107], v16 offset:50208\n"
"ds_read_b64_tr_b16 v[108:109], v16 offset:53792\n"
"v_rcp_f32 v132, v233\n"
"s_waitcnt lgkmcnt(8) vmcnt(28)\n"
"v_mfma_f32_16x16x32_bf16 v[164:167], v[22:25], v[192:195], v[164:167]\n"
"ds_read_b64_tr_b16 v[110:111], v16 offset:50240\n"
"ds_read_b64_tr_b16 v[112:113], v16 offset:53824\n"
"v_mul_f32 v232, -1.0, v232\n"
"s_add_u32 s9, s15, 40960\n"
"buffer_load_dwordx4 a[172:175], v3, s[52:55], s9 offen offset:1024 sc1\n"
"v_mfma_f32_16x16x32_bf16 v[120:123], v[26:29], v[192:195], v[120:123]\n"
"ds_read_b64_tr_b16 v[114:115], v16 offset:50272\n"
"ds_read_b64_tr_b16 v[116:117], v16 offset:53856\n"
"v_mul_f32 v131, 0xc0135761, v233\n"
"s_waitcnt lgkmcnt(8)\n"
"v_mfma_f32_16x16x32_bf16 v[124:127], v[30:33], v[192:195], v[124:127]\n"
"ds_read_b64_tr_b16 v[22:23], v17\n"
"ds_read_b64_tr_b16 v[24:25], v17 offset:3584\n"
"v_mul_f32 v128, v233, v233\n"
"v_mfma_f32_16x16x32_bf16 v[240:243], v[34:37], v[192:195], v[240:243]\n"
"ds_read_b64_tr_b16 v[26:27], v17 offset:32\n"
"ds_read_b64_tr_b16 v[28:29], v17 offset:3616\n"
"v_cvt_pk_bf16_f32 v134, v232, v232\n"
"s_add_u32 s10, s14, 45056\n"
"buffer_load_dwordx4 a[176:179], v3, s[52:55], s10 offen  sc1\n"
"s_waitcnt lgkmcnt(8) vmcnt(29)\n"
"v_mfma_f32_16x16x32_bf16 v[164:167], v[102:105], v[196:199], v[164:167]\n"
"ds_read_b64_tr_b16 v[30:31], v17 offset:64\n"
"ds_read_b64_tr_b16 v[32:33], v17 offset:3648\n"
"v_and_b32 v252, 0xffff0000, v134\n"
"v_mfma_f32_16x16x32_bf16 v[120:123], v[106:109], v[196:199], v[120:123]\n"
"ds_read_b64_tr_b16 v[34:35], v17 offset:96\n"
"ds_read_b64_tr_b16 v[36:37], v17 offset:3680\n"
"v_sub_f32 v252, v232, v252\n"
"s_waitcnt lgkmcnt(8)\n"
"v_mfma_f32_16x16x32_bf16 v[124:127], v[110:113], v[196:199], v[124:127]\n"
"ds_read_b64_tr_b16 v[102:103], v17 offset:7168\n"
"ds_read_b64_tr_b16 v[104:105], v17 offset:10752\n"
"v_cvt_pk_bf16_f32 v135, v252, v252\n"
"s_add_u32 s11, s14, 45056\n"
"buffer_load_dwordx4 a[180:183], v3, s[52:55], s11 offen offset:1024 sc1\n"
"v_mfma_f32_16x16x32_bf16 v[240:243], v[114:117], v[196:199], v[240:243]\n"
"ds_read_b64_tr_b16 v[106:107], v17 offset:7200\n"
"ds_read_b64_tr_b16 v[108:109], v17 offset:10784\n"
"v_mul_f32 v128, 0x3d372713, v128\n"
"s_waitcnt lgkmcnt(8) vmcnt(29)\n"
"v_mfma_f32_16x16x32_bf16 v[164:167], v[22:25], v[200:203], v[164:167]\n"
"ds_read_b64_tr_b16 v[110:111], v17 offset:7232\n"
"ds_read_b64_tr_b16 v[112:113], v17 offset:10816\n"
"v_mul_f32 v130, v128, v131\n"
"v_mfma_f32_16x16x32_bf16 v[120:123], v[26:29], v[200:203], v[120:123]\n"
"ds_read_b64_tr_b16 v[114:115], v17 offset:7264\n"
"ds_read_b64_tr_b16 v[116:117], v17 offset:10848\n"
"v_and_b32 v128, 0xffff0000, v132\n"
"s_add_u32 s8, s15, 45056\n"
"buffer_load_dwordx4 a[184:187], v3, s[52:55], s8 offen  sc1\n"
"s_waitcnt lgkmcnt(8)\n"
"v_mfma_f32_16x16x32_bf16 v[124:127], v[30:33], v[200:203], v[124:127]\n"
"ds_read_b64_tr_b16 v[22:23], v17 offset:14336\n"
"ds_read_b64_tr_b16 v[24:25], v17 offset:17920\n"
"v_sub_f32 v129, v132, v128\n"
"v_mfma_f32_16x16x32_bf16 v[240:243], v[34:37], v[200:203], v[240:243]\n"
"ds_read_b64_tr_b16 v[26:27], v17 offset:14368\n"
"ds_read_b64_tr_b16 v[28:29], v17 offset:17952\n"
"v_cvt_pk_bf16_f32 v129, v128, v129\n"
"s_waitcnt lgkmcnt(8) vmcnt(29)\n"
"v_mfma_f32_16x16x32_bf16 v[164:167], v[102:105], v[204:207], v[164:167]\n"
"ds_read_b64_tr_b16 v[30:31], v17 offset:14400\n"
"ds_read_b64_tr_b16 v[32:33], v17 offset:17984\n"
"v_cmp_eq_u32 vcc, 1, v1\n"
"s_nop 1\n"
"v_cndmask_b32 v129, 0, v129, vcc\n"
"v_cmp_eq_u32 vcc, 0, v1\n"
"s_nop 1\n"
"v_cndmask_b32 v134, v129, v134, vcc\n"
"v_cndmask_b32 v135, 0, v135, vcc\n"
"s_add_u32 s9, s15, 45056\n"
"buffer_load_dwordx4 a[188:191], v3, s[52:55], s9 offen offset:1024 sc1\n"
"v_mfma_f32_16x16x32_bf16 v[120:123], v[106:109], v[204:207], v[120:123]\n"
"ds_read_b64_tr_b16 v[34:35], v17 offset:14432\n"
"ds_read_b64_tr_b16 v[36:37], v17 offset:18016\n"
"s_waitcnt lgkmcnt(8)\n"
"v_mfma_f32_16x16x32_bf16 v[124:127], v[110:113], v[204:207], v[124:127]\n"
"ds_read_b64_tr_b16 v[102:103], v17 offset:21504\n"
"ds_read_b64_tr_b16 v[104:105], v17 offset:25088\n"
"v_mfma_f32_16x16x32_bf16 v[240:243], v[114:117], v[204:207], v[240:243]\n"
"ds_read_b64_tr_b16 v[106:107], v17 offset:21536\n"
"ds_read_b64_tr_b16 v[108:109], v17 offset:25120\n"
"s_add_u32 s10, s14, 49152\n"
"buffer_load_dwordx4 a[192:195], v3, s[52:55], s10 offen  sc1\n"
"s_waitcnt vmcnt(14)\n"
"v_mov_b32 v42, s60\n"
"v_mov_b32 v43, s61\n"
"s_add_u32 s11, s78, 1343512\n"
"s_barrier\n"
"s_mov_b64 exec, 1\n"
"buffer_store_dwordx2 v[42:43], off, s[84:87], s11 sc1\n"
"s_mov_b64 exec, -1\n"
"s_waitcnt lgkmcnt(8)\n"
"v_mfma_f32_16x16x32_bf16 v[164:167], v[22:25], v[208:211], v[164:167]\n"
"ds_read_b64_tr_b16 v[110:111], v17 offset:21568\n"
"ds_read_b64_tr_b16 v[112:113], v17 offset:25152\n"
"v_mfma_f32_16x16x32_bf16 v[120:123], v[26:29], v[208:211], v[120:123]\n"
"ds_read_b64_tr_b16 v[114:115], v17 offset:21600\n"
"ds_read_b64_tr_b16 v[116:117], v17 offset:25184\n"
"s_waitcnt lgkmcnt(8)\n"
"v_mfma_f32_16x16x32_bf16 v[124:127], v[30:33], v[208:211], v[124:127]\n"
"ds_read_b64_tr_b16 v[22:23], v17 offset:28672\n"
"ds_read_b64_tr_b16 v[24:25], v17 offset:32256\n"
"s_add_u32 s8, s14, 49152\n"
"buffer_load_dwordx4 a[196:199], v3, s[52:55], s8 offen offset:1024 sc1\n"
"v_mfma_f32_16x16x32_bf16 v[240:243], v[34:37], v[208:211], v[240:243]\n"
"ds_read_b64_tr_b16 v[26:27], v17 offset:28704\n"
"ds_read_b64_tr_b16 v[28:29], v17 offset:32288\n"
"s_waitcnt lgkmcnt(8)\n"
"v_mfma_f32_16x16x32_bf16 v[164:167], v[102:105], v[212:215], v[164:167]\n"
"ds_read_b64_tr_b16 v[30:31], v17 offset:28736\n"
"ds_read_b64_tr_b16 v[32:33], v17 offset:32320\n"
"v_mfma_f32_16x16x32_bf16 v[120:123], v[106:109], v[212:215], v[120:123]\n"
"ds_read_b64_tr_b16 v[34:35], v17 offset:28768\n"
"ds_read_b64_tr_b16 v[36:37], v17 offset:32352\n"
"s_add_u32 s9, s15, 49152\n"
"buffer_load_dwordx4 a[200:203], v3, s[52:55], s9 offen  sc1\n"
"s_waitcnt lgkmcnt(8)\n"
"v_mfma_f32_16x16x32_bf16 v[124:127], v[110:113], v[212:215], v[124:127]\n"
"ds_read_b64_tr_b16 v[102:103], v17 offset:35840\n"
"ds_read_b64_tr_b16 v[104:105], v17 offset:39424\n"
"v_mfma_f32_16x16x32_bf16 v[240:243], v[114:117], v[212:215], v[240:243]\n"
"ds_read_b64_tr_b16 v[106:107], v17 offset:35872\n"
"ds_read_b64_tr_b16 v[108:109], v17 offset:39456\n"
"s_waitcnt lgkmcnt(8)\n"
"v_mfma_f32_16x16x32_bf16 v[164:167], v[22:25], v[216:219], v[164:167]\n"
"ds_read_b64_tr_b16 v[110:111], v17 offset:35904\n"
"ds_read_b64_tr_b16 v[112:113], v17 offset:39488\n"
"s_add_u32 s10, s15, 49152\n"
"buffer_load_dwordx4 a[204:207], v3, s[52:55], s10 offen offset:1024 sc1\n"
"v_mfma_f32_16x16x32_bf16 v[120:123], v[26:29], v[216:219], v[120:123]\n"
"ds_read_b64_tr_b16 v[114:115], v17 offset:35936\n"
"ds_read_b64_tr_b16 v[116:117], v17 offset:39520\n"
"s_waitcnt lgkmcnt(8)\n"
"v_mfma_f32_16x16x32_bf16 v[124:127], v[30:33], v[216:219], v[124:127]\n"
"ds_read_b64_tr_b16 v[22:23], v17 offset:43008\n"
"ds_read_b64_tr_b16 v[24:25], v17 offset:46592\n"
"v_mfma_f32_16x16x32_bf16 v[240:243], v[34:37], v[216:219], v[240:243]\n"
"ds_read_b64_tr_b16 v[26:27], v17 offset:43040\n"
"ds_read_b64_tr_b16 v[28:29], v17 offset:46624\n"
"s_add_u32 s11, s14, 53248\n"
"buffer_load_dwordx4 a[208:211], v3, s[52:55], s11 offen  sc1\n"
"s_waitcnt lgkmcnt(8)\n"
"v_mfma_f32_16x16x32_bf16 v[164:167], v[102:105], v[220:223], v[164:167]\n"
"ds_read_b64_tr_b16 v[30:31], v17 offset:43072\n"
"ds_read_b64_tr_b16 v[32:33], v17 offset:46656\n"
"v_mfma_f32_16x16x32_bf16 v[120:123], v[106:109], v[220:223], v[120:123]\n"
"ds_read_b64_tr_b16 v[34:35], v17 offset:43104\n"
"ds_read_b64_tr_b16 v[36:37], v17 offset:46688\n"
"s_waitcnt lgkmcnt(8)\n"
"v_mfma_f32_16x16x32_bf16 v[124:127], v[110:113], v[220:223], v[124:127]\n"
"ds_read_b64_tr_b16 v[102:103], v17 offset:50176\n"
"ds_read_b64_tr_b16 v[104:105], v17 offset:53760\n"
"s_add_u32 s8, s14, 53248\n"
"buffer_load_dwordx4 a[212:215], v3, s[52:55], s8 offen offset:1024 sc1\n"
"v_mfma_f32_16x16x32_bf16 v[240:243], v[114:117], v[220:223], v[240:243]\n"
"ds_read_b64_tr_b16 v[106:107], v17 offset:50208\n"
"ds_read_b64_tr_b16 v[108:109], v17 offset:53792\n"
"s_waitcnt lgkmcnt(8)\n"
"v_mfma_f32_16x16x32_bf16 v[164:167], v[22:25], v[224:227], v[164:167]\n"
"ds_read_b64_tr_b16 v[110:111], v17 offset:50240\n"
"ds_read_b64_tr_b16 v[112:113], v17 offset:53824\n"
"v_mfma_f32_16x16x32_bf16 v[120:123], v[26:29], v[224:227], v[120:123]\n"
"ds_read_b64_tr_b16 v[114:115], v17 offset:50272\n"
"ds_read_b64_tr_b16 v[116:117], v17 offset:53856\n"
"s_add_u32 s9, s15, 53248\n"
"buffer_load_dwordx4 a[216:219], v3, s[52:55], s9 offen  sc1\n"
"v_mfma_f32_16x16x16_bf16 v[164:167], a[242:243], v[134:135], v[164:167]\n"
"s_nop 0\n"
"v_mfma_f32_16x16x16_bf16 v[120:123], a[244:245], v[134:135], v[120:123]\n"
"v_mfma_f32_16x16x16_bf16 v[124:127], a[246:247], v[134:135], v[124:127]\n"
"v_mfma_f32_16x16x16_bf16 v[240:243], a[248:249], v[134:135], v[240:243]\n"
"s_waitcnt lgkmcnt(8)\n"
"s_nop 3\n"
"v_mfma_f32_16x16x32_bf16 v[124:127], v[30:33], v[224:227], v[124:127]\n"
"v_mfma_f32_16x16x32_bf16 v[240:243], v[34:37], v[224:227], v[240:243]\n"
"s_waitcnt lgkmcnt(4)\n"
"v_mfma_f32_16x16x32_bf16 v[164:167], v[102:105], v[228:231], v[164:167]\n"
"s_add_u32 s10, s15, 53248\n"
"buffer_load_dwordx4 a[220:223], v3, s[52:55], s10 offen offset:1024 sc1\n"
"v_mfma_f32_16x16x32_bf16 v[120:123], v[106:109], v[228:231], v[120:123]\n"
"s_waitcnt lgkmcnt(0)\n"
"v_mfma_f32_16x16x32_bf16 v[124:127], v[110:113], v[228:231], v[124:127]\n"
"v_mfma_f32_16x16x32_bf16 v[240:243], v[114:117], v[228:231], v[240:243]\n"
"v_and_b32 v20, 3, v2\n"
"v_lshlrev_b32 v20, 7, v20\n"
"s_lshl_b32 s12, s34, 9\n"
"s_add_u32 s12, s12, 1343512\n"
"ds_read_b64_tr_b16 v[22:23], v16 offset:128\n"
"ds_read_b64_tr_b16 v[24:25], v16 offset:3712\n"
"ds_read_b64_tr_b16 v[26:27], v16 offset:160\n"
"ds_read_b64_tr_b16 v[28:29], v16 offset:3744\n"
"ds_read_b64_tr_b16 v[30:31], v18\n"
"ds_read_b64_tr_b16 v[32:33], v18 offset:3584\n"
"ds_read_b64_tr_b16 v[34:35], v16 offset:7296\n"
"ds_read_b64_tr_b16 v[36:37], v16 offset:10880\n"
"ds_read_b64_tr_b16 v[102:103], v16 offset:7328\n"
"ds_read_b64_tr_b16 v[104:105], v16 offset:10912\n"
"ds_read_b64_tr_b16 v[106:107], v18 offset:7168\n"
"ds_read_b64_tr_b16 v[108:109], v18 offset:10752\n"
"v_pk_mul_f32 v[136:137], v[164:165], v[164:165] op_sel:[0,0] op_sel_hi:[1,1]\n"
"v_pk_mul_f32 v[138:139], v[166:167], v[166:167] op_sel:[0,0] op_sel_hi:[1,1]\n"
"v_pk_mul_f32 v[140:141], v[120:121], v[120:121] op_sel:[0,0] op_sel_hi:[1,1]\n"
"v_pk_mul_f32 v[142:143], v[122:123], v[122:123] op_sel:[0,0] op_sel_hi:[1,1]\n"
"v_pk_mul_f32 v[144:145], v[124:125], v[124:125] op_sel:[0,0] op_sel_hi:[1,1]\n"
"v_pk_mul_f32 v[146:147], v[126:127], v[126:127] op_sel:[0,0] op_sel_hi:[1,1]\n"
"v_pk_mul_f32 v[148:149], v[240:241], v[240:241] op_sel:[0,0] op_sel_hi:[1,1]\n"
"v_pk_mul_f32 v[150:151], v[242:243], v[242:243] op_sel:[0,0] op_sel_hi:[1,1]\n"
"v_pk_fma_f32 v[136:137], v[136:137], v[130:131], v[130:131] op_sel:[0,0,1] op_sel_hi:[1,0,1]\n"
"v_pk_fma_f32 v[138:139], v[138:139], v[130:131], v[130:131] op_sel:[0,0,1] op_sel_hi:[1,0,1]\n"
"v_pk_fma_f32 v[140:141], v[140:141], v[130:131], v[130:131] op_sel:[0,0,1] op_sel_hi:[1,0,1]\n"
"v_pk_fma_f32 v[142:143], v[142:143], v[130:131], v[130:131] op_sel:[0,0,1] op_sel_hi:[1,0,1]\n"
"v_pk_fma_f32 v[144:145], v[144:145], v[130:131], v[130:131] op_sel:[0,0,1] op_sel_hi:[1,0,1]\n"
"v_pk_fma_f32 v[146:147], v[146:147], v[130:131], v[130:131] op_sel:[0,0,1] op_sel_hi:[1,0,1]\n"
"v_pk_fma_f32 v[148:149], v[148:149], v[130:131], v[130:131] op_sel:[0,0,1] op_sel_hi:[1,0,1]\n"
"v_pk_fma_f32 v[150:151], v[150:151], v[130:131], v[130:131] op_sel:[0,0,1] op_sel_hi:[1,0,1]\n"
"v_pk_mul_f32 v[136:137], v[136:137], v[164:165] op_sel:[0,0] op_sel_hi:[1,1]\n"
"v_pk_mul_f32 v[138:139], v[138:139], v[166:167] op_sel:[0,0] op_sel_hi:[1,1]\n"
"v_pk_mul_f32 v[140:141], v[140:141], v[120:121] op_sel:[0,0] op_sel_hi:[1,1]\n"
"v_pk_mul_f32 v[142:143], v[142:143], v[122:123] op_sel:[0,0] op_sel_hi:[1,1]\n"
"v_pk_mul_f32 v[144:145], v[144:145], v[124:125] op_sel:[0,0] op_sel_hi:[1,1]\n"
"v_pk_mul_f32 v[146:147], v[146:147], v[126:127] op_sel:[0,0] op_sel_hi:[1,1]\n"
"v_pk_mul_f32 v[148:149], v[148:149], v[240:241] op_sel:[0,0] op_sel_hi:[1,1]\n"
"v_pk_mul_f32 v[150:151], v[150:151], v[242:243] op_sel:[0,0] op_sel_hi:[1,1]\n"
"v_exp_f32 v136, v136\n"
"v_exp_f32 v137, v137\n"
"v_exp_f32 v138, v138\n"
"v_exp_f32 v139, v139\n"
"v_exp_f32 v140, v140\n"
"v_exp_f32 v141, v141\n"
"v_exp_f32 v142, v142\n"
"v_exp_f32 v143, v143\n"
"v_exp_f32 v144, v144\n"
"v_exp_f32 v145, v145\n"
"v_exp_f32 v146, v146\n"
"v_exp_f32 v147, v147\n"
"v_exp_f32 v148, v148\n"
"v_exp_f32 v149, v149\n"
"v_exp_f32 v150, v150\n"
"v_exp_f32 v151, v151\n"
"v_pk_fma_f32 v[136:137], v[136:137], v[132:133], v[132:133] op_sel:[0,0,0] op_sel_hi:[1,0,0]\n"
"buffer_load_dwordx2 v[128:129], v20, s[84:87], s12 offen  sc1\n"
"v_pk_fma_f32 v[138:139], v[138:139], v[132:133], v[132:133] op_sel:[0,0,0] op_sel_hi:[1,0,0]\n"
"v_pk_fma_f32 v[140:141], v[140:141], v[132:133], v[132:133] op_sel:[0,0,0] op_sel_hi:[1,0,0]\n"
"v_pk_fma_f32 v[142:143], v[142:143], v[132:133], v[132:133] op_sel:[0,0,0] op_sel_hi:[1,0,0]\n"
"v_pk_fma_f32 v[144:145], v[144:145], v[132:133], v[132:133] op_sel:[0,0,0] op_sel_hi:[1,0,0]\n"
"v_pk_fma_f32 v[146:147], v[146:147], v[132:133], v[132:133] op_sel:[0,0,0] op_sel_hi:[1,0,0]\n"
"v_pk_fma_f32 v[148:149], v[148:149], v[132:133], v[132:133] op_sel:[0,0,0] op_sel_hi:[1,0,0]\n"
"v_pk_fma_f32 v[150:151], v[150:151], v[132:133], v[132:133] op_sel:[0,0,0] op_sel_hi:[1,0,0]\n"
"v_rcp_f32 v136, v136\n"
"v_rcp_f32 v137, v137\n"
"v_rcp_f32 v138, v138\n"
"v_rcp_f32 v139, v139\n"
"v_rcp_f32 v140, v140\n"
"v_rcp_f32 v141, v141\n"
"v_rcp_f32 v142, v142\n"
"v_rcp_f32 v143, v143\n"
"v_rcp_f32 v144, v144\n"
"v_rcp_f32 v145, v145\n"
"v_rcp_f32 v146, v146\n"
"v_rcp_f32 v147, v147\n"
"v_rcp_f32 v148, v148\n"
"v_rcp_f32 v149, v149\n"
"v_rcp_f32 v150, v150\n"
"v_rcp_f32 v151, v151\n"
"v_pk_mul_f32 v[164:165], v[164:165], v[136:137] op_sel:[0,0] op_sel_hi:[1,1]\n"
"v_pk_mul_f32 v[166:167], v[166:167], v[138:139] op_sel:[0,0] op_sel_hi:[1,1]\n"
"v_pk_mul_f32 v[120:121], v[120:121], v[140:141] op_sel:[0,0] op_sel_hi:[1,1]\n"
"v_pk_mul_f32 v[122:123], v[122:123], v[142:143] op_sel:[0,0] op_sel_hi:[1,1]\n"
"v_pk_mul_f32 v[124:125], v[124:125], v[144:145] op_sel:[0,0] op_sel_hi:[1,1]\n"
"v_pk_mul_f32 v[126:127], v[126:127], v[146:147] op_sel:[0,0] op_sel_hi:[1,1]\n"
"v_pk_mul_f32 v[240:241], v[240:241], v[148:149] op_sel:[0,0] op_sel_hi:[1,1]\n"
"v_pk_mul_f32 v[242:243], v[242:243], v[150:151] op_sel:[0,0] op_sel_hi:[1,1]\n"
"v_cvt_pk_bf16_f32 v164, v164, v165\n"
"v_cvt_pk_bf16_f32 v165, v166, v167\n"
"v_cvt_pk_bf16_f32 v166, v120, v121\n"
"v_cvt_pk_bf16_f32 v167, v122, v123\n"
"s_add_u32 s11, s98, 524288\n"
"buffer_store_dwordx4 v[164:167], v3, s[84:87], s11 offen  sc1\n"
"v_cvt_pk_bf16_f32 v124, v124, v125\n"
"v_cvt_pk_bf16_f32 v125, v126, v127\n"
"v_cvt_pk_bf16_f32 v126, v240, v241\n"
"v_cvt_pk_bf16_f32 v127, v242, v243\n"
"s_add_u32 s8, s98, 524288\n"
"buffer_store_dwordx4 v[124:127], v3, s[84:87], s8 offen offset:1024 sc1\n"
"s_waitcnt lgkmcnt(8)\n"
"v_mfma_f32_16x16x32_bf16 v[244:247], v[22:25], v[168:171], 0\n"
"ds_read_b64_tr_b16 v[110:111], v16 offset:14464\n"
"ds_read_b64_tr_b16 v[112:113], v16 offset:18048\n"
"v_mfma_f32_16x16x32_bf16 v[248:251], v[26:29], v[168:171], 0\n"
"ds_read_b64_tr_b16 v[114:115], v16 offset:14496\n"
"ds_read_b64_tr_b16 v[116:117], v16 offset:18080\n"
"s_waitcnt lgkmcnt(8)\n"
"v_mfma_f32_16x16x32_bf16 v[38:41], v[30:33], v[168:171], 0\n"
"ds_read_b64_tr_b16 v[22:23], v18 offset:14336\n"
"ds_read_b64_tr_b16 v[24:25], v18 offset:17920\n"
"v_mfma_f32_16x16x32_bf16 v[244:247], v[34:37], v[172:175], v[244:247]\n"
"ds_read_b64_tr_b16 v[26:27], v16 offset:21632\n"
"ds_read_b64_tr_b16 v[28:29], v16 offset:25216\n"
"s_waitcnt lgkmcnt(8)\n"
"v_mfma_f32_16x16x32_bf16 v[248:251], v[102:105], v[172:175], v[248:251]\n"
"ds_read_b64_tr_b16 v[30:31], v16 offset:21664\n"
"ds_read_b64_tr_b16 v[32:33], v16 offset:25248\n"
"v_mfma_f32_16x16x32_bf16 v[38:41], v[106:109], v[172:175], v[38:41]\n"
"ds_read_b64_tr_b16 v[34:35], v18 offset:21504\n"
"ds_read_b64_tr_b16 v[36:37], v18 offset:25088\n"
"s_waitcnt lgkmcnt(8)\n"
"v_mfma_f32_16x16x32_bf16 v[244:247], v[110:113], v[176:179], v[244:247]\n"
"ds_read_b64_tr_b16 v[102:103], v16 offset:28800\n"
"ds_read_b64_tr_b16 v[104:105], v16 offset:32384\n"
"v_mfma_f32_16x16x32_bf16 v[248:251], v[114:117], v[176:179], v[248:251]\n"
"ds_read_b64_tr_b16 v[106:107], v16 offset:28832\n"
"ds_read_b64_tr_b16 v[108:109], v16 offset:32416\n"
"s_waitcnt vmcnt(2)\n"
"v_cmp_ne_u64 vcc, s[60:61], v[128:129]\n"
"s_nop 4\n"
"s_cbranch_vccz L_13_%=\n"
"s_mov_b32 s62, 40000\n"
"buffer_load_dwordx2 v[12:13], v20, s[84:87], s12 offen  sc1\n"
"L_14_%=:\n"
"s_waitcnt vmcnt(0)\n"
"v_cmp_ne_u64 vcc, s[60:61], v[12:13]\n"
"s_nop 4\n"
"s_cbranch_vccz L_13_%=\n"
"buffer_load_dwordx2 v[12:13], v20, s[84:87], s12 offen  sc1\n"
"s_sub_u32 s62, s62, 1\n"
"s_cbranch_scc0 L_14_%=\n"
"L_13_%=:\n"
"s_waitcnt lgkmcnt(8)\n"
"v_mfma_f32_16x16x32_bf16 v[38:41], v[22:25], v[176:179], v[38:41]\n"
"ds_read_b64_tr_b16 v[110:111], v18 offset:28672\n"
"ds_read_b64_tr_b16 v[112:113], v18 offset:32256\n"
"buffer_load_dwordx4 v[120:123], v3, s[84:87], s72 offen  sc1\n"
"v_mfma_f32_16x16x32_bf16 v[244:247], v[26:29], v[180:183], v[244:247]\n"
"ds_read_b64_tr_b16 v[114:115], v16 offset:35968\n"
"ds_read_b64_tr_b16 v[116:117], v16 offset:39552\n"
"s_waitcnt lgkmcnt(8)\n"
"v_mfma_f32_16x16x32_bf16 v[248:251], v[30:33], v[180:183], v[248:251]\n"
"ds_read_b64_tr_b16 v[22:23], v16 offset:36000\n"
"ds_read_b64_tr_b16 v[24:25], v16 offset:39584\n"
"v_mfma_f32_16x16x32_bf16 v[38:41], v[34:37], v[180:183], v[38:41]\n"
"ds_read_b64_tr_b16 v[26:27], v18 offset:35840\n"
"ds_read_b64_tr_b16 v[28:29], v18 offset:39424\n"
"buffer_load_dwordx4 v[152:155], v3, s[84:87], s72 offen offset:1024 sc1\n"
"s_waitcnt lgkmcnt(8)\n"
"v_mfma_f32_16x16x32_bf16 v[244:247], v[102:105], v[184:187], v[244:247]\n"
"ds_read_b64_tr_b16 v[30:31], v16 offset:43136\n"
"ds_read_b64_tr_b16 v[32:33], v16 offset:46720\n"
"v_mfma_f32_16x16x32_bf16 v[248:251], v[106:109], v[184:187], v[248:251]\n"
"ds_read_b64_tr_b16 v[34:35], v16 offset:43168\n"
"ds_read_b64_tr_b16 v[36:37], v16 offset:46752\n"
"s_waitcnt lgkmcnt(8)\n"
"v_mfma_f32_16x16x32_bf16 v[38:41], v[110:113], v[184:187], v[38:41]\n"
"ds_read_b64_tr_b16 v[102:103], v18 offset:43008\n"
"ds_read_b64_tr_b16 v[104:105], v18 offset:46592\n"
"buffer_load_dwordx4 v[156:159], v3, s[84:87], s72 offen offset:2048 sc1\n"
"v_mfma_f32_16x16x32_bf16 v[244:247], v[114:117], v[188:191], v[244:247]\n"
"ds_read_b64_tr_b16 v[106:107], v16 offset:50304\n"
"ds_read_b64_tr_b16 v[108:109], v16 offset:53888\n"
"s_waitcnt lgkmcnt(8)\n"
"v_mfma_f32_16x16x32_bf16 v[248:251], v[22:25], v[188:191], v[248:251]\n"
"ds_read_b64_tr_b16 v[110:111], v16 offset:50336\n"
"ds_read_b64_tr_b16 v[112:113], v16 offset:53920\n"
"v_mfma_f32_16x16x32_bf16 v[38:41], v[26:29], v[188:191], v[38:41]\n"
"ds_read_b64_tr_b16 v[114:115], v18 offset:50176\n"
"ds_read_b64_tr_b16 v[116:117], v18 offset:53760\n"
"buffer_load_dwordx4 v[160:163], v3, s[84:87], s72 offen offset:3072 sc1\n"
"s_waitcnt lgkmcnt(8)\n"
"v_mfma_f32_16x16x32_bf16 v[244:247], v[30:33], v[192:195], v[244:247]\n"
"ds_read_b64_tr_b16 v[22:23], v17 offset:128\n"
"ds_read_b64_tr_b16 v[24:25], v17 offset:3712\n"
"v_mfma_f32_16x16x32_bf16 v[248:251], v[34:37], v[192:195], v[248:251]\n"
"ds_read_b64_tr_b16 v[26:27], v17 offset:160\n"
"ds_read_b64_tr_b16 v[28:29], v17 offset:3744\n"
"s_waitcnt lgkmcnt(8)\n"
"v_mfma_f32_16x16x32_bf16 v[38:41], v[102:105], v[192:195], v[38:41]\n"
"ds_read_b64_tr_b16 v[30:31], v19\n"
"ds_read_b64_tr_b16 v[32:33], v19 offset:3584\n"
"s_add_u32 s13, s72, 4096\n"
"buffer_load_dwordx4 v[232:235], v3, s[84:87], s13 offen  sc1\n"
"v_mfma_f32_16x16x32_bf16 v[244:247], v[106:109], v[196:199], v[244:247]\n"
"ds_read_b64_tr_b16 v[34:35], v17 offset:7296\n"
"ds_read_b64_tr_b16 v[36:37], v17 offset:10880\n"
"s_waitcnt lgkmcnt(8)\n"
"v_mfma_f32_16x16x32_bf16 v[248:251], v[110:113], v[196:199], v[248:251]\n"
"ds_read_b64_tr_b16 v[102:103], v17 offset:7328\n"
"ds_read_b64_tr_b16 v[104:105], v17 offset:10912\n"
"v_mfma_f32_16x16x32_bf16 v[38:41], v[114:117], v[196:199], v[38:41]\n"
"ds_read_b64_tr_b16 v[106:107], v19 offset:7168\n"
"ds_read_b64_tr_b16 v[108:109], v19 offset:10752\n"
"buffer_load_dwordx4 v[236:239], v3, s[84:87], s13 offen offset:1024 sc1\n"
"s_waitcnt lgkmcnt(8)\n"
"v_mfma_f32_16x16x32_bf16 v[244:247], v[22:25], v[200:203], v[244:247]\n"
"ds_read_b64_tr_b16 v[110:111], v17 offset:14464\n"
"ds_read_b64_tr_b16 v[112:113], v17 offset:18048\n"
"v_mfma_f32_16x16x32_bf16 v[248:251], v[26:29], v[200:203], v[248:251]\n"
"ds_read_b64_tr_b16 v[114:115], v17 offset:14496\n"
"ds_read_b64_tr_b16 v[116:117], v17 offset:18080\n"
"s_waitcnt lgkmcnt(8)\n"
"v_mfma_f32_16x16x32_bf16 v[38:41], v[30:33], v[200:203], v[38:41]\n"
"ds_read_b64_tr_b16 v[22:23], v19 offset:14336\n"
"ds_read_b64_tr_b16 v[24:25], v19 offset:17920\n"
"buffer_load_dwordx4 v[240:243], v3, s[84:87], s13 offen offset:2048 sc1\n"
"v_mfma_f32_16x16x32_bf16 v[244:247], v[34:37], v[204:207], v[244:247]\n"
"ds_read_b64_tr_b16 v[26:27], v17 offset:21632\n"
"ds_read_b64_tr_b16 v[28:29], v17 offset:25216\n"
"s_waitcnt lgkmcnt(8)\n"
"v_mfma_f32_16x16x32_bf16 v[248:251], v[102:105], v[204:207], v[248:251]\n"
"ds_read_b64_tr_b16 v[30:31], v17 offset:21664\n"
"ds_read_b64_tr_b16 v[32:33], v17 offset:25248\n"
"v_mfma_f32_16x16x32_bf16 v[38:41], v[106:109], v[204:207], v[38:41]\n"
"ds_read_b64_tr_b16 v[34:35], v19 offset:21504\n"
"ds_read_b64_tr_b16 v[36:37], v19 offset:25088\n"
"s_add_u32 s13, s72, 16384\n"
"buffer_load_dwordx4 v[42:45], v3, s[84:87], s13 offen  sc1\n"
"s_waitcnt vmcnt(8)\n"
"v_mov_b32 v128, s60\n"
"v_mov_b32 v129, s61\n"
"s_add_u32 s9, s78, 1343520\n"
"s_barrier\n"
"s_mov_b64 exec, 1\n"
"L_pad7_%=:\n"
"s_cbranch_execz L_ret_%=\n"
"buffer_store_dwordx2 v[128:129], off, s[84:87], s9 sc1\n"
"s_mov_b64 exec, -1\n"
"v_mfma_f32_16x16x16_bf16 v[244:247], a[250:251], v[134:135], v[244:247]\n"
"v_mfma_f32_16x16x16_bf16 v[248:251], a[252:253], v[134:135], v[248:251]\n"
"v_mfma_f32_16x16x16_bf16 v[38:41], a[254:255], v[134:135], v[38:41]\n"
"s_waitcnt lgkmcnt(8)\n"
"s_nop 2\n"
"v_mfma_f32_16x16x32_bf16 v[244:247], v[110:113], v[208:211], v[244:247]\n"
"ds_read_b64_tr_b16 v[102:103], v17 offset:28800\n"
"ds_read_b64_tr_b16 v[104:105], v17 offset:32384\n"
"v_mfma_f32_16x16x32_bf16 v[248:251], v[114:117], v[208:211], v[248:251]\n"
"ds_read_b64_tr_b16 v[106:107], v17 offset:28832\n"
"ds_read_b64_tr_b16 v[108:109], v17 offset:32416\n"
"s_waitcnt lgkmcnt(8)\n"
"v_mfma_f32_16x16x32_bf16 v[38:41], v[22:25], v[208:211], v[38:41]\n"
"ds_read_b64_tr_b16 v[110:111], v19 offset:28672\n"
"ds_read_b64_tr_b16 v[112:113], v19 offset:32256\n"
"buffer_load_dwordx4 v[46:49], v3, s[84:87], s13 offen offset:1024 sc1\n"
"v_mfma_f32_16x16x32_bf16 v[244:247], v[26:29], v[212:215], v[244:247]\n"
"ds_read_b64_tr_b16 v[114:115], v17 offset:35968\n"
"ds_read_b64_tr_b16 v[116:117], v17 offset:39552\n"
"s_waitcnt lgkmcnt(8)\n"
"v_mfma_f32_16x16x32_bf16 v[248:251], v[30:33], v[212:215], v[248:251]\n"
"ds_read_b64_tr_b16 v[22:23], v17 offset:36000\n"
"ds_read_b64_tr_b16 v[24:25], v17 offset:39584\n"
"v_mfma_f32_16x16x32_bf16 v[38:41], v[34:37], v[212:215], v[38:41]\n"
"ds_read_b64_tr_b16 v[26:27], v19 offset:35840\n"
"ds_read_b64_tr_b16 v[28:29], v19 offset:39424\n"
"buffer_load_dwordx4 v[50:53], v3, s[84:87], s13 offen offset:2048 sc1\n"
"s_waitcnt lgkmcnt(8)\n"
"v_mfma_f32_16x16x32_bf16 v[244:247], v[102:105], v[216:219], v[244:247]\n"
"ds_read_b64_tr_b16 v[30:31], v17 offset:43136\n"
"ds_read_b64_tr_b16 v[32:33], v17 offset:46720\n"
"v_mfma_f32_16x16x32_bf16 v[248:251], v[106:109], v[216:219], v[248:251]\n"
"ds_read_b64_tr_b16 v[34:35], v17 offset:43168\n"
"ds_read_b64_tr_b16 v[36:37], v17 offset:46752\n"
"s_waitcnt lgkmcnt(8)\n"
"v_mfma_f32_16x16x32_bf16 v[38:41], v[110:113], v[216:219], v[38:41]\n"
"ds_read_b64_tr_b16 v[102:103], v19 offset:43008\n"
"ds_read_b64_tr_b16 v[104:105], v19 offset:46592\n"
"buffer_load_dwordx4 v[54:57], v3, s[84:87], s13 offen offset:3072 sc1\n"
"v_mfma_f32_16x16x32_bf16 v[244:247], v[114:117], v[220:223], v[244:247]\n"
"ds_read_b64_tr_b16 v[106:107], v17 offset:50304\n"
"ds_read_b64_tr_b16 v[108:109], v17 offset:53888\n"
"s_waitcnt lgkmcnt(8)\n"
"v_mfma_f32_16x16x32_bf16 v[248:251], v[22:25], v[220:223], v[248:251]\n"
"ds_read_b64_tr_b16 v[110:111], v17 offset:50336\n"
"ds_read_b64_tr_b16 v[112:113], v17 offset:53920\n"
"v_mfma_f32_16x16x32_bf16 v[38:41], v[26:29], v[220:223], v[38:41]\n"
"ds_read_b64_tr_b16 v[114:115], v19 offset:50176\n"
"ds_read_b64_tr_b16 v[116:117], v19 offset:53760\n"
"s_add_u32 s13, s72, 20480\n"
"buffer_load_dwordx4 v[58:61], v3, s[84:87], s13 offen  sc1\n"
"s_waitcnt lgkmcnt(8)\n"
"v_mfma_f32_16x16x32_bf16 v[244:247], v[30:33], v[224:227], v[244:247]\n"
"v_mfma_f32_16x16x32_bf16 v[248:251], v[34:37], v[224:227], v[248:251]\n"
"s_waitcnt lgkmcnt(4)\n"
"v_mfma_f32_16x16x32_bf16 v[38:41], v[102:105], v[224:227], v[38:41]\n"
"buffer_load_dwordx4 v[62:65], v3, s[84:87], s13 offen offset:1024 sc1\n"
"v_mfma_f32_16x16x32_bf16 v[244:247], v[106:109], v[228:231], v[244:247]\n"
"s_waitcnt lgkmcnt(0)\n"
"v_mfma_f32_16x16x32_bf16 v[248:251], v[110:113], v[228:231], v[248:251]\n"
"v_mfma_f32_16x16x32_bf16 v[38:41], v[114:117], v[228:231], v[38:41]\n"
"buffer_load_dwordx4 v[66:69], v3, s[84:87], s13 offen offset:2048 sc1\n"
"buffer_load_dwordx4 v[16:19], v3, s[84:87], s71 offen  sc1\n"
"buffer_load_dwordx4 v[124:127], v3, s[84:87], s71 offen offset:1024 sc1\n"
"s_lshr_b32 s63, s82, 6\n"
"s_add_u32 s63, s63, 161280\n"
"v_lshl_add_u32 v20, v1, 6, s63\n"
"ds_read_b128 v[82:85], v20\n"
"ds_read_b128 v[86:89], v20 offset:16\n"
"v_add_u32 v133, s73, v3\n"
"v_add_u32 v128, s74, v3\n"
"v_add_u32 v129, s75, v3\n"
"s_mul_i32 s63, s34, 6144\n"
"s_add_u32 s63, s63, 90112\n"
"v_add_u32 v134, s63, v3\n"
"v_lshlrev_b32 v20, 10, v2\n"
"v_lshl_add_u32 v20, v1, 5, v20\n"
"v_and_b32 v135, 3, v2\n"
"v_lshlrev_b32 v135, 7, v135\n"
"s_lshl_b32 s12, s34, 9\n"
"s_add_u32 s12, s12, 1343520\n"
"s_waitcnt vmcnt(16)\n"
"v_mfma_f32_16x16x32_bf16 v[34:37], a[0:3], v[120:123], 0\n"
"buffer_load_dwordx4 v[90:93], v3, s[84:87], s71 offen offset:2048 sc1\n"
"v_mul_f32 v136, v244, v244\n"
"v_mul_f32 v137, v245, v245\n"
"v_mul_f32 v138, v246, v246\n"
"v_mfma_f32_16x16x32_bf16 v[70:73], a[4:7], v[120:123], 0\n"
"v_mul_f32 v139, v247, v247\n"
"v_mul_f32 v140, v248, v248\n"
"v_mul_f32 v141, v249, v249\n"
"v_mfma_f32_16x16x32_bf16 v[74:77], a[8:11], v[120:123], 0\n"
"v_mul_f32 v142, v250, v250\n"
"v_mul_f32 v143, v251, v251\n"
"v_mul_f32 v144, v38, v38\n"
"v_mfma_f32_16x16x32_bf16 v[78:81], a[12:15], v[120:123], 0\n"
"v_mul_f32 v145, v39, v39\n"
"v_mul_f32 v146, v40, v40\n"
"v_mul_f32 v147, v41, v41\n"
"s_waitcnt vmcnt(16)\n"
"v_mfma_f32_16x16x32_bf16 v[34:37], a[16:19], v[152:155], v[34:37]\n"
"buffer_load_dwordx4 v[120:123], v3, s[84:87], s71 offen offset:3072 sc1\n"
"v_fma_f32 v136, v136, v130, v131\n"
"v_fma_f32 v137, v137, v130, v131\n"
"v_fma_f32 v138, v138, v130, v131\n"
"v_mfma_f32_16x16x32_bf16 v[70:73], a[20:23], v[152:155], v[70:73]\n"
"v_fma_f32 v139, v139, v130, v131\n"
"v_fma_f32 v140, v140, v130, v131\n"
"v_fma_f32 v141, v141, v130, v131\n"
"v_mfma_f32_16x16x32_bf16 v[74:77], a[24:27], v[152:155], v[74:77]\n"
"v_fma_f32 v142, v142, v130, v131\n"
"v_fma_f32 v143, v143, v130, v131\n"
"v_fma_f32 v144, v144, v130, v131\n"
"v_mfma_f32_16x16x32_bf16 v[78:81], a[28:31], v[152:155], v[78:81]\n"
"v_fma_f32 v145, v145, v130, v131\n"
"v_fma_f32 v146, v146, v130, v131\n"
"v_fma_f32 v147, v147, v130, v131\n"
"s_waitcnt vmcnt(16)\n"
"v_mfma_f32_16x16x32_bf16 v[34:37], a[32:35], v[156:159], v[34:37]\n"
"s_add_u32 s13, s71, 4096\n"
"buffer_load_dwordx4 v[152:155], v3, s[84:87], s13 offen  sc1\n"
"v_mul_f32 v136, v136, v244\n"
"v_mul_f32 v137, v137, v245\n"
"v_mul_f32 v138, v138, v246\n"
"v_mfma_f32_16x16x32_bf16 v[70:73], a[36:39], v[156:159], v[70:73]\n"
"v_mul_f32 v139, v139, v247\n"
"v_mul_f32 v140, v140, v248\n"
"v_mul_f32 v141, v141, v249\n"
"v_mfma_f32_16x16x32_bf16 v[74:77], a[40:43], v[156:159], v[74:77]\n"
"v_mul_f32 v142, v142, v250\n"
"v_mul_f32 v143, v143, v251\n"
"v_mul_f32 v144, v144, v38\n"
"v_mfma_f32_16x16x32_bf16 v[78:81], a[44:47], v[156:159], v[78:81]\n"
"v_mul_f32 v145, v145, v39\n"
"v_mul_f32 v146, v146, v40\n"
"v_mul_f32 v147, v147, v41\n"
"s_waitcnt vmcnt(16)\n"
"v_mfma_f32_16x16x32_bf16 v[34:37], a[48:51], v[160:163], v[34:37]\n"
"buffer_load_dwordx4 v[156:159], v3, s[84:87], s13 offen offset:1024 sc1\n"
"v_exp_f32 v136, v136\n"
"v_exp_f32 v137, v137\n"
"v_mfma_f32_16x16x32_bf16 v[70:73], a[52:55], v[160:163], v[70:73]\n"
"v_exp_f32 v138, v138\n"
"v_exp_f32 v139, v139\n"
"v_mfma_f32_16x16x32_bf16 v[74:77], a[56:59], v[160:163], v[74:77]\n"
"v_exp_f32 v140, v140\n"
"v_exp_f32 v141, v141\n"
"v_mfma_f32_16x16x32_bf16 v[78:81], a[60:63], v[160:163], v[78:81]\n"
"v_exp_f32 v142, v142\n"
"v_exp_f32 v143, v143\n"
"s_waitcnt vmcnt(16)\n"
"v_mfma_f32_16x16x32_bf16 v[34:37], a[64:67], v[232:235], v[34:37]\n"
"buffer_load_dwordx4 v[94:97], v3, s[84:87], s13 offen offset:2048 sc1\n"
"v_exp_f32 v144, v144\n"
"v_exp_f32 v145, v145\n"
"v_mfma_f32_16x16x32_bf16 v[70:73], a[68:71], v[232:235], v[70:73]\n"
"v_exp_f32 v146, v146\n"
"v_exp_f32 v147, v147\n"
"v_mfma_f32_16x16x32_bf16 v[74:77], a[72:75], v[232:235], v[74:77]\n"
"v_fma_f32 v136, v136, v132, v132\n"
"v_fma_f32 v137, v137, v132, v132\n"
"v_fma_f32 v138, v138, v132, v132\n"
"v_mfma_f32_16x16x32_bf16 v[78:81], a[76:79], v[232:235], v[78:81]\n"
"v_fma_f32 v139, v139, v132, v132\n"
"v_fma_f32 v140, v140, v132, v132\n"
"v_fma_f32 v141, v141, v132, v132\n"
"buffer_load_dwordx2 v[252:253], v135, s[84:87], s12 offen  sc1\n"
"s_waitcnt vmcnt(17)\n"
"v_mfma_f32_16x16x32_bf16 v[34:37], a[80:83], v[236:239], v[34:37]\n"
"s_add_u32 s13, s71, 16384\n"
"buffer_load_dwordx4 v[98:101], v3, s[84:87], s13 offen  sc1\n"
"v_fma_f32 v142, v142, v132, v132\n"
"v_fma_f32 v143, v143, v132, v132\n"
"v_fma_f32 v144, v144, v132, v132\n"
"v_mfma_f32_16x16x32_bf16 v[70:73], a[84:87], v[236:239], v[70:73]\n"
"v_fma_f32 v145, v145, v132, v132\n"
"v_fma_f32 v146, v146, v132, v132\n"
"v_fma_f32 v147, v147, v132, v132\n"
"v_mfma_f32_16x16x32_bf16 v[74:77], a[88:91], v[236:239], v[74:77]\n"
"v_rcp_f32 v136, v136\n"
"v_rcp_f32 v137, v137\n"
"v_mfma_f32_16x16x32_bf16 v[78:81], a[92:95], v[236:239], v[78:81]\n"
"v_rcp_f32 v138, v138\n"
"v_rcp_f32 v139, v139\n"
"s_waitcnt vmcnt(17)\n"
"v_mfma_f32_16x16x32_bf16 v[34:37], a[96:99], v[240:243], v[34:37]\n"
"buffer_load_dwordx4 v[102:105], v3, s[84:87], s13 offen offset:1024 sc1\n"
"v_rcp_f32 v140, v140\n"
"v_rcp_f32 v141, v141\n"
"v_mfma_f32_16x16x32_bf16 v[70:73], a[100:103], v[240:243], v[70:73]\n"
"v_rcp_f32 v142, v142\n"
"v_rcp_f32 v143, v143\n"
"v_mfma_f32_16x16x32_bf16 v[74:77], a[104:107], v[240:243], v[74:77]\n"
"v_rcp_f32 v144, v144\n"
"v_rcp_f32 v145, v145\n"
"v_mfma_f32_16x16x32_bf16 v[78:81], a[108:111], v[240:243], v[78:81]\n"
"v_rcp_f32 v146, v146\n"
"v_rcp_f32 v147, v147\n"
"s_waitcnt vmcnt(17)\n"
"v_mfma_f32_16x16x32_bf16 v[34:37], a[112:115], v[42:45], v[34:37]\n"
"buffer_load_dwordx4 v[106:109], v3, s[84:87], s13 offen offset:2048 sc1\n"
"v_mul_f32 v244, v244, v136\n"
"v_mul_f32 v245, v245, v137\n"
"v_mul_f32 v246, v246, v138\n"
"v_mfma_f32_16x16x32_bf16 v[70:73], a[116:119], v[42:45], v[70:73]\n"
"v_mul_f32 v247, v247, v139\n"
"v_mul_f32 v248, v248, v140\n"
"v_mul_f32 v249, v249, v141\n"
"v_mfma_f32_16x16x32_bf16 v[74:77], a[120:123], v[42:45], v[74:77]\n"
"v_mul_f32 v250, v250, v142\n"
"v_mul_f32 v251, v251, v143\n"
"v_mul_f32 v38, v38, v144\n"
"v_mfma_f32_16x16x32_bf16 v[78:81], a[124:127], v[42:45], v[78:81]\n"
"v_mul_f32 v39, v39, v145\n"
"v_mul_f32 v40, v40, v146\n"
"v_mul_f32 v41, v41, v147\n"
"s_waitcnt vmcnt(16)\n"
"v_mfma_f32_16x16x32_bf16 v[34:37], a[128:131], v[46:49], v[34:37]\n"
"buffer_load_dwordx4 v[42:45], v3, s[84:87], s13 offen offset:3072 sc1\n"
"v_cvt_pk_bf16_f32 v244, v244, v245\n"
"v_cvt_pk_bf16_f32 v245, v246, v247\n"
"v_cvt_pk_bf16_f32 v246, v248, v249\n"
"v_mfma_f32_16x16x32_bf16 v[70:73], a[132:135], v[46:49], v[70:73]\n"
"v_cvt_pk_bf16_f32 v247, v250, v251\n"
"s_add_u32 s10, s98, 524288\n"
"buffer_store_dwordx4 v[244:247], v3, s[84:87], s10 offen offset:2048 sc1\n"
"v_cvt_pk_bf16_f32 v38, v38, v39\n"
"v_mfma_f32_16x16x32_bf16 v[74:77], a[136:139], v[46:49], v[74:77]\n"
"v_cvt_pk_bf16_f32 v39, v40, v41\n"
"s_add_u32 s11, s99, 524288\n"
"buffer_store_dwordx2 v[38:39], v3, s[84:87], s11 offen offset:3072 sc1\n"
"v_mfma_f32_16x16x32_bf16 v[78:81], a[140:143], v[46:49], v[78:81]\n"
"s_waitcnt vmcnt(18)\n"
"v_mfma_f32_16x16x32_bf16 v[34:37], a[144:147], v[50:53], v[34:37]\n"
"s_add_u32 s13, s71, 20480\n"
"buffer_load_dwordx4 v[38:41], v3, s[84:87], s13 offen  sc1\n"
"v_mfma_f32_16x16x32_bf16 v[70:73], a[148:151], v[50:53], v[70:73]\n"
"v_mfma_f32_16x16x32_bf16 v[74:77], a[152:155], v[50:53], v[74:77]\n"
"v_mfma_f32_16x16x32_bf16 v[78:81], a[156:159], v[50:53], v[78:81]\n"
"s_waitcnt vmcnt(18)\n"
"v_mfma_f32_16x16x32_bf16 v[34:37], a[160:163], v[54:57], v[34:37]\n"
"buffer_load_dwordx4 v[46:49], v3, s[84:87], s13 offen offset:1024 sc1\n"
"v_mfma_f32_16x16x32_bf16 v[70:73], a[164:167], v[54:57], v[70:73]\n"
"v_mfma_f32_16x16x32_bf16 v[74:77], a[168:171], v[54:57], v[74:77]\n"
"v_mfma_f32_16x16x32_bf16 v[78:81], a[172:175], v[54:57], v[78:81]\n"
"s_waitcnt vmcnt(18)\n"
"v_mfma_f32_16x16x32_bf16 v[34:37], a[176:179], v[58:61], v[34:37]\n"
"buffer_load_dwordx4 v[50:53], v3, s[84:87], s13 offen offset:2048 sc1\n"
"v_mfma_f32_16x16x32_bf16 v[70:73], a[180:183], v[58:61], v[70:73]\n"
"v_mfma_f32_16x16x32_bf16 v[74:77], a[184:187], v[58:61], v[74:77]\n"
"v_mfma_f32_16x16x32_bf16 v[78:81], a[188:191], v[58:61], v[78:81]\n"
"s_waitcnt vmcnt(9)\n"
"v_cmp_ne_u64 vcc, s[60:61], v[252:253]\n"
"s_nop 4\n"
"s_cbranch_vccz L_15_%=\n"
"s_mov_b32 s62, 40000\n"
"buffer_load_dwordx2 v[12:13], v135, s[84:87], s12 offen  sc1\n"
"L_16_%=:\n"
"s_waitcnt vmcnt(0)\n"
"v_cmp_ne_u64 vcc, s[60:61], v[12:13]\n"
"s_nop 4\n"
"s_cbranch_vccz L_15_%=\n"
"buffer_load_dwordx2 v[12:13], v135, s[84:87], s12 offen  sc1\n"
"s_sub_u32 s62, s62, 1\n"
"s_cbranch_scc0 L_16_%=\n"
"L_15_%=:\n"
"s_add_u32 s13, s72, 524288\n"
"buffer_load_dwordx4 v[54:57], v3, s[84:87], s13 offen  sc1\n"
"v_mfma_f32_16x16x32_bf16 v[34:37], a[192:195], v[62:65], v[34:37]\n"
"v_mfma_f32_16x16x32_bf16 v[70:73], a[196:199], v[62:65], v[70:73]\n"
"v_mfma_f32_16x16x32_bf16 v[74:77], a[200:203], v[62:65], v[74:77]\n"
"v_mfma_f32_16x16x32_bf16 v[78:81], a[204:207], v[62:65], v[78:81]\n"
"buffer_load_dwordx4 v[58:61], v3, s[84:87], s13 offen offset:1024 sc1\n"
"v_mfma_f32_16x16x32_bf16 v[34:37], a[208:211], v[66:69], v[34:37]\n"
"v_mfma_f32_16x16x32_bf16 v[70:73], a[212:215], v[66:69], v[70:73]\n"
"v_mfma_f32_16x16x32_bf16 v[74:77], a[216:219], v[66:69], v[74:77]\n"
"v_mfma_f32_16x16x32_bf16 v[78:81], a[220:223], v[66:69], v[78:81]\n"
"s_add_u32 s13, s72, 528384\n"
"buffer_load_dwordx4 v[62:65], v3, s[84:87], s13 offen  sc1\n"
"v_mfma_f32_16x16x32_bf16 v[26:29], a[8:11], v[16:19], 0\n"
"v_mfma_f32_16x16x32_bf16 v[30:33], a[12:15], v[16:19], 0\n"
"s_nop 0\n"
"ds_write_b128 v128, v[34:37]\n"
"v_mfma_f32_16x16x32_bf16 v[26:29], a[24:27], v[124:127], v[26:29]\n"
"v_mfma_f32_16x16x32_bf16 v[30:33], a[28:31], v[124:127], v[30:33]\n"
"v_lshlrev_b32 v148, 16, v4\n"
"buffer_load_dwordx4 v[66:69], v3, s[84:87], s13 offen offset:1024 sc1\n"
"v_mfma_f32_16x16x32_bf16 v[26:29], a[40:43], v[90:93], v[26:29]\n"
"v_and_b32 v149, 0xffff0000, v4\n"
"v_mfma_f32_16x16x32_bf16 v[30:33], a[44:47], v[90:93], v[30:33]\n"
"v_lshlrev_b32 v150, 16, v5\n"
"ds_write_b128 v128, v[70:73] offset:1024\n"
"v_mfma_f32_16x16x32_bf16 v[26:29], a[56:59], v[120:123], v[26:29]\n"
"v_and_b32 v151, 0xffff0000, v5\n"
"v_mfma_f32_16x16x32_bf16 v[30:33], a[60:63], v[120:123], v[30:33]\n"
"v_lshlrev_b32 v22, 16, v6\n"
"s_add_u32 s13, s72, 540672\n"
"buffer_load_dwordx4 v[110:113], v3, s[84:87], s13 offen  sc1\n"
"v_mfma_f32_16x16x32_bf16 v[26:29], a[72:75], v[152:155], v[26:29]\n"
"v_and_b32 v23, 0xffff0000, v6\n"
"v_mfma_f32_16x16x32_bf16 v[30:33], a[76:79], v[152:155], v[30:33]\n"
"v_lshlrev_b32 v24, 16, v7\n"
"ds_write_b128 v129, v[74:77]\n"
"v_mfma_f32_16x16x32_bf16 v[26:29], a[88:91], v[156:159], v[26:29]\n"
"v_and_b32 v25, 0xffff0000, v7\n"
"v_mfma_f32_16x16x32_bf16 v[30:33], a[92:95], v[156:159], v[30:33]\n"
"s_waitcnt lgkmcnt(4)\n"
"v_add_f32 v148, v148, v82\n"
"buffer_load_dwordx4 v[114:117], v3, s[84:87], s13 offen offset:1024 sc1\n"
"v_mfma_f32_16x16x32_bf16 v[26:29], a[104:107], v[94:97], v[26:29]\n"
"v_add_f32 v149, v149, v83\n"
"v_mfma_f32_16x16x32_bf16 v[30:33], a[108:111], v[94:97], v[30:33]\n"
"v_add_f32 v150, v150, v84\n"
"ds_write_b128 v129, v[78:81] offset:1024\n"
"s_waitcnt vmcnt(14)\n"
"v_mfma_f32_16x16x32_bf16 v[26:29], a[120:123], v[98:101], v[26:29]\n"
"v_add_f32 v151, v151, v85\n"
"v_mfma_f32_16x16x32_bf16 v[30:33], a[124:127], v[98:101], v[30:33]\n"
"s_waitcnt lgkmcnt(4)\n"
"v_add_f32 v22, v22, v86\n"
"s_waitcnt vmcnt(9)\n"
"v_mov_b32 v118, s60\n"
"v_mov_b32 v119, s61\n"
"s_add_u32 s8, s78, 1343528\n"
"s_barrier\n"
"s_mov_b64 exec, 1\n"
"buffer_store_dwordx2 v[118:119], off, s[84:87], s8 sc1\n"
"s_mov_b64 exec, -1\n"
"v_bfe_u32 v21, v2, 2, 2\n"
"v_lshl_add_u32 v21, v1, 4, v21\n"
"v_mul_u32_u24 v21, 3072, v21\n"
"v_and_b32 v132, 1, v1\n"
"v_lshlrev_b32 v132, 1, v132\n"
"v_and_b32 v135, 3, v2\n"
"v_xor_b32 v132, v132, v135\n"
"v_lshl_add_u32 v21, v132, 4, v21\n"
"s_add_u32 s13, s72, 544768\n"
"buffer_load_dwordx4 v[136:139], v3, s[84:87], s13 offen  sc1\n"
"v_mfma_f32_16x16x32_bf16 v[26:29], a[136:139], v[102:105], v[26:29]\n"
"v_add_f32 v23, v23, v87\n"
"v_mfma_f32_16x16x32_bf16 v[30:33], a[140:143], v[102:105], v[30:33]\n"
"v_add_f32 v24, v24, v88\n"
"v_mfma_f32_16x16x32_bf16 v[26:29], a[152:155], v[106:109], v[26:29]\n"
"v_add_f32 v25, v25, v89\n"
"v_mfma_f32_16x16x32_bf16 v[30:33], a[156:159], v[106:109], v[30:33]\n"
"buffer_load_dwordx4 v[140:143], v3, s[84:87], s13 offen offset:1024 sc1\n"
"v_mfma_f32_16x16x32_bf16 v[26:29], a[168:171], v[42:45], v[26:29]\n"
"v_mfma_f32_16x16x32_bf16 v[30:33], a[172:175], v[42:45], v[30:33]\n"
"s_waitcnt vmcnt(11)\n"
"v_mfma_f32_16x16x32_bf16 v[26:29], a[184:187], v[38:41], v[26:29]\n"
"v_mfma_f32_16x16x32_bf16 v[30:33], a[188:191], v[38:41], v[30:33]\n"
"s_add_u32 s13, s71, 524288\n"
"buffer_load_dwordx4 v[144:147], v3, s[84:87], s13 offen  sc1\n"
"s_waitcnt vmcnt(11)\n"
"v_mfma_f32_16x16x32_bf16 v[26:29], a[200:203], v[46:49], v[26:29]\n"
"v_mfma_f32_16x16x32_bf16 v[30:33], a[204:207], v[46:49], v[30:33]\n"
"s_waitcnt vmcnt(10)\n"
"v_mfma_f32_16x16x32_bf16 v[26:29], a[216:219], v[50:53], v[26:29]\n"
"v_mfma_f32_16x16x32_bf16 v[30:33], a[220:223], v[50:53], v[30:33]\n"
"buffer_load_dwordx4 v[160:163], v3, s[84:87], s13 offen offset:1024 sc1\n"
"v_mfma_f32_16x16x32_bf16 v[148:151], a[0:3], v[16:19], v[148:151]\n"
"v_mfma_f32_16x16x32_bf16 v[22:25], a[4:7], v[16:19], v[22:25]\n"
"s_nop 3\n"
"ds_write_b128 v133, v[26:29]\n"
"v_mfma_f32_16x16x32_bf16 v[148:151], a[16:19], v[124:127], v[148:151]\n"
"v_mfma_f32_16x16x32_bf16 v[22:25], a[20:23], v[124:127], v[22:25]\n"
"s_add_u32 s13, s71, 528384\n"
"buffer_load_dwordx4 v[16:19], v3, s[84:87], s13 offen  sc1\n"
"v_mfma_f32_16x16x32_bf16 v[148:151], a[32:35], v[90:93], v[148:151]\n"
"v_mfma_f32_16x16x32_bf16 v[22:25], a[36:39], v[90:93], v[22:25]\n"
"ds_write_b128 v133, v[30:33] offset:1024\n"
"v_mfma_f32_16x16x32_bf16 v[148:151], a[48:51], v[120:123], v[148:151]\n"
"v_mfma_f32_16x16x32_bf16 v[22:25], a[52:55], v[120:123], v[22:25]\n"
"buffer_load_dwordx4 v[90:93], v3, s[84:87], s13 offen offset:1024 sc1\n"
"v_mfma_f32_16x16x32_bf16 v[148:151], a[64:67], v[152:155], v[148:151]\n"
"v_mfma_f32_16x16x32_bf16 v[22:25], a[68:71], v[152:155], v[22:25]\n"
"v_mfma_f32_16x16x32_bf16 v[148:151], a[80:83], v[156:159], v[148:151]\n"
"v_mfma_f32_16x16x32_bf16 v[22:25], a[84:87], v[156:159], v[22:25]\n"
"s_add_u32 s13, s71, 540672\n"
"buffer_load_dwordx4 v[152:155], v3, s[84:87], s13 offen  sc1\n"
"s_waitcnt lgkmcnt(0)\n"
"s_barrier\n"
"ds_read_b128 v[26:29], v134\n"
"ds_read_b128 v[30:33], v134 offset:1024\n"
"v_mfma_f32_16x16x32_bf16 v[148:151], a[96:99], v[94:97], v[148:151]\n"
"ds_read_b128 v[34:37], v134 offset:2048\n"
"ds_read_b128 v[70:73], v134 offset:3072\n"
"v_mfma_f32_16x16x32_bf16 v[22:25], a[100:103], v[94:97], v[22:25]\n"
"ds_read_b128 v[74:77], v134 offset:4096\n"
"ds_read_b128 v[78:81], v134 offset:5120\n"
"v_mfma_f32_16x16x32_bf16 v[148:151], a[112:115], v[98:101], v[148:151]\n"
"v_mfma_f32_16x16x32_bf16 v[22:25], a[116:119], v[98:101], v[22:25]\n"
"buffer_load_dwordx4 v[156:159], v3, s[84:87], s13 offen offset:1024 sc1\n"
"v_mfma_f32_16x16x32_bf16 v[148:151], a[128:131], v[102:105], v[148:151]\n"
"v_mfma_f32_16x16x32_bf16 v[22:25], a[132:135], v[102:105], v[22:25]\n"
"v_mfma_f32_16x16x32_bf16 v[148:151], a[144:147], v[106:109], v[148:151]\n"
"v_mfma_f32_16x16x32_bf16 v[22:25], a[148:151], v[106:109], v[22:25]\n"
"s_add_u32 s13, s71, 544768\n"
"buffer_load_dwordx4 v[118:121], v3, s[84:87], s13 offen  sc1\n"
"v_mfma_f32_16x16x32_bf16 v[148:151], a[160:163], v[42:45], v[148:151]\n"
"v_mfma_f32_16x16x32_bf16 v[22:25], a[164:167], v[42:45], v[22:25]\n"
"v_mfma_f32_16x16x32_bf16 v[148:151], a[176:179], v[38:41], v[148:151]\n"
"v_mfma_f32_16x16x32_bf16 v[22:25], a[180:183], v[38:41], v[22:25]\n"
"buffer_load_dwordx4 v[122:125], v3, s[84:87], s13 offen offset:1024 sc1\n"
"v_mfma_f32_16x16x32_bf16 v[148:151], a[192:195], v[46:49], v[148:151]\n"
"v_mfma_f32_16x16x32_bf16 v[22:25], a[196:199], v[46:49], v[22:25]\n"
"v_mfma_f32_16x16x32_bf16 v[148:151], a[208:211], v[50:53], v[148:151]\n"
"v_mfma_f32_16x16x32_bf16 v[22:25], a[212:215], v[50:53], v[22:25]\n"
"s_waitcnt lgkmcnt(5)\n"
"s_nop 5\n"
"v_pk_add_f32 v[26:27], v[26:27], v[148:149] op_sel:[0,0] op_sel_hi:[1,1]\n"
"s_waitcnt lgkmcnt(1)\n"
"v_pk_add_f32 v[34:35], v[34:35], v[74:75] op_sel:[0,0] op_sel_hi:[1,1]\n"
"v_pk_add_f32 v[28:29], v[28:29], v[150:151] op_sel:[0,0] op_sel_hi:[1,1]\n"
"v_pk_add_f32 v[36:37], v[36:37], v[76:77] op_sel:[0,0] op_sel_hi:[1,1]\n"
"v_pk_add_f32 v[30:31], v[30:31], v[22:23] op_sel:[0,0] op_sel_hi:[1,1]\n"
"s_waitcnt lgkmcnt(0)\n"
"v_pk_add_f32 v[70:71], v[70:71], v[78:79] op_sel:[0,0] op_sel_hi:[1,1]\n"
"v_pk_add_f32 v[32:33], v[32:33], v[24:25] op_sel:[0,0] op_sel_hi:[1,1]\n"
"v_pk_add_f32 v[72:73], v[72:73], v[80:81] op_sel:[0,0] op_sel_hi:[1,1]\n"
"v_pk_add_f32 v[26:27], v[26:27], v[34:35] op_sel:[0,0] op_sel_hi:[1,1]\n"
"v_pk_add_f32 v[28:29], v[28:29], v[36:37] op_sel:[0,0] op_sel_hi:[1,1]\n"
"v_pk_add_f32 v[30:31], v[30:31], v[70:71] op_sel:[0,0] op_sel_hi:[1,1]\n"
"v_pk_add_f32 v[32:33], v[32:33], v[72:73] op_sel:[0,0] op_sel_hi:[1,1]\n"
"v_cvt_pk_bf16_f32 v4, v26, v27\n"
"v_cvt_pk_bf16_f32 v5, v28, v29\n"
"v_cvt_pk_bf16_f32 v6, v30, v31\n"
"v_cvt_pk_bf16_f32 v7, v32, v33\n"
"buffer_store_dwordx4 v[4:7], v3, s[84:87], s80 offen  sc1\n"
"v_mov_b32 v126, 0\n"
"v_mov_b32 v127, 0\n"
"v_lshlrev_b32 v132, 4, v2\n"
"v_mov_b32 v135, 2147483392\n"
"v_cmp_eq_u32 vcc, 0, v1\n"
"v_dot2c_f32_bf16 v126, s90, v4\n"
"v_dot2c_f32_bf16 v127, v4, v4\n"
"v_dot2c_f32_bf16 v126, s90, v5\n"
"v_dot2c_f32_bf16 v127, v5, v5\n"
"v_dot2c_f32_bf16 v126, s90, v6\n"
"v_dot2c_f32_bf16 v127, v6, v6\n"
"v_dot2c_f32_bf16 v126, s90, v7\n"
"v_dot2c_f32_bf16 v127, v7, v7\n"
"v_cndmask_b32 v132, v135, v132, vcc\n"
"s_nop 1\n"
"v_permlane32_swap_b32 v126, v127\n"
"v_add_f32 v126, v126, v127\n"
"v_mov_b32 v127, v126\n"
"s_nop 1\n"
"v_permlane16_swap_b32 v126, v127\n"
"v_add_f32 v126, v126, v127\n"
"v_mov_b32 v127, v126\n"
"s_nop 1\n"
"v_permlane32_swap_b32 v126, v127\n"
"buffer_store_dwordx2 v[126:127], v132, s[84:87], s81 offen  sc1\n"
"v_and_b32 v132, 3, v2\n"
"v_lshlrev_b32 v132, 7, v132\n"
"s_lshl_b32 s12, s34, 9\n"
"s_add_u32 s12, s12, 1343528\n"
"s_waitcnt vmcnt(18)\n"
"v_mfma_f32_16x16x32_bf16 v[34:37], a[0:3], v[54:57], 0\n"
"v_mfma_f32_16x16x32_bf16 v[70:73], a[4:7], v[54:57], 0\n"
"v_lshlrev_b32 v148, 16, v8\n"
"v_mfma_f32_16x16x32_bf16 v[74:77], a[8:11], v[54:57], 0\n"
"v_and_b32 v149, 0xffff0000, v8\n"
"v_mfma_f32_16x16x32_bf16 v[78:81], a[12:15], v[54:57], 0\n"
"v_lshlrev_b32 v150, 16, v9\n"
"s_waitcnt vmcnt(17)\n"
"v_mfma_f32_16x16x32_bf16 v[34:37], a[16:19], v[58:61], v[34:37]\n"
"v_and_b32 v151, 0xffff0000, v9\n"
"v_mfma_f32_16x16x32_bf16 v[70:73], a[20:23], v[58:61], v[70:73]\n"
"v_lshlrev_b32 v22, 16, v10\n"
"v_mfma_f32_16x16x32_bf16 v[74:77], a[24:27], v[58:61], v[74:77]\n"
"v_and_b32 v23, 0xffff0000, v10\n"
"v_mfma_f32_16x16x32_bf16 v[78:81], a[28:31], v[58:61], v[78:81]\n"
"v_lshlrev_b32 v24, 16, v11\n"
"s_waitcnt vmcnt(16)\n"
"v_mfma_f32_16x16x32_bf16 v[34:37], a[64:67], v[62:65], v[34:37]\n"
"v_and_b32 v25, 0xffff0000, v11\n"
"v_mfma_f32_16x16x32_bf16 v[70:73], a[68:71], v[62:65], v[70:73]\n"
"v_add_f32 v148, v148, v82\n"
"v_mfma_f32_16x16x32_bf16 v[74:77], a[72:75], v[62:65], v[74:77]\n"
"v_add_f32 v149, v149, v83\n"
"v_mfma_f32_16x16x32_bf16 v[78:81], a[76:79], v[62:65], v[78:81]\n"
"v_add_f32 v150, v150, v84\n"
"s_waitcnt vmcnt(15)\n"
"v_mfma_f32_16x16x32_bf16 v[34:37], a[80:83], v[66:69], v[34:37]\n"
"v_add_f32 v151, v151, v85\n"
"v_mfma_f32_16x16x32_bf16 v[70:73], a[84:87], v[66:69], v[70:73]\n"
"v_add_f32 v22, v22, v86\n"
"buffer_load_dwordx2 v[126:127], v132, s[84:87], s12 offen  sc1\n"
"v_mfma_f32_16x16x32_bf16 v[74:77], a[88:91], v[66:69], v[74:77]\n"
"v_add_f32 v23, v23, v87\n"
"v_mfma_f32_16x16x32_bf16 v[78:81], a[92:95], v[66:69], v[78:81]\n"
"v_add_f32 v24, v24, v88\n"
"s_waitcnt vmcnt(15)\n"
"v_mfma_f32_16x16x32_bf16 v[34:37], a[112:115], v[110:113], v[34:37]\n"
"v_add_f32 v25, v25, v89\n"
"v_mfma_f32_16x16x32_bf16 v[70:73], a[116:119], v[110:113], v[70:73]\n"
"v_mfma_f32_16x16x32_bf16 v[74:77], a[120:123], v[110:113], v[74:77]\n"
"v_mfma_f32_16x16x32_bf16 v[78:81], a[124:127], v[110:113], v[78:81]\n"
"s_waitcnt vmcnt(14)\n"
"v_mfma_f32_16x16x32_bf16 v[34:37], a[128:131], v[114:117], v[34:37]\n"
"v_mfma_f32_16x16x32_bf16 v[70:73], a[132:135], v[114:117], v[70:73]\n"
"v_mfma_f32_16x16x32_bf16 v[74:77], a[136:139], v[114:117], v[74:77]\n"
"v_mfma_f32_16x16x32_bf16 v[78:81], a[140:143], v[114:117], v[78:81]\n"
"s_waitcnt vmcnt(12)\n"
"v_mfma_f32_16x16x32_bf16 v[34:37], a[176:179], v[136:139], v[34:37]\n"
"v_mfma_f32_16x16x32_bf16 v[70:73], a[180:183], v[136:139], v[70:73]\n"
"v_mfma_f32_16x16x32_bf16 v[74:77], a[184:187], v[136:139], v[74:77]\n"
"v_mfma_f32_16x16x32_bf16 v[78:81], a[188:191], v[136:139], v[78:81]\n"
"s_waitcnt vmcnt(11)\n"
"v_mfma_f32_16x16x32_bf16 v[34:37], a[192:195], v[140:143], v[34:37]\n"
"v_mfma_f32_16x16x32_bf16 v[70:73], a[196:199], v[140:143], v[70:73]\n"
"v_mfma_f32_16x16x32_bf16 v[74:77], a[200:203], v[140:143], v[74:77]\n"
"v_mfma_f32_16x16x32_bf16 v[78:81], a[204:207], v[140:143], v[78:81]\n"
"s_waitcnt vmcnt(10)\n"
"v_mfma_f32_16x16x32_bf16 v[148:151], a[0:3], v[144:147], v[148:151]\n"
"v_mfma_f32_16x16x32_bf16 v[22:25], a[4:7], v[144:147], v[22:25]\n"
"v_mfma_f32_16x16x32_bf16 v[26:29], a[8:11], v[144:147], 0\n"
"v_mfma_f32_16x16x32_bf16 v[30:33], a[12:15], v[144:147], 0\n"
"s_waitcnt vmcnt(0)\n"
"v_cmp_ne_u64 vcc, s[60:61], v[126:127]\n"
"s_nop 4\n"
"s_cbranch_vccz L_17_%=\n"
"s_mov_b32 s62, 40000\n"
"buffer_load_dwordx2 v[12:13], v132, s[84:87], s12 offen  sc1\n"
"L_18_%=:\n"
"s_waitcnt vmcnt(0)\n"
"v_cmp_ne_u64 vcc, s[60:61], v[12:13]\n"
"s_nop 4\n"
"s_cbranch_vccz L_17_%=\n"
"buffer_load_dwordx2 v[12:13], v132, s[84:87], s12 offen  sc1\n"
"s_sub_u32 s62, s62, 1\n"
"s_cbranch_scc0 L_18_%=\n"
"L_17_%=:\n"
"s_add_u32 s13, s72, 524288\n"
"buffer_load_dwordx4 v[136:139], v3, s[84:87], s13 offen offset:2048 sc1\n"
"v_mfma_f32_16x16x32_bf16 v[148:151], a[16:19], v[160:163], v[148:151]\n"
"v_mfma_f32_16x16x32_bf16 v[22:25], a[20:23], v[160:163], v[22:25]\n"
"v_mfma_f32_16x16x32_bf16 v[26:29], a[24:27], v[160:163], v[26:29]\n"
"buffer_load_dwordx4 v[140:143], v3, s[84:87], s13 offen offset:3072 sc1\n"
"v_mfma_f32_16x16x32_bf16 v[30:33], a[28:31], v[160:163], v[30:33]\n"
"v_mfma_f32_16x16x32_bf16 v[148:151], a[64:67], v[16:19], v[148:151]\n"
"v_mfma_f32_16x16x32_bf16 v[22:25], a[68:71], v[16:19], v[22:25]\n"
"s_add_u32 s13, s72, 528384\n"
"buffer_load_dwordx4 v[144:147], v3, s[84:87], s13 offen offset:2048 sc1\n"
"v_mfma_f32_16x16x32_bf16 v[26:29], a[72:75], v[16:19], v[26:29]\n"
"v_mfma_f32_16x16x32_bf16 v[30:33], a[76:79], v[16:19], v[30:33]\n"
"v_mfma_f32_16x16x32_bf16 v[148:151], a[80:83], v[90:93], v[148:151]\n"
"s_add_u32 s13, s72, 540672\n"
"buffer_load_dwordx4 v[16:19], v3, s[84:87], s13 offen offset:2048 sc1\n"
"v_mfma_f32_16x16x32_bf16 v[22:25], a[84:87], v[90:93], v[22:25]\n"
"v_mfma_f32_16x16x32_bf16 v[26:29], a[88:91], v[90:93], v[26:29]\n"
"v_mfma_f32_16x16x32_bf16 v[30:33], a[92:95], v[90:93], v[30:33]\n"
"buffer_load_dwordx4 v[90:93], v3, s[84:87], s13 offen offset:3072 sc1\n"
"v_mfma_f32_16x16x32_bf16 v[148:151], a[112:115], v[152:155], v[148:151]\n"
"v_mfma_f32_16x16x32_bf16 v[22:25], a[116:119], v[152:155], v[22:25]\n"
"v_mfma_f32_16x16x32_bf16 v[26:29], a[120:123], v[152:155], v[26:29]\n"
"s_add_u32 s13, s72, 544768\n"
"buffer_load_dwordx4 v[94:97], v3, s[84:87], s13 offen offset:2048 sc1\n"
"v_mfma_f32_16x16x32_bf16 v[30:33], a[124:127], v[152:155], v[30:33]\n"
"v_mov_b32 v126, s60\n"
"v_mov_b32 v127, s61\n"
"s_add_u32 s9, s78, 1343536\n"
"s_barrier\n"
"s_mov_b64 exec, 1\n"
"buffer_store_dwordx2 v[126:127], off, s[84:87], s9 sc1\n"
"s_mov_b64 exec, -1\n"
"v_mfma_f32_16x16x32_bf16 v[148:151], a[128:131], v[156:159], v[148:151]\n"
"v_mfma_f32_16x16x32_bf16 v[22:25], a[132:135], v[156:159], v[22:25]\n"
"s_add_u32 s13, s71, 524288\n"
"buffer_load_dwordx4 v[152:155], v3, s[84:87], s13 offen offset:2048 sc1\n"
"v_mfma_f32_16x16x32_bf16 v[26:29], a[136:139], v[156:159], v[26:29]\n"
"v_mfma_f32_16x16x32_bf16 v[30:33], a[140:143], v[156:159], v[30:33]\n"
"v_mfma_f32_16x16x32_bf16 v[148:151], a[176:179], v[118:121], v[148:151]\n"
"buffer_load_dwordx4 v[98:101], v3, s[84:87], s13 offen offset:3072 sc1\n"
"v_mfma_f32_16x16x32_bf16 v[22:25], a[180:183], v[118:121], v[22:25]\n"
"v_mfma_f32_16x16x32_bf16 v[26:29], a[184:187], v[118:121], v[26:29]\n"
"v_mfma_f32_16x16x32_bf16 v[30:33], a[188:191], v[118:121], v[30:33]\n"
"s_add_u32 s13, s71, 528384\n"
"buffer_load_dwordx4 v[102:105], v3, s[84:87], s13 offen offset:2048 sc1\n"
"v_mfma_f32_16x16x32_bf16 v[148:151], a[192:195], v[122:125], v[148:151]\n"
"v_mfma_f32_16x16x32_bf16 v[22:25], a[196:199], v[122:125], v[22:25]\n"
"v_mfma_f32_16x16x32_bf16 v[26:29], a[200:203], v[122:125], v[26:29]\n"
"s_add_u32 s13, s71, 540672\n"
"buffer_load_dwordx4 v[106:109], v3, s[84:87], s13 offen offset:2048 sc1\n"
"v_mfma_f32_16x16x32_bf16 v[30:33], a[204:207], v[122:125], v[30:33]\n"
"s_waitcnt vmcnt(10)\n"
"v_mfma_f32_16x16x32_bf16 v[34:37], a[32:35], v[136:139], v[34:37]\n"
"v_mfma_f32_16x16x32_bf16 v[70:73], a[36:39], v[136:139], v[70:73]\n"
"buffer_load_dwordx4 v[110:113], v3, s[84:87], s13 offen offset:3072 sc1\n"
"v_lshlrev_b32 v180, 4, v2\n"
"v_lshl_add_u32 v180, v1, 9, v180\n"
"s_lshl_b32 s12, s34, 11\n"
"s_add_u32 s12, s12, 1310720\n"
"s_lshl_b32 s94, s34, 15\n"
"s_add_u32 s94, s94, 1048576\n"
"s_and_b32 s63, s93, 512\n"
"s_add_u32 s63, s63, s93\n"
"s_lshl4_add_u32 s94, s63, s94\n"
"v_and_b32 v132, 7, v2\n"
"v_lshlrev_b32 v132, 7, v132\n"
"s_lshr_b32 s96, s34, 1\n"
"s_lshl_b32 s96, s96, 10\n"
"s_add_u32 s96, s96, 1343536\n"
"v_mfma_f32_16x16x32_bf16 v[74:77], a[40:43], v[136:139], v[74:77]\n"
"v_mfma_f32_16x16x32_bf16 v[78:81], a[44:47], v[136:139], v[78:81]\n"
"s_waitcnt vmcnt(10)\n"
"v_mfma_f32_16x16x32_bf16 v[34:37], a[48:51], v[140:143], v[34:37]\n"
"s_add_u32 s13, s71, 544768\n"
"buffer_load_dwordx4 v[136:139], v3, s[84:87], s13 offen offset:2048 sc1\n"
"v_mfma_f32_16x16x32_bf16 v[70:73], a[52:55], v[140:143], v[70:73]\n"
"v_mfma_f32_16x16x32_bf16 v[74:77], a[56:59], v[140:143], v[74:77]\n"
"v_mfma_f32_16x16x32_bf16 v[78:81], a[60:63], v[140:143], v[78:81]\n"
"s_add_u32 m0, s91, 61440\n"
"s_xor_b32 s10, s93, 384\n"
"s_mul_i32 s10, s10, 1536\n"
"s_add_u32 s10, s10, s88\n"
"s_add_u32 s10, s10, 2048\n"
"buffer_load_dwordx4 v21, s[16:19], s10 offen lds\n"
"L_pad8_%=:\n"
"s_cbranch_execz L_ret_%=\n"
"s_waitcnt vmcnt(11)\n"
"v_mfma_f32_16x16x32_bf16 v[34:37], a[96:99], v[144:147], v[34:37]\n"
"v_mfma_f32_16x16x32_bf16 v[70:73], a[100:103], v[144:147], v[70:73]\n"
"v_mfma_f32_16x16x32_bf16 v[74:77], a[104:107], v[144:147], v[74:77]\n"
"s_add_u32 m0, s91, 65536\n"
"s_xor_b32 s11, s93, 512\n"
"s_mul_i32 s11, s11, 1536\n"
"s_add_u32 s11, s11, s88\n"
"s_add_u32 s11, s11, 2048\n"
"buffer_load_dwordx4 v21, s[16:19], s11 offen lds\n"
"v_mfma_f32_16x16x32_bf16 v[78:81], a[108:111], v[144:147], v[78:81]\n"
"s_waitcnt vmcnt(11)\n"
"v_mfma_f32_16x16x32_bf16 v[34:37], a[144:147], v[16:19], v[34:37]\n"
"v_mfma_f32_16x16x32_bf16 v[70:73], a[148:151], v[16:19], v[70:73]\n"
"s_add_u32 m0, s91, 69632\n"
"s_xor_b32 s8, s93, 640\n"
"s_mul_i32 s8, s8, 1536\n"
"s_add_u32 s8, s8, s88\n"
"s_add_u32 s8, s8, 2048\n"
"buffer_load_dwordx4 v21, s[16:19], s8 offen lds\n"
"v_mfma_f32_16x16x32_bf16 v[74:77], a[152:155], v[16:19], v[74:77]\n"
"v_mfma_f32_16x16x32_bf16 v[78:81], a[156:159], v[16:19], v[78:81]\n"
"s_waitcnt vmcnt(11)\n"
"v_mfma_f32_16x16x32_bf16 v[34:37], a[160:163], v[90:93], v[34:37]\n"
"s_add_u32 m0, s91, 73728\n"
"s_xor_b32 s9, s93, 768\n"
"s_mul_i32 s9, s9, 1536\n"
"s_add_u32 s9, s9, s88\n"
"s_add_u32 s9, s9, 2048\n"
"buffer_load_dwordx4 v21, s[16:19], s9 offen lds\n"
"v_mfma_f32_16x16x32_bf16 v[70:73], a[164:167], v[90:93], v[70:73]\n"
"v_mfma_f32_16x16x32_bf16 v[74:77], a[168:171], v[90:93], v[74:77]\n"
"v_mfma_f32_16x16x32_bf16 v[78:81], a[172:175], v[90:93], v[78:81]\n"
"s_add_u32 m0, s91, 77824\n"
"s_xor_b32 s10, s93, 896\n"
"s_mul_i32 s10, s10, 1536\n"
"s_add_u32 s10, s10, s88\n"
"s_add_u32 s10, s10, 2048\n"
"buffer_load_dwordx4 v21, s[16:19], s10 offen lds\n"
"s_waitcnt vmcnt(12)\n"
"v_mfma_f32_16x16x32_bf16 v[34:37], a[208:211], v[94:97], v[34:37]\n"
"v_mfma_f32_16x16x32_bf16 v[70:73], a[212:215], v[94:97], v[70:73]\n"
"v_mfma_f32_16x16x32_bf16 v[74:77], a[216:219], v[94:97], v[74:77]\n"
"s_add_u32 m0, s91, 81920\n"
"s_mul_i32 s11, s93, 1536\n"
"s_add_u32 s11, s11, s88\n"
"buffer_load_dwordx4 v21, s[16:19], s11 offen lds\n"
"v_mfma_f32_16x16x32_bf16 v[78:81], a[220:223], v[94:97], v[78:81]\n"
"s_waitcnt vmcnt(11)\n"
"v_mfma_f32_16x16x32_bf16 v[26:29], a[40:43], v[152:155], v[26:29]\n"
"v_mfma_f32_16x16x32_bf16 v[30:33], a[44:47], v[152:155], v[30:33]\n"
"s_add_u32 m0, s91, 86016\n"
"s_xor_b32 s8, s93, 128\n"
"s_mul_i32 s8, s8, 1536\n"
"s_add_u32 s8, s8, s88\n"
"buffer_load_dwordx4 v21, s[16:19], s8 offen lds\n"
"buffer_load_dwordx2 v[126:127], v132, s[84:87], s96 offen  sc1\n"
"ds_write_b128 v128, v[34:37]\n"
"s_waitcnt vmcnt(12)\n"
"v_mfma_f32_16x16x32_bf16 v[26:29], a[56:59], v[98:101], v[26:29]\n"
"v_mfma_f32_16x16x32_bf16 v[30:33], a[60:63], v[98:101], v[30:33]\n"
"s_waitcnt vmcnt(11)\n"
"v_mfma_f32_16x16x32_bf16 v[26:29], a[104:107], v[102:105], v[26:29]\n"
"ds_write_b128 v128, v[70:73] offset:1024\n"
"v_mfma_f32_16x16x32_bf16 v[30:33], a[108:111], v[102:105], v[30:33]\n"
"s_waitcnt vmcnt(10)\n"
"v_mfma_f32_16x16x32_bf16 v[26:29], a[152:155], v[106:109], v[26:29]\n"
"v_mfma_f32_16x16x32_bf16 v[30:33], a[156:159], v[106:109], v[30:33]\n"
"ds_write_b128 v129, v[74:77]\n"
"s_waitcnt vmcnt(9)\n"
"v_mfma_f32_16x16x32_bf16 v[26:29], a[168:171], v[110:113], v[26:29]\n"
"v_mfma_f32_16x16x32_bf16 v[30:33], a[172:175], v[110:113], v[30:33]\n"
"s_waitcnt vmcnt(8)\n"
"v_mfma_f32_16x16x32_bf16 v[26:29], a[216:219], v[136:139], v[26:29]\n"
"ds_write_b128 v129, v[78:81] offset:1024\n"
"v_mfma_f32_16x16x32_bf16 v[30:33], a[220:223], v[136:139], v[30:33]\n"
"v_mfma_f32_16x16x32_bf16 v[148:151], a[32:35], v[152:155], v[148:151]\n"
"v_mfma_f32_16x16x32_bf16 v[22:25], a[36:39], v[152:155], v[22:25]\n"
"s_nop 3\n"
"ds_write_b128 v133, v[26:29]\n"
"v_mfma_f32_16x16x32_bf16 v[148:151], a[48:51], v[98:101], v[148:151]\n"
"v_mfma_f32_16x16x32_bf16 v[22:25], a[52:55], v[98:101], v[22:25]\n"
"buffer_load_dwordx2 v[130:131], v132, s[84:87], s96 offen  sc1\n"
"ds_write_b128 v133, v[30:33] offset:1024\n"
"v_mfma_f32_16x16x32_bf16 v[148:151], a[96:99], v[102:105], v[148:151]\n"
"v_mfma_f32_16x16x32_bf16 v[22:25], a[100:103], v[102:105], v[22:25]\n"
"s_waitcnt lgkmcnt(0)\n"
"s_barrier\n"
"ds_read_b128 v[26:29], v134\n"
"ds_read_b128 v[30:33], v134 offset:1024\n"
"v_mfma_f32_16x16x32_bf16 v[148:151], a[144:147], v[106:109], v[148:151]\n"
"ds_read_b128 v[34:37], v134 offset:2048\n"
"ds_read_b128 v[70:73], v134 offset:3072\n"
"v_mfma_f32_16x16x32_bf16 v[22:25], a[148:151], v[106:109], v[22:25]\n"
"ds_read_b128 v[74:77], v134 offset:4096\n"
"ds_read_b128 v[78:81], v134 offset:5120\n"
"v_mfma_f32_16x16x32_bf16 v[148:151], a[160:163], v[110:113], v[148:151]\n"
"v_mfma_f32_16x16x32_bf16 v[22:25], a[164:167], v[110:113], v[22:25]\n"
"v_mfma_f32_16x16x32_bf16 v[148:151], a[208:211], v[136:139], v[148:151]\n"
"v_mfma_f32_16x16x32_bf16 v[22:25], a[212:215], v[136:139], v[22:25]\n"
"s_waitcnt lgkmcnt(5)\n"
"s_nop 5\n"
"v_pk_add_f32 v[26:27], v[26:27], v[148:149] op_sel:[0,0] op_sel_hi:[1,1]\n"
"s_waitcnt lgkmcnt(1)\n"
"v_pk_add_f32 v[34:35], v[34:35], v[74:75] op_sel:[0,0] op_sel_hi:[1,1]\n"
"v_pk_add_f32 v[28:29], v[28:29], v[150:151] op_sel:[0,0] op_sel_hi:[1,1]\n"
"v_pk_add_f32 v[36:37], v[36:37], v[76:77] op_sel:[0,0] op_sel_hi:[1,1]\n"
"v_pk_add_f32 v[30:31], v[30:31], v[22:23] op_sel:[0,0] op_sel_hi:[1,1]\n"
"s_waitcnt lgkmcnt(0)\n"
"v_pk_add_f32 v[70:71], v[70:71], v[78:79] op_sel:[0,0] op_sel_hi:[1,1]\n"
"v_pk_add_f32 v[32:33], v[32:33], v[24:25] op_sel:[0,0] op_sel_hi:[1,1]\n"
"v_pk_add_f32 v[72:73], v[72:73], v[80:81] op_sel:[0,0] op_sel_hi:[1,1]\n"
"v_pk_add_f32 v[26:27], v[26:27], v[34:35] op_sel:[0,0] op_sel_hi:[1,1]\n"
"v_pk_add_f32 v[28:29], v[28:29], v[36:37] op_sel:[0,0] op_sel_hi:[1,1]\n"
"v_pk_add_f32 v[30:31], v[30:31], v[70:71] op_sel:[0,0] op_sel_hi:[1,1]\n"
"v_pk_add_f32 v[32:33], v[32:33], v[72:73] op_sel:[0,0] op_sel_hi:[1,1]\n"
"v_cvt_pk_bf16_f32 v8, v26, v27\n"
"v_cvt_pk_bf16_f32 v9, v28, v29\n"
"v_cvt_pk_bf16_f32 v10, v30, v31\n"
"v_cvt_pk_bf16_f32 v11, v32, v33\n"
"s_add_u32 s9, s80, 131072\n"
"buffer_store_dwordx4 v[8:11], v3, s[84:87], s9 offen  sc1\n"
"v_mov_b32 v16, 0\n"
"v_mov_b32 v17, 0\n"
"v_lshlrev_b32 v135, 4, v2\n"
"v_mov_b32 v18, 2147483392\n"
"v_cmp_eq_u32 vcc, 0, v1\n"
"v_dot2c_f32_bf16 v16, s90, v8\n"
"v_dot2c_f32_bf16 v17, v8, v8\n"
"v_dot2c_f32_bf16 v16, s90, v9\n"
"v_dot2c_f32_bf16 v17, v9, v9\n"
"v_dot2c_f32_bf16 v16, s90, v10\n"
"v_dot2c_f32_bf16 v17, v10, v10\n"
"v_dot2c_f32_bf16 v16, s90, v11\n"
"v_dot2c_f32_bf16 v17, v11, v11\n"
"v_cndmask_b32 v135, v18, v135, vcc\n"
"s_nop 1\n"
"v_permlane32_swap_b32 v16, v17\n"
"v_add_f32 v16, v16, v17\n"
"v_mov_b32 v17, v16\n"
"s_nop 1\n"
"v_permlane16_swap_b32 v16, v17\n"
"v_add_f32 v16, v16, v17\n"
"v_mov_b32 v17, v16\n"
"s_nop 1\n"
"v_permlane32_swap_b32 v16, v17\n"
"s_add_u32 s10, s81, 16384\n"
"buffer_store_dwordx2 v[16:17], v135, s[84:87], s10 offen  sc1\n"
"s_waitcnt vmcnt(3)\n"
"v_cmp_ne_u64 vcc, s[60:61], v[126:127]\n"
"s_nop 4\n"
"s_cbranch_vccz L_19_%=\n"
"s_waitcnt vmcnt(2)\n"
"v_cmp_ne_u64 vcc, s[60:61], v[130:131]\n"
"s_nop 4\n"
"s_cbranch_vccz L_19_%=\n"
"s_mov_b32 s62, 40000\n"
"buffer_load_dwordx2 v[12:13], v132, s[84:87], s96 offen  sc1\n"
"L_20_%=:\n"
"s_waitcnt vmcnt(0)\n"
"v_cmp_ne_u64 vcc, s[60:61], v[12:13]\n"
"s_nop 4\n"
"s_cbranch_vccz L_19_%=\n"
"buffer_load_dwordx2 v[12:13], v132, s[84:87], s96 offen  sc1\n"
"s_sub_u32 s62, s62, 1\n"
"s_cbranch_scc0 L_20_%=\n"
"L_19_%=:\n"
"buffer_load_dwordx4 v[114:117], v3, s[84:87], s94 offen  sc1\n"
"buffer_load_dwordx4 v[118:121], v3, s[84:87], s94 offen offset:1024 sc1\n"
"buffer_load_dwordx4 v[176:179], v180, s[84:87], s12 offen  sc1\n"
"buffer_load_dwordx4 v[180:183], v180, s[84:87], s12 offen offset:256 sc1\n"
"s_xor_b32 s95, s94, 2048\n"
"buffer_load_dwordx4 v[122:125], v3, s[84:87], s95 offen  sc1\n"
"buffer_load_dwordx4 v[38:41], v3, s[84:87], s95 offen offset:1024 sc1\n"
"s_xor_b32 s95, s94, 4096\n"
"buffer_load_dwordx4 v[42:45], v3, s[84:87], s95 offen  sc1\n"
"buffer_load_dwordx4 v[46:49], v3, s[84:87], s95 offen offset:1024 sc1\n"
"s_xor_b32 s95, s94, 6144\n"
"buffer_load_dwordx4 v[50:53], v3, s[84:87], s95 offen  sc1\n"
"buffer_load_dwordx4 v[54:57], v3, s[84:87], s95 offen offset:1024 sc1\n"
"s_xor_b32 s95, s94, 16384\n"
"buffer_load_dwordx4 v[58:61], v3, s[84:87], s95 offen  sc1\n"
"buffer_load_dwordx4 v[62:65], v3, s[84:87], s95 offen offset:1024 sc1\n"
"s_xor_b32 s95, s94, 18432\n"
"buffer_load_dwordx4 v[66:69], v3, s[84:87], s95 offen  sc1\n"
"v_lshrrev_b32 v20, 2, v2\n"
"v_lshlrev_b32 v20, 6, v20\n"
"v_lshl_add_u32 v20, v1, 8, v20\n"
"v_and_b32 v16, 3, v2\n"
"v_lshl_add_u32 v20, v16, 3, v20\n"
"v_and_b32 v16, 1, v1\n"
"v_lshl_add_u32 v20, v16, 5, v20\n"
"v_add_u32 v16, 114688, v20\n"
"v_xor_b32 v17, 32, v16\n"
"v_add_u32 v18, 61440, v20\n"
"v_xor_b32 v19, 32, v18\n"
"v_lshlrev_b32 v20, 2, v2\n"
"v_add_u32 v20, 159744, v20\n"
"ds_read2_b32 a[242:243], v20 offset0:0 offset1:0\n"
"ds_read2_b32 a[244:245], v20 offset0:16 offset1:16\n"
"ds_read2_b32 a[246:247], v20 offset0:32 offset1:32\n"
"ds_read2_b32 a[248:249], v20 offset0:48 offset1:48\n"
"ds_read2_b32 a[250:251], v20 offset0:64 offset1:64\n"
"ds_read2_b32 a[252:253], v20 offset0:80 offset1:80\n"
"s_waitcnt lgkmcnt(0)\n"
"s_barrier\n"
"v_and_b32 v85, 7, v2\n"
"v_lshlrev_b32 v85, 7, v85\n"
"s_lshr_b32 s13, s34, 1\n"
"s_lshl_b32 s13, s13, 10\n"
"s_add_u32 s13, s13, 1343544\n"
"ds_read_b64_tr_b16 v[22:23], v16\n"
"ds_read_b64_tr_b16 v[24:25], v16 offset:1024\n"
"ds_read_b64_tr_b16 v[26:27], v17\n"
"ds_read_b64_tr_b16 v[28:29], v17 offset:1024\n"
"ds_read_b64_tr_b16 v[30:31], v16 offset:32768\n"
"ds_read_b64_tr_b16 v[32:33], v16 offset:33792\n"
"ds_read_b64_tr_b16 v[34:35], v17 offset:32768\n"
"ds_read_b64_tr_b16 v[36:37], v17 offset:33792\n"
"ds_read_b64_tr_b16 v[126:127], v16 offset:2048\n"
"ds_read_b64_tr_b16 v[128:129], v16 offset:3072\n"
"s_waitcnt vmcnt(15)\n"
"ds_read_b64_tr_b16 v[130:131], v17 offset:2048\n"
"ds_read_b64_tr_b16 v[132:133], v17 offset:3072\n"
"s_waitcnt lgkmcnt(8) vmcnt(12)\n"
"v_mfma_f32_16x16x32_bf16 v[152:155], v[22:25], v[114:117], 0\n"
"ds_read_b64_tr_b16 v[134:135], v16 offset:34816\n"
"ds_read_b64_tr_b16 v[136:137], v16 offset:35840\n"
"buffer_load_dwordx4 v[156:159], v3, s[84:87], s95 offen offset:1024 sc1\n"
"v_mfma_f32_16x16x32_bf16 v[70:73], v[26:29], v[114:117], 0\n"
"ds_read_b64_tr_b16 v[138:139], v17 offset:34816\n"
"ds_read_b64_tr_b16 v[140:141], v17 offset:35840\n"
"s_waitcnt lgkmcnt(8)\n"
"v_mfma_f32_16x16x32_bf16 v[74:77], v[114:117], v[30:33], 0\n"
"ds_read_b64_tr_b16 v[22:23], v16 offset:4096\n"
"ds_read_b64_tr_b16 v[24:25], v16 offset:5120\n"
"v_mfma_f32_16x16x32_bf16 v[78:81], v[114:117], v[34:37], 0\n"
"ds_read_b64_tr_b16 v[26:27], v17 offset:4096\n"
"ds_read_b64_tr_b16 v[28:29], v17 offset:5120\n"
"s_xor_b32 s95, s94, 20480\n"
"buffer_load_dwordx4 v[160:163], v3, s[84:87], s95 offen  sc1\n"
"s_waitcnt lgkmcnt(8) vmcnt(13)\n"
"v_mfma_f32_16x16x32_bf16 v[152:155], v[126:129], v[118:121], v[152:155]\n"
"ds_read_b64_tr_b16 v[30:31], v16 offset:36864\n"
"ds_read_b64_tr_b16 v[32:33], v16 offset:37888\n"
"v_mfma_f32_16x16x32_bf16 v[70:73], v[130:133], v[118:121], v[70:73]\n"
"ds_read_b64_tr_b16 v[34:35], v17 offset:36864\n"
"ds_read_b64_tr_b16 v[36:37], v17 offset:37888\n"
"s_waitcnt lgkmcnt(8)\n"
"v_mfma_f32_16x16x32_bf16 v[74:77], v[118:121], v[134:137], v[74:77]\n"
"ds_read_b64_tr_b16 v[126:127], v16 offset:6144\n"
"ds_read_b64_tr_b16 v[128:129], v16 offset:7168\n"
"buffer_load_dwordx4 v[164:167], v3, s[84:87], s95 offen offset:1024 sc1\n"
"s_waitcnt vmcnt(13)\n"
"v_add_f32 v176, v176, v178\n"
"v_mfma_f32_16x16x32_bf16 v[78:81], v[118:121], v[138:141], v[78:81]\n"
"ds_read_b64_tr_b16 v[130:131], v17 offset:6144\n"
"ds_read_b64_tr_b16 v[132:133], v17 offset:7168\n"
"v_add_f32 v177, v177, v179\n"
"v_mov_b32 v88, s60\n"
"v_mov_b32 v89, s61\n"
"s_add_u32 s11, s78, 1343544\n"
"s_barrier\n"
"s_mov_b64 exec, 1\n"
"buffer_store_dwordx2 v[88:89], off, s[84:87], s11 sc1\n"
"s_mov_b64 exec, -1\n"
"s_waitcnt lgkmcnt(8) vmcnt(12)\n"
"v_mfma_f32_16x16x32_bf16 v[152:155], v[22:25], v[122:125], v[152:155]\n"
"ds_read_b64_tr_b16 v[134:135], v16 offset:38912\n"
"ds_read_b64_tr_b16 v[136:137], v16 offset:39936\n"
"v_add_f32 v180, v180, v182\n"
"v_mfma_f32_16x16x32_bf16 v[70:73], v[26:29], v[122:125], v[70:73]\n"
"ds_read_b64_tr_b16 v[138:139], v17 offset:38912\n"
"ds_read_b64_tr_b16 v[140:141], v17 offset:39936\n"
"s_xor_b32 s95, s94, 22528\n"
"buffer_load_dwordx4 v[168:171], v3, s[84:87], s95 offen  sc1\n"
"v_add_f32 v181, v181, v183\n"
"s_waitcnt lgkmcnt(8)\n"
"v_mfma_f32_16x16x32_bf16 v[74:77], v[122:125], v[30:33], v[74:77]\n"
"ds_read_b64_tr_b16 v[22:23], v16 offset:8192\n"
"ds_read_b64_tr_b16 v[24:25], v16 offset:9216\n"
"v_add_f32 v176, v176, v180\n"
"v_mfma_f32_16x16x32_bf16 v[78:81], v[122:125], v[34:37], v[78:81]\n"
"ds_read_b64_tr_b16 v[26:27], v17 offset:8192\n"
"ds_read_b64_tr_b16 v[28:29], v17 offset:9216\n"
"v_add_f32 v177, v177, v181\n"
"s_waitcnt lgkmcnt(8) vmcnt(12)\n"
"v_mfma_f32_16x16x32_bf16 v[152:155], v[126:129], v[38:41], v[152:155]\n"
"ds_read_b64_tr_b16 v[30:31], v16 offset:40960\n"
"ds_read_b64_tr_b16 v[32:33], v16 offset:41984\n"
"buffer_load_dwordx4 v[172:175], v3, s[84:87], s95 offen offset:1024 sc1\n"
"v_permlane32_swap_b32 v176, v177\n"
"v_mfma_f32_16x16x32_bf16 v[70:73], v[130:133], v[38:41], v[70:73]\n"
"ds_read_b64_tr_b16 v[34:35], v17 offset:40960\n"
"ds_read_b64_tr_b16 v[36:37], v17 offset:41984\n"
"v_add_f32 v176, v176, v177\n"
"s_waitcnt lgkmcnt(8)\n"
"v_mfma_f32_16x16x32_bf16 v[74:77], v[38:41], v[134:137], v[74:77]\n"
"ds_read_b64_tr_b16 v[126:127], v16 offset:10240\n"
"ds_read_b64_tr_b16 v[128:129], v16 offset:11264\n"
"v_mov_b32 v177, v176\n"
"v_mfma_f32_16x16x32_bf16 v[78:81], v[38:41], v[138:141], v[78:81]\n"
"ds_read_b64_tr_b16 v[130:131], v17 offset:10240\n"
"ds_read_b64_tr_b16 v[132:133], v17 offset:11264\n"
"v_permlane16_swap_b32 v176, v177\n"
"s_waitcnt lgkmcnt(8) vmcnt(12)\n"
"v_mfma_f32_16x16x32_bf16 v[152:155], v[22:25], v[42:45], v[152:155]\n"
"ds_read_b64_tr_b16 v[134:135], v16 offset:43008\n"
"ds_read_b64_tr_b16 v[136:137], v16 offset:44032\n"
"v_add_f32 v176, v176, v177\n"
"v_mfma_f32_16x16x32_bf16 v[70:73], v[26:29], v[42:45], v[70:73]\n"
"ds_read_b64_tr_b16 v[138:139], v17 offset:43008\n"
"ds_read_b64_tr_b16 v[140:141], v17 offset:44032\n"
"v_mov_b32 v177, v176\n"
"s_waitcnt lgkmcnt(8)\n"
"v_mfma_f32_16x16x32_bf16 v[74:77], v[42:45], v[30:33], v[74:77]\n"
"ds_read_b64_tr_b16 v[22:23], v16 offset:12288\n"
"ds_read_b64_tr_b16 v[24:25], v16 offset:13312\n"
"v_permlane32_swap_b32 v176, v177\n"
"v_mfma_f32_16x16x32_bf16 v[78:81], v[42:45], v[34:37], v[78:81]\n"
"ds_read_b64_tr_b16 v[26:27], v17 offset:12288\n"
"ds_read_b64_tr_b16 v[28:29], v17 offset:13312\n"
"v_mul_f32 v176, 0x3b000000, v176\n"
"s_waitcnt lgkmcnt(8) vmcnt(11)\n"
"v_mfma_f32_16x16x32_bf16 v[152:155], v[126:129], v[46:49], v[152:155]\n"
"ds_read_b64_tr_b16 v[30:31], v18\n"
"ds_read_b64_tr_b16 v[32:33], v18 offset:1024\n"
"v_mul_f32 v177, 0x3b000000, v177\n"
"v_mfma_f32_16x16x32_bf16 v[70:73], v[130:133], v[46:49], v[70:73]\n"
"ds_read_b64_tr_b16 v[34:35], v19\n"
"ds_read_b64_tr_b16 v[36:37], v19 offset:1024\n"
"v_fma_f32 v177, -v176, v176, v177\n"
"s_waitcnt lgkmcnt(8)\n"
"v_mfma_f32_16x16x32_bf16 v[74:77], v[46:49], v[134:137], v[74:77]\n"
"ds_read_b64_tr_b16 v[126:127], v16 offset:14336\n"
"ds_read_b64_tr_b16 v[128:129], v16 offset:15360\n"
"v_max_f32 v177, 0, v177\n"
"v_mfma_f32_16x16x32_bf16 v[78:81], v[46:49], v[138:141], v[78:81]\n"
"ds_read_b64_tr_b16 v[130:131], v17 offset:14336\n"
"ds_read_b64_tr_b16 v[132:133], v17 offset:15360\n"
"v_add_f32 v177, 0x3727c5ac, v177\n"
"s_waitcnt lgkmcnt(8) vmcnt(10)\n"
"v_mfma_f32_16x16x32_bf16 v[152:155], v[22:25], v[50:53], v[152:155]\n"
"ds_read_b64_tr_b16 v[134:135], v18 offset:2048\n"
"ds_read_b64_tr_b16 v[136:137], v18 offset:3072\n"
"v_rsq_f32 v177, v177\n"
"v_mfma_f32_16x16x32_bf16 v[70:73], v[26:29], v[50:53], v[70:73]\n"
"ds_read_b64_tr_b16 v[138:139], v19 offset:2048\n"
"ds_read_b64_tr_b16 v[140:141], v19 offset:3072\n"
"v_mul_f32 v142, 0x3fb8aa3b, v177\n"
"s_waitcnt lgkmcnt(8)\n"
"v_mfma_f32_16x16x32_bf16 v[74:77], v[50:53], v[30:33], v[74:77]\n"
"ds_read_b64_tr_b16 v[22:23], v16 offset:16384\n"
"ds_read_b64_tr_b16 v[24:25], v16 offset:17408\n"
"v_mul_f32 v176, 0xbe800000, v176\n"
"v_mfma_f32_16x16x32_bf16 v[78:81], v[50:53], v[34:37], v[78:81]\n"
"ds_read_b64_tr_b16 v[26:27], v17 offset:16384\n"
"ds_read_b64_tr_b16 v[28:29], v17 offset:17408\n"
"v_mul_f32 v177, 0x3e000000, v177\n"
"s_waitcnt lgkmcnt(8) vmcnt(9)\n"
"v_mfma_f32_16x16x32_bf16 v[152:155], v[126:129], v[54:57], v[152:155]\n"
"ds_read_b64_tr_b16 v[30:31], v18 offset:4096\n"
"ds_read_b64_tr_b16 v[32:33], v18 offset:5120\n"
"v_cvt_pk_bf16_f32 v144, v176, v176\n"
"v_mfma_f32_16x16x32_bf16 v[70:73], v[130:133], v[54:57], v[70:73]\n"
"ds_read_b64_tr_b16 v[34:35], v19 offset:4096\n"
"ds_read_b64_tr_b16 v[36:37], v19 offset:5120\n"
"v_cvt_pk_bf16_f32 v86, v177, v177\n"
"s_waitcnt lgkmcnt(8)\n"
"v_mfma_f32_16x16x32_bf16 v[74:77], v[54:57], v[134:137], v[74:77]\n"
"ds_read_b64_tr_b16 v[126:127], v16 offset:18432\n"
"ds_read_b64_tr_b16 v[128:129], v16 offset:19456\n"
"v_and_b32 v83, 0xffff0000, v144\n"
"v_mfma_f32_16x16x32_bf16 v[78:81], v[54:57], v[138:141], v[78:81]\n"
"ds_read_b64_tr_b16 v[130:131], v17 offset:18432\n"
"ds_read_b64_tr_b16 v[132:133], v17 offset:19456\n"
"v_and_b32 v84, 0xffff0000, v86\n"
"s_waitcnt lgkmcnt(8) vmcnt(8)\n"
"v_mfma_f32_16x16x32_bf16 v[152:155], v[22:25], v[58:61], v[152:155]\n"
"ds_read_b64_tr_b16 v[134:135], v18 offset:6144\n"
"ds_read_b64_tr_b16 v[136:137], v18 offset:7168\n"
"v_sub_f32 v83, v176, v83\n"
"v_mfma_f32_16x16x32_bf16 v[70:73], v[26:29], v[58:61], v[70:73]\n"
"ds_read_b64_tr_b16 v[138:139], v19 offset:6144\n"
"ds_read_b64_tr_b16 v[140:141], v19 offset:7168\n"
"v_sub_f32 v84, v177, v84\n"
"s_waitcnt lgkmcnt(8)\n"
"v_mfma_f32_16x16x32_bf16 v[74:77], v[58:61], v[30:33], v[74:77]\n"
"ds_read_b64_tr_b16 v[22:23], v16 offset:20480\n"
"ds_read_b64_tr_b16 v[24:25], v16 offset:21504\n"
"v_cvt_pk_bf16_f32 v145, v83, v83\n"
"v_mfma_f32_16x16x32_bf16 v[78:81], v[58:61], v[34:37], v[78:81]\n"
"ds_read_b64_tr_b16 v[26:27], v17 offset:20480\n"
"ds_read_b64_tr_b16 v[28:29], v17 offset:21504\n"
"v_cvt_pk_bf16_f32 v87, v84, v84\n"
"s_waitcnt lgkmcnt(8) vmcnt(7)\n"
"v_mfma_f32_16x16x32_bf16 v[152:155], v[126:129], v[62:65], v[152:155]\n"
"ds_read_b64_tr_b16 v[30:31], v18 offset:8192\n"
"ds_read_b64_tr_b16 v[32:33], v18 offset:9216\n"
"v_mfma_f32_16x16x16_bf16 v[148:151], v[86:87], a[228:229], 0\n"
"v_mfma_f32_16x16x32_bf16 v[70:73], v[130:133], v[62:65], v[70:73]\n"
"ds_read_b64_tr_b16 v[34:35], v19 offset:8192\n"
"ds_read_b64_tr_b16 v[36:37], v19 offset:9216\n"
"s_waitcnt lgkmcnt(8)\n"
"v_mfma_f32_16x16x32_bf16 v[74:77], v[62:65], v[134:137], v[74:77]\n"
"ds_read_b64_tr_b16 v[126:127], v16 offset:22528\n"
"ds_read_b64_tr_b16 v[128:129], v16 offset:23552\n"
"v_mfma_f32_16x16x32_bf16 v[78:81], v[62:65], v[138:141], v[78:81]\n"
"ds_read_b64_tr_b16 v[130:131], v17 offset:22528\n"
"ds_read_b64_tr_b16 v[132:133], v17 offset:23552\n"
"s_waitcnt lgkmcnt(8) vmcnt(6)\n"
"v_mfma_f32_16x16x32_bf16 v[152:155], v[22:25], v[66:69], v[152:155]\n"
"ds_read_b64_tr_b16 v[134:135], v18 offset:10240\n"
"ds_read_b64_tr_b16 v[136:137], v18 offset:11264\n"
"v_mfma_f32_16x16x32_bf16 v[70:73], v[26:29], v[66:69], v[70:73]\n"
"ds_read_b64_tr_b16 v[138:139], v19 offset:10240\n"
"ds_read_b64_tr_b16 v[140:141], v19 offset:11264\n"
"s_waitcnt lgkmcnt(8)\n"
"v_mfma_f32_16x16x32_bf16 v[74:77], v[66:69], v[30:33], v[74:77]\n"
"ds_read_b64_tr_b16 v[22:23], v16 offset:24576\n"
"ds_read_b64_tr_b16 v[24:25], v16 offset:25600\n"
"v_mfma_f32_16x16x32_bf16 v[78:81], v[66:69], v[34:37], v[78:81]\n"
"ds_read_b64_tr_b16 v[26:27], v17 offset:24576\n"
"ds_read_b64_tr_b16 v[28:29], v17 offset:25600\n"
"s_waitcnt lgkmcnt(8) vmcnt(5)\n"
"v_mfma_f32_16x16x32_bf16 v[152:155], v[126:129], v[156:159], v[152:155]\n"
"ds_read_b64_tr_b16 v[30:31], v18 offset:12288\n"
"ds_read_b64_tr_b16 v[32:33], v18 offset:13312\n"
"v_mfma_f32_16x16x32_bf16 v[70:73], v[130:133], v[156:159], v[70:73]\n"
"ds_read_b64_tr_b16 v[34:35], v19 offset:12288\n"
"ds_read_b64_tr_b16 v[36:37], v19 offset:13312\n"
"s_waitcnt lgkmcnt(8)\n"
"v_mfma_f32_16x16x32_bf16 v[74:77], v[156:159], v[134:137], v[74:77]\n"
"ds_read_b64_tr_b16 v[126:127], v16 offset:26624\n"
"ds_read_b64_tr_b16 v[128:129], v16 offset:27648\n"
"v_mfma_f32_16x16x32_bf16 v[78:81], v[156:159], v[138:141], v[78:81]\n"
"ds_read_b64_tr_b16 v[130:131], v17 offset:26624\n"
"ds_read_b64_tr_b16 v[132:133], v17 offset:27648\n"
"s_waitcnt lgkmcnt(8) vmcnt(4)\n"
"v_mfma_f32_16x16x32_bf16 v[152:155], v[22:25], v[160:163], v[152:155]\n"
"ds_read_b64_tr_b16 v[134:135], v18 offset:14336\n"
"ds_read_b64_tr_b16 v[136:137], v18 offset:15360\n"
"v_mfma_f32_16x16x32_bf16 v[70:73], v[26:29], v[160:163], v[70:73]\n"
"ds_read_b64_tr_b16 v[138:139], v19 offset:14336\n"
"ds_read_b64_tr_b16 v[140:141], v19 offset:15360\n"
"s_waitcnt lgkmcnt(8)\n"
"v_mfma_f32_16x16x32_bf16 v[74:77], v[160:163], v[30:33], v[74:77]\n"
"ds_read_b64_tr_b16 v[22:23], v16 offset:28672\n"
"ds_read_b64_tr_b16 v[24:25], v16 offset:29696\n"
"v_mfma_f32_16x16x32_bf16 v[78:81], v[160:163], v[34:37], v[78:81]\n"
"ds_read_b64_tr_b16 v[26:27], v17 offset:28672\n"
"ds_read_b64_tr_b16 v[28:29], v17 offset:29696\n"
"s_waitcnt lgkmcnt(8) vmcnt(3)\n"
"v_mfma_f32_16x16x32_bf16 v[152:155], v[126:129], v[164:167], v[152:155]\n"
"ds_read_b64_tr_b16 v[30:31], v18 offset:16384\n"
"ds_read_b64_tr_b16 v[32:33], v18 offset:17408\n"
"v_mfma_f32_16x16x32_bf16 v[70:73], v[130:133], v[164:167], v[70:73]\n"
"ds_read_b64_tr_b16 v[34:35], v19 offset:16384\n"
"ds_read_b64_tr_b16 v[36:37], v19 offset:17408\n"
"s_waitcnt lgkmcnt(8)\n"
"v_mfma_f32_16x16x32_bf16 v[74:77], v[164:167], v[134:137], v[74:77]\n"
"ds_read_b64_tr_b16 v[126:127], v16 offset:30720\n"
"ds_read_b64_tr_b16 v[128:129], v16 offset:31744\n"
"v_mfma_f32_16x16x32_bf16 v[78:81], v[164:167], v[138:141], v[78:81]\n"
"ds_read_b64_tr_b16 v[130:131], v17 offset:30720\n"
"ds_read_b64_tr_b16 v[132:133], v17 offset:31744\n"
"buffer_load_dwordx2 v[88:89], v85, s[84:87], s13 offen  sc1\n"
"s_waitcnt lgkmcnt(8) vmcnt(2)\n"
"v_mfma_f32_16x16x32_bf16 v[152:155], v[22:25], v[168:171], v[152:155]\n"
"ds_read_b64_tr_b16 v[134:135], v18 offset:18432\n"
"ds_read_b64_tr_b16 v[136:137], v18 offset:19456\n"
"v_mfma_f32_16x16x32_bf16 v[70:73], v[26:29], v[168:171], v[70:73]\n"
"ds_read_b64_tr_b16 v[138:139], v19 offset:18432\n"
"ds_read_b64_tr_b16 v[140:141], v19 offset:19456\n"
"s_waitcnt lgkmcnt(8)\n"
"v_mfma_f32_16x16x32_bf16 v[74:77], v[168:171], v[30:33], v[74:77]\n"
"v_mfma_f32_16x16x32_bf16 v[78:81], v[168:171], v[34:37], v[78:81]\n"
"v_mfma_f32_16x16x16_bf16 v[152:155], a[242:243], v[144:145], v[152:155]\n"
"s_nop 3\n"
"v_mfma_f32_16x16x16_bf16 v[74:77], v[144:145], a[246:247], v[74:77]\n"
"v_mfma_f32_16x16x16_bf16 v[70:73], a[244:245], v[144:145], v[70:73]\n"
"v_mfma_f32_16x16x16_bf16 v[78:81], v[144:145], a[248:249], v[78:81]\n"
"s_waitcnt lgkmcnt(4) vmcnt(1)\n"
"v_mfma_f32_16x16x32_bf16 v[152:155], v[126:129], v[172:175], v[152:155]\n"
"s_nop 2\n"
"v_mfma_f32_16x16x32_bf16 v[70:73], v[130:133], v[172:175], v[70:73]\n"
"s_waitcnt lgkmcnt(0)\n"
"v_mfma_f32_16x16x32_bf16 v[74:77], v[172:175], v[134:137], v[74:77]\n"
"v_mfma_f32_16x16x32_bf16 v[78:81], v[172:175], v[138:141], v[78:81]\n"
"v_lshlrev_b32 v220, 4, v2\n"
"v_lshl_add_u32 v220, v1, 9, v220\n"
"s_lshl_b32 s12, s34, 11\n"
"s_add_u32 s12, s12, 1327104\n"
"s_lshl_b32 s94, s34, 15\n"
"s_add_u32 s94, s94, 1179648\n"
"s_and_b32 s63, s93, 512\n"
"s_add_u32 s63, s63, s93\n"
"s_lshl4_add_u32 s94, s63, s94\n"
"v_cvt_pk_bf16_f32 v232, v152, v153\n"
"v_cvt_pk_bf16_f32 v233, v154, v155\n"
"v_cvt_pk_bf16_f32 v236, v70, v71\n"
"v_cvt_pk_bf16_f32 v237, v72, v73\n"
"v_mfma_f32_16x16x16_bf16 v[152:155], a[224:225], v[232:233], 0\n"
"s_nop 0\n"
"v_mfma_f32_16x16x16_bf16 v[70:73], a[226:227], v[236:237], 0\n"
"v_pk_mul_f32 v[74:75], v[74:75], v[148:149] op_sel:[0,0] op_sel_hi:[1,1]\n"
"v_pk_mul_f32 v[76:77], v[76:77], v[150:151] op_sel:[0,0] op_sel_hi:[1,1]\n"
"v_pk_mul_f32 v[78:79], v[78:79], v[148:149] op_sel:[0,0] op_sel_hi:[1,1]\n"
"v_pk_mul_f32 v[80:81], v[80:81], v[150:151] op_sel:[0,0] op_sel_hi:[1,1]\n"
"v_cvt_pk_bf16_f32 v224, v74, v75\n"
"v_cvt_pk_bf16_f32 v225, v76, v77\n"
"v_cvt_pk_bf16_f32 v228, v78, v79\n"
"v_cvt_pk_bf16_f32 v229, v80, v81\n"
"v_max3_f32 v20, v152, v153, v154\n"
"v_max3_f32 v84, -v152, -v153, -v154\n"
"v_max3_f32 v82, v70, v71, v72\n"
"v_max3_f32 v83, -v70, -v71, -v72\n"
"v_max_f32 v20, v20, v155\n"
"v_max_f32 v84, v84, -v155\n"
"v_max_f32 v82, v82, v73\n"
"v_max_f32 v83, v83, -v73\n"
"v_permlane32_swap_b32 v20, v84\n"
"s_nop 0\n"
"v_permlane32_swap_b32 v82, v83\n"
"v_max_f32 v20, v20, v84\n"
"v_max_f32 v82, v82, v83\n"
"s_nop 1\n"
"v_permlane16_swap_b32 v20, v82\n"
"v_max_f32 v20, v20, v82\n"
"v_mul_f32 v20, -v20, v142\n"
"v_mov_b32 v82, v20\n"
"s_nop 1\n"
"v_permlane16_swap_b32 v20, v82\n"
"v_mov_b32 v84, v20\n"
"v_mov_b32 v83, v82\n"
"s_nop 0\n"
"v_permlane32_swap_b32 v20, v84\n"
"v_permlane32_swap_b32 v82, v83\n"
"s_waitcnt vmcnt(0)\n"
"v_cmp_ne_u64 vcc, s[60:61], v[88:89]\n"
"s_nop 4\n"
"s_cbranch_vccz L_21_%=\n"
"s_mov_b32 s62, 40000\n"
"buffer_load_dwordx2 v[12:13], v85, s[84:87], s13 offen  sc1\n"
"L_22_%=:\n"
"s_waitcnt vmcnt(0)\n"
"v_cmp_ne_u64 vcc, s[60:61], v[12:13]\n"
"s_nop 4\n"
"s_cbranch_vccz L_21_%=\n"
"buffer_load_dwordx2 v[12:13], v85, s[84:87], s13 offen  sc1\n"
"s_sub_u32 s62, s62, 1\n"
"s_cbranch_scc0 L_22_%=\n"
"L_21_%=:\n"
"buffer_load_dwordx4 v[90:93], v3, s[84:87], s94 offen  sc1\n"
"buffer_load_dwordx4 v[94:97], v3, s[84:87], s94 offen offset:1024 sc1\n"
"v_pk_fma_f32 v[240:241], v[152:153], v[142:143], v[84:85] op_sel:[0,0,0] op_sel_hi:[1,0,0] neg_lo:[1,0,0] neg_hi:[1,0,0]\n"
"v_pk_fma_f32 v[152:153], v[152:153], v[142:143], v[20:21] op_sel:[0,0,0] op_sel_hi:[1,0,0]\n"
"buffer_load_dwordx4 v[216:219], v220, s[84:87], s12 offen  sc1\n"
"v_pk_fma_f32 v[242:243], v[154:155], v[142:143], v[84:85] op_sel:[0,0,0] op_sel_hi:[1,0,0] neg_lo:[1,0,0] neg_hi:[1,0,0]\n"
"v_pk_fma_f32 v[154:155], v[154:155], v[142:143], v[20:21] op_sel:[0,0,0] op_sel_hi:[1,0,0]\n"
"v_pk_fma_f32 v[244:245], v[70:71], v[142:143], v[82:83] op_sel:[0,0,1] op_sel_hi:[1,0,1] neg_lo:[1,0,0] neg_hi:[1,0,0]\n"
"v_pk_fma_f32 v[70:71], v[70:71], v[142:143], v[82:83] op_sel:[0,0,0] op_sel_hi:[1,0,0]\n"
"v_pk_fma_f32 v[246:247], v[72:73], v[142:143], v[82:83] op_sel:[0,0,1] op_sel_hi:[1,0,1] neg_lo:[1,0,0] neg_hi:[1,0,0]\n"
"v_pk_fma_f32 v[72:73], v[72:73], v[142:143], v[82:83] op_sel:[0,0,0] op_sel_hi:[1,0,0]\n"
"v_exp_f32 v240, v240\n"
"v_exp_f32 v152, v152\n"
"buffer_load_dwordx4 v[220:223], v220, s[84:87], s12 offen offset:256 sc1\n"
"v_exp_f32 v241, v241\n"
"v_exp_f32 v153, v153\n"
"v_exp_f32 v242, v242\n"
"v_exp_f32 v154, v154\n"
"v_exp_f32 v243, v243\n"
"v_exp_f32 v155, v155\n"
"v_exp_f32 v244, v244\n"
"s_xor_b32 s95, s94, 2048\n"
"buffer_load_dwordx4 v[98:101], v3, s[84:87], s95 offen  sc1\n"
"v_exp_f32 v70, v70\n"
"v_exp_f32 v245, v245\n"
"v_exp_f32 v71, v71\n"
"v_exp_f32 v246, v246\n"
"v_exp_f32 v72, v72\n"
"v_exp_f32 v247, v247\n"
"v_exp_f32 v73, v73\n"
"v_pk_add_f32 v[232:233], v[152:153], v[154:155] op_sel:[0,0] op_sel_hi:[1,1]\n"
"buffer_load_dwordx4 v[102:105], v3, s[84:87], s95 offen offset:1024 sc1\n"
"v_pk_add_f32 v[234:235], v[240:241], v[242:243] op_sel:[0,0] op_sel_hi:[1,1]\n"
"v_pk_add_f32 v[236:237], v[70:71], v[72:73] op_sel:[0,0] op_sel_hi:[1,1]\n"
"v_pk_add_f32 v[238:239], v[244:245], v[246:247] op_sel:[0,0] op_sel_hi:[1,1]\n"
"v_add_f32 v20, v232, v233\n"
"v_add_f32 v84, v234, v235\n"
"v_add_f32 v82, v236, v237\n"
"v_add_f32 v83, v238, v239\n"
"s_xor_b32 s95, s94, 4096\n"
"buffer_load_dwordx4 v[106:109], v3, s[84:87], s95 offen  sc1\n"
"v_permlane32_swap_b32 v20, v84\n"
"v_permlane32_swap_b32 v82, v83\n"
"v_add_f32 v20, v20, v84\n"
"v_add_f32 v82, v82, v83\n"
"s_nop 1\n"
"v_permlane16_swap_b32 v20, v82\n"
"v_add_f32 v20, v20, v82\n"
"v_rcp_f32 v20, v20\n"
"buffer_load_dwordx4 v[110:113], v3, s[84:87], s95 offen offset:1024 sc1\n"
"v_mov_b32 v82, v20\n"
"s_nop 1\n"
"v_permlane16_swap_b32 v20, v82\n"
"v_mov_b32 v84, v20\n"
"v_mov_b32 v83, v82\n"
"s_nop 0\n"
"v_permlane32_swap_b32 v20, v84\n"
"v_permlane32_swap_b32 v82, v83\n"
"v_pk_mul_f32 v[152:153], v[152:153], v[20:21] op_sel:[0,0] op_sel_hi:[1,0]\n"
"v_pk_mul_f32 v[240:241], v[240:241], v[84:85] op_sel:[0,0] op_sel_hi:[1,0]\n"
"s_xor_b32 s95, s94, 6144\n"
"buffer_load_dwordx4 v[176:179], v3, s[84:87], s95 offen  sc1\n"
"v_pk_mul_f32 v[154:155], v[154:155], v[20:21] op_sel:[0,0] op_sel_hi:[1,0]\n"
"L_pad9_%=:\n"
"s_cbranch_execz L_ret_%=\n"
"v_pk_mul_f32 v[242:243], v[242:243], v[84:85] op_sel:[0,0] op_sel_hi:[1,0]\n"
"v_pk_mul_f32 v[70:71], v[70:71], v[82:83] op_sel:[0,0] op_sel_hi:[1,0]\n"
"v_pk_mul_f32 v[244:245], v[244:245], v[82:83] op_sel:[0,1] op_sel_hi:[1,1]\n"
"v_pk_mul_f32 v[72:73], v[72:73], v[82:83] op_sel:[0,0] op_sel_hi:[1,0]\n"
"v_pk_mul_f32 v[246:247], v[246:247], v[82:83] op_sel:[0,1] op_sel_hi:[1,1]\n"
"v_cvt_pk_bf16_f32 v232, v152, v153\n"
"buffer_load_dwordx4 v[180:183], v3, s[84:87], s95 offen offset:1024 sc1\n"
"v_cvt_pk_bf16_f32 v233, v154, v155\n"
"v_cvt_pk_bf16_f32 v234, v240, v241\n"
"v_cvt_pk_bf16_f32 v235, v242, v243\n"
"v_cvt_pk_bf16_f32 v236, v70, v71\n"
"v_cvt_pk_bf16_f32 v237, v72, v73\n"
"v_cvt_pk_bf16_f32 v238, v244, v245\n"
"v_cvt_pk_bf16_f32 v239, v246, v247\n"
"v_mfma_f32_16x16x16_bf16 v[152:155], v[232:233], a[232:233], 0\n"
"v_mfma_f32_16x16x16_bf16 v[70:73], v[234:235], a[232:233], 0\n"
"v_mfma_f32_16x16x16_bf16 v[74:77], v[236:237], a[232:233], 0\n"
"v_mfma_f32_16x16x16_bf16 v[78:81], v[238:239], a[232:233], 0\n"
"s_xor_b32 s95, s94, 16384\n"
"buffer_load_dwordx4 v[184:187], v3, s[84:87], s95 offen  sc1\n"
"buffer_load_dwordx4 v[188:191], v3, s[84:87], s95 offen offset:1024 sc1\n"
"s_nop 1\n"
"v_cvt_pk_bf16_f32 v82, v152, v153\n"
"v_cvt_pk_bf16_f32 v83, v154, v155\n"
"v_cvt_pk_bf16_f32 v86, v70, v71\n"
"v_cvt_pk_bf16_f32 v87, v72, v73\n"
"v_cvt_pk_bf16_f32 v240, v74, v75\n"
"v_cvt_pk_bf16_f32 v241, v76, v77\n"
"v_cvt_pk_bf16_f32 v244, v78, v79\n"
"v_cvt_pk_bf16_f32 v245, v80, v81\n"
"ds_read_b64_tr_b16 v[22:23], v16\n"
"ds_read_b64_tr_b16 v[24:25], v16 offset:1024\n"
"ds_read_b64_tr_b16 v[26:27], v17\n"
"ds_read_b64_tr_b16 v[28:29], v17 offset:1024\n"
"ds_read_b64_tr_b16 v[30:31], v16 offset:32768\n"
"ds_read_b64_tr_b16 v[32:33], v16 offset:33792\n"
"ds_read_b64_tr_b16 v[34:35], v17 offset:32768\n"
"ds_read_b64_tr_b16 v[36:37], v17 offset:33792\n"
"ds_read_b64_tr_b16 v[126:127], v16 offset:2048\n"
"ds_read_b64_tr_b16 v[128:129], v16 offset:3072\n"
"ds_read_b64_tr_b16 v[130:131], v17 offset:2048\n"
"ds_read_b64_tr_b16 v[132:133], v17 offset:3072\n"
"s_waitcnt lgkmcnt(8) vmcnt(11)\n"
"v_mfma_f32_16x16x32_bf16 v[152:155], v[22:25], v[90:93], 0\n"
"ds_read_b64_tr_b16 v[134:135], v16 offset:34816\n"
"ds_read_b64_tr_b16 v[136:137], v16 offset:35840\n"
"s_xor_b32 s95, s94, 18432\n"
"buffer_load_dwordx4 v[192:195], v3, s[84:87], s95 offen  sc1\n"
"v_mfma_f32_16x16x32_bf16 v[70:73], v[26:29], v[90:93], 0\n"
"ds_read_b64_tr_b16 v[138:139], v17 offset:34816\n"
"ds_read_b64_tr_b16 v[140:141], v17 offset:35840\n"
"s_waitcnt lgkmcnt(8)\n"
"v_mfma_f32_16x16x32_bf16 v[74:77], v[90:93], v[30:33], 0\n"
"ds_read_b64_tr_b16 v[22:23], v16 offset:4096\n"
"ds_read_b64_tr_b16 v[24:25], v16 offset:5120\n"
"v_mfma_f32_16x16x32_bf16 v[78:81], v[90:93], v[34:37], 0\n"
"ds_read_b64_tr_b16 v[26:27], v17 offset:4096\n"
"ds_read_b64_tr_b16 v[28:29], v17 offset:5120\n"
"buffer_load_dwordx4 v[196:199], v3, s[84:87], s95 offen offset:1024 sc1\n"
"s_waitcnt lgkmcnt(8) vmcnt(12)\n"
"v_mfma_f32_16x16x32_bf16 v[152:155], v[126:129], v[94:97], v[152:155]\n"
"ds_read_b64_tr_b16 v[30:31], v16 offset:36864\n"
"ds_read_b64_tr_b16 v[32:33], v16 offset:37888\n"
"v_mfma_f32_16x16x32_bf16 v[70:73], v[130:133], v[94:97], v[70:73]\n"
"ds_read_b64_tr_b16 v[34:35], v17 offset:36864\n"
"ds_read_b64_tr_b16 v[36:37], v17 offset:37888\n"
"s_waitcnt lgkmcnt(8)\n"
"v_mfma_f32_16x16x32_bf16 v[74:77], v[94:97], v[134:137], v[74:77]\n"
"ds_read_b64_tr_b16 v[126:127], v16 offset:6144\n"
"ds_read_b64_tr_b16 v[128:129], v16 offset:7168\n"
"s_xor_b32 s95, s94, 20480\n"
"buffer_load_dwordx4 v[200:203], v3, s[84:87], s95 offen  sc1\n"
"s_waitcnt vmcnt(12)\n"
"v_add_f32 v216, v216, v218\n"
"v_mfma_f32_16x16x32_bf16 v[78:81], v[94:97], v[138:141], v[78:81]\n"
"ds_read_b64_tr_b16 v[130:131], v17 offset:6144\n"
"ds_read_b64_tr_b16 v[132:133], v17 offset:7168\n"
"v_add_f32 v217, v217, v219\n"
"s_waitcnt lgkmcnt(8) vmcnt(10)\n"
"v_mfma_f32_16x16x32_bf16 v[152:155], v[22:25], v[98:101], v[152:155]\n"
"ds_read_b64_tr_b16 v[134:135], v16 offset:38912\n"
"ds_read_b64_tr_b16 v[136:137], v16 offset:39936\n"
"v_add_f32 v220, v220, v222\n"
"v_mfma_f32_16x16x32_bf16 v[70:73], v[26:29], v[98:101], v[70:73]\n"
"ds_read_b64_tr_b16 v[138:139], v17 offset:38912\n"
"ds_read_b64_tr_b16 v[140:141], v17 offset:39936\n"
"buffer_load_dwordx4 v[204:207], v3, s[84:87], s95 offen offset:1024 sc1\n"
"v_add_f32 v221, v221, v223\n"
"s_waitcnt lgkmcnt(8)\n"
"v_mfma_f32_16x16x32_bf16 v[74:77], v[98:101], v[30:33], v[74:77]\n"
"ds_read_b64_tr_b16 v[22:23], v16 offset:8192\n"
"ds_read_b64_tr_b16 v[24:25], v16 offset:9216\n"
"v_add_f32 v216, v216, v220\n"
"v_mfma_f32_16x16x32_bf16 v[78:81], v[98:101], v[34:37], v[78:81]\n"
"ds_read_b64_tr_b16 v[26:27], v17 offset:8192\n"
"ds_read_b64_tr_b16 v[28:29], v17 offset:9216\n"
"v_add_f32 v217, v217, v221\n"
"s_waitcnt lgkmcnt(8) vmcnt(10)\n"
"v_mfma_f32_16x16x32_bf16 v[152:155], v[126:129], v[102:105], v[152:155]\n"
"ds_read_b64_tr_b16 v[30:31], v16 offset:40960\n"
"ds_read_b64_tr_b16 v[32:33], v16 offset:41984\n"
"s_xor_b32 s95, s94, 22528\n"
"buffer_load_dwordx4 v[208:211], v3, s[84:87], s95 offen  sc1\n"
"v_permlane32_swap_b32 v216, v217\n"
"v_mfma_f32_16x16x32_bf16 v[70:73], v[130:133], v[102:105], v[70:73]\n"
"ds_read_b64_tr_b16 v[34:35], v17 offset:40960\n"
"ds_read_b64_tr_b16 v[36:37], v17 offset:41984\n"
"v_add_f32 v216, v216, v217\n"
"s_waitcnt lgkmcnt(8)\n"
"v_mfma_f32_16x16x32_bf16 v[74:77], v[102:105], v[134:137], v[74:77]\n"
"ds_read_b64_tr_b16 v[126:127], v16 offset:10240\n"
"ds_read_b64_tr_b16 v[128:129], v16 offset:11264\n"
"v_mov_b32 v217, v216\n"
"v_mfma_f32_16x16x32_bf16 v[78:81], v[102:105], v[138:141], v[78:81]\n"
"ds_read_b64_tr_b16 v[130:131], v17 offset:10240\n"
"ds_read_b64_tr_b16 v[132:133], v17 offset:11264\n"
"buffer_load_dwordx4 v[212:215], v3, s[84:87], s95 offen offset:1024 sc1\n"
"v_permlane16_swap_b32 v216, v217\n"
"s_waitcnt lgkmcnt(8) vmcnt(11)\n"
"v_mfma_f32_16x16x32_bf16 v[152:155], v[22:25], v[106:109], v[152:155]\n"
"ds_read_b64_tr_b16 v[134:135], v16 offset:43008\n"
"ds_read_b64_tr_b16 v[136:137], v16 offset:44032\n"
"v_add_f32 v216, v216, v217\n"
"v_mfma_f32_16x16x32_bf16 v[70:73], v[26:29], v[106:109], v[70:73]\n"
"ds_read_b64_tr_b16 v[138:139], v17 offset:43008\n"
"ds_read_b64_tr_b16 v[140:141], v17 offset:44032\n"
"v_mov_b32 v217, v216\n"
"s_waitcnt lgkmcnt(8)\n"
"v_mfma_f32_16x16x32_bf16 v[74:77], v[106:109], v[30:33], v[74:77]\n"
"ds_read_b64_tr_b16 v[22:23], v16 offset:12288\n"
"ds_read_b64_tr_b16 v[24:25], v16 offset:13312\n"
"s_add_u32 m0, s91, 90112\n"
"s_xor_b32 s8, s93, 256\n"
"s_mul_i32 s8, s8, 1536\n"
"s_add_u32 s8, s8, s88\n"
"buffer_load_dwordx4 v21, s[16:19], s8 offen lds\n"
"v_permlane32_swap_b32 v216, v217\n"
"v_mfma_f32_16x16x32_bf16 v[78:81], v[106:109], v[34:37], v[78:81]\n"
"ds_read_b64_tr_b16 v[26:27], v17 offset:12288\n"
"ds_read_b64_tr_b16 v[28:29], v17 offset:13312\n"
"v_mul_f32 v216, 0x3b000000, v216\n"
"s_waitcnt lgkmcnt(8) vmcnt(11)\n"
"v_mfma_f32_16x16x32_bf16 v[152:155], v[126:129], v[110:113], v[152:155]\n"
"ds_read_b64_tr_b16 v[30:31], v18\n"
"ds_read_b64_tr_b16 v[32:33], v18 offset:1024\n"
"v_mul_f32 v217, 0x3b000000, v217\n"
"v_mfma_f32_16x16x32_bf16 v[70:73], v[130:133], v[110:113], v[70:73]\n"
"ds_read_b64_tr_b16 v[34:35], v19\n"
"ds_read_b64_tr_b16 v[36:37], v19 offset:1024\n"
"s_add_u32 m0, s91, 94208\n"
"s_xor_b32 s9, s93, 384\n"
"s_mul_i32 s9, s9, 1536\n"
"s_add_u32 s9, s9, s88\n"
"buffer_load_dwordx4 v21, s[16:19], s9 offen lds\n"
"v_fma_f32 v217, -v216, v216, v217\n"
"s_waitcnt lgkmcnt(8)\n"
"v_mfma_f32_16x16x32_bf16 v[74:77], v[110:113], v[134:137], v[74:77]\n"
"ds_read_b64_tr_b16 v[126:127], v16 offset:14336\n"
"ds_read_b64_tr_b16 v[128:129], v16 offset:15360\n"
"v_max_f32 v217, 0, v217\n"
"v_mfma_f32_16x16x32_bf16 v[78:81], v[110:113], v[138:141], v[78:81]\n"
"ds_read_b64_tr_b16 v[130:131], v17 offset:14336\n"
"ds_read_b64_tr_b16 v[132:133], v17 offset:15360\n"
"v_add_f32 v217, 0x3727c5ac, v217\n"
"s_waitcnt lgkmcnt(8) vmcnt(11)\n"
"v_mfma_f32_16x16x32_bf16 v[152:155], v[22:25], v[176:179], v[152:155]\n"
"ds_read_b64_tr_b16 v[134:135], v18 offset:2048\n"
"ds_read_b64_tr_b16 v[136:137], v18 offset:3072\n"
"s_add_u32 m0, s91, 98304\n"
"s_xor_b32 s10, s93, 512\n"
"s_mul_i32 s10, s10, 1536\n"
"s_add_u32 s10, s10, s88\n"
"buffer_load_dwordx4 v21, s[16:19], s10 offen lds\n"
"v_rsq_f32 v217, v217\n"
"v_mfma_f32_16x16x32_bf16 v[70:73], v[26:29], v[176:179], v[70:73]\n"
"ds_read_b64_tr_b16 v[138:139], v19 offset:2048\n"
"ds_read_b64_tr_b16 v[140:141], v19 offset:3072\n"
"v_mul_f32 v143, 0x3fb8aa3b, v217\n"
"s_waitcnt lgkmcnt(8)\n"
"v_mfma_f32_16x16x32_bf16 v[74:77], v[176:179], v[30:33], v[74:77]\n"
"ds_read_b64_tr_b16 v[22:23], v16 offset:16384\n"
"ds_read_b64_tr_b16 v[24:25], v16 offset:17408\n"
"v_mul_f32 v216, 0xbe800000, v216\n"
"v_mfma_f32_16x16x32_bf16 v[78:81], v[176:179], v[34:37], v[78:81]\n"
"ds_read_b64_tr_b16 v[26:27], v17 offset:16384\n"
"ds_read_b64_tr_b16 v[28:29], v17 offset:17408\n"
"s_add_u32 m0, s91, 102400\n"
"s_xor_b32 s11, s93, 640\n"
"s_mul_i32 s11, s11, 1536\n"
"s_add_u32 s11, s11, s88\n"
"buffer_load_dwordx4 v21, s[16:19], s11 offen lds\n"
"v_mul_f32 v217, 0x3e000000, v217\n"
"s_waitcnt lgkmcnt(8) vmcnt(12)\n"
"v_mfma_f32_16x16x32_bf16 v[152:155], v[126:129], v[180:183], v[152:155]\n"
"ds_read_b64_tr_b16 v[30:31], v18 offset:4096\n"
"ds_read_b64_tr_b16 v[32:33], v18 offset:5120\n"
"v_cvt_pk_bf16_f32 v146, v216, v216\n"
"v_mfma_f32_16x16x32_bf16 v[70:73], v[130:133], v[180:183], v[70:73]\n"
"ds_read_b64_tr_b16 v[34:35], v19 offset:4096\n"
"ds_read_b64_tr_b16 v[36:37], v19 offset:5120\n"
"v_cvt_pk_bf16_f32 v252, v217, v217\n"
"s_waitcnt lgkmcnt(8)\n"
"v_mfma_f32_16x16x32_bf16 v[74:77], v[180:183], v[134:137], v[74:77]\n"
"ds_read_b64_tr_b16 v[126:127], v16 offset:18432\n"
"ds_read_b64_tr_b16 v[128:129], v16 offset:19456\n"
"s_add_u32 m0, s91, 106496\n"
"s_xor_b32 s8, s93, 768\n"
"s_mul_i32 s8, s8, 1536\n"
"s_add_u32 s8, s8, s88\n"
"buffer_load_dwordx4 v21, s[16:19], s8 offen lds\n"
"v_and_b32 v249, 0xffff0000, v146\n"
"v_mfma_f32_16x16x32_bf16 v[78:81], v[180:183], v[138:141], v[78:81]\n"
"ds_read_b64_tr_b16 v[130:131], v17 offset:18432\n"
"ds_read_b64_tr_b16 v[132:133], v17 offset:19456\n"
"v_and_b32 v250, 0xffff0000, v252\n"
"s_waitcnt lgkmcnt(8) vmcnt(12)\n"
"v_mfma_f32_16x16x32_bf16 v[152:155], v[22:25], v[184:187], v[152:155]\n"
"ds_read_b64_tr_b16 v[134:135], v18 offset:6144\n"
"ds_read_b64_tr_b16 v[136:137], v18 offset:7168\n"
"v_sub_f32 v249, v216, v249\n"
"v_mfma_f32_16x16x32_bf16 v[70:73], v[26:29], v[184:187], v[70:73]\n"
"ds_read_b64_tr_b16 v[138:139], v19 offset:6144\n"
"ds_read_b64_tr_b16 v[140:141], v19 offset:7168\n"
"s_add_u32 m0, s91, 110592\n"
"s_xor_b32 s9, s93, 896\n"
"s_mul_i32 s9, s9, 1536\n"
"s_add_u32 s9, s9, s88\n"
"buffer_load_dwordx4 v21, s[16:19], s9 offen lds\n"
"v_sub_f32 v250, v217, v250\n"
"s_waitcnt lgkmcnt(8)\n"
"v_mfma_f32_16x16x32_bf16 v[74:77], v[184:187], v[30:33], v[74:77]\n"
"ds_read_b64_tr_b16 v[22:23], v16 offset:20480\n"
"ds_read_b64_tr_b16 v[24:25], v16 offset:21504\n"
"v_cvt_pk_bf16_f32 v147, v249, v249\n"
"v_mfma_f32_16x16x32_bf16 v[78:81], v[184:187], v[34:37], v[78:81]\n"
"ds_read_b64_tr_b16 v[26:27], v17 offset:20480\n"
"ds_read_b64_tr_b16 v[28:29], v17 offset:21504\n"
"v_cvt_pk_bf16_f32 v253, v250, v250\n"
"s_waitcnt lgkmcnt(8) vmcnt(12)\n"
"v_mfma_f32_16x16x32_bf16 v[152:155], v[126:129], v[188:191], v[152:155]\n"
"ds_read_b64_tr_b16 v[30:31], v18 offset:8192\n"
"ds_read_b64_tr_b16 v[32:33], v18 offset:9216\n"
"v_mfma_f32_16x16x16_bf16 v[148:151], v[252:253], a[228:229], 0\n"
"v_mfma_f32_16x16x32_bf16 v[70:73], v[130:133], v[188:191], v[70:73]\n"
"ds_read_b64_tr_b16 v[34:35], v19 offset:8192\n"
"ds_read_b64_tr_b16 v[36:37], v19 offset:9216\n"
"s_waitcnt lgkmcnt(8)\n"
"v_mfma_f32_16x16x32_bf16 v[74:77], v[188:191], v[134:137], v[74:77]\n"
"ds_read_b64_tr_b16 v[126:127], v16 offset:22528\n"
"ds_read_b64_tr_b16 v[128:129], v16 offset:23552\n"
"v_mfma_f32_16x16x32_bf16 v[78:81], v[188:191], v[138:141], v[78:81]\n"
"ds_read_b64_tr_b16 v[130:131], v17 offset:22528\n"
"ds_read_b64_tr_b16 v[132:133], v17 offset:23552\n"
"s_waitcnt lgkmcnt(8) vmcnt(11)\n"
"v_mfma_f32_16x16x32_bf16 v[152:155], v[22:25], v[192:195], v[152:155]\n"
"ds_read_b64_tr_b16 v[134:135], v18 offset:10240\n"
"ds_read_b64_tr_b16 v[136:137], v18 offset:11264\n"
"v_mfma_f32_16x16x32_bf16 v[70:73], v[26:29], v[192:195], v[70:73]\n"
"ds_read_b64_tr_b16 v[138:139], v19 offset:10240\n"
"ds_read_b64_tr_b16 v[140:141], v19 offset:11264\n"
"s_waitcnt lgkmcnt(8)\n"
"v_mfma_f32_16x16x32_bf16 v[74:77], v[192:195], v[30:33], v[74:77]\n"
"ds_read_b64_tr_b16 v[22:23], v16 offset:24576\n"
"ds_read_b64_tr_b16 v[24:25], v16 offset:25600\n"
"v_mfma_f32_16x16x32_bf16 v[78:81], v[192:195], v[34:37], v[78:81]\n"
"ds_read_b64_tr_b16 v[26:27], v17 offset:24576\n"
"ds_read_b64_tr_b16 v[28:29], v17 offset:25600\n"
"s_waitcnt lgkmcnt(8) vmcnt(10)\n"
"v_mfma_f32_16x16x32_bf16 v[152:155], v[126:129], v[196:199], v[152:155]\n"
"ds_read_b64_tr_b16 v[30:31], v18 offset:12288\n"
"ds_read_b64_tr_b16 v[32:33], v18 offset:13312\n"
"v_mfma_f32_16x16x32_bf16 v[70:73], v[130:133], v[196:199], v[70:73]\n"
"ds_read_b64_tr_b16 v[34:35], v19 offset:12288\n"
"ds_read_b64_tr_b16 v[36:37], v19 offset:13312\n"
"s_waitcnt lgkmcnt(8)\n"
"v_mfma_f32_16x16x32_bf16 v[74:77], v[196:199], v[134:137], v[74:77]\n"
"ds_read_b64_tr_b16 v[126:127], v16 offset:26624\n"
"ds_read_b64_tr_b16 v[128:129], v16 offset:27648\n"
"v_mfma_f32_16x16x32_bf16 v[78:81], v[196:199], v[138:141], v[78:81]\n"
"ds_read_b64_tr_b16 v[130:131], v17 offset:26624\n"
"ds_read_b64_tr_b16 v[132:133], v17 offset:27648\n"
"s_waitcnt lgkmcnt(8) vmcnt(9)\n"
"v_mfma_f32_16x16x32_bf16 v[152:155], v[22:25], v[200:203], v[152:155]\n"
"ds_read_b64_tr_b16 v[134:135], v18 offset:14336\n"
"ds_read_b64_tr_b16 v[136:137], v18 offset:15360\n"
"v_mfma_f32_16x16x32_bf16 v[70:73], v[26:29], v[200:203], v[70:73]\n"
"ds_read_b64_tr_b16 v[138:139], v19 offset:14336\n"
"ds_read_b64_tr_b16 v[140:141], v19 offset:15360\n"
"s_waitcnt lgkmcnt(8)\n"
"v_mfma_f32_16x16x32_bf16 v[74:77], v[200:203], v[30:33], v[74:77]\n"
"ds_read_b64_tr_b16 v[22:23], v16 offset:28672\n"
"ds_read_b64_tr_b16 v[24:25], v16 offset:29696\n"
"v_mfma_f32_16x16x32_bf16 v[78:81], v[200:203], v[34:37], v[78:81]\n"
"ds_read_b64_tr_b16 v[26:27], v17 offset:28672\n"
"ds_read_b64_tr_b16 v[28:29], v17 offset:29696\n"
"s_waitcnt lgkmcnt(8) vmcnt(8)\n"
"v_mfma_f32_16x16x32_bf16 v[152:155], v[126:129], v[204:207], v[152:155]\n"
"ds_read_b64_tr_b16 v[30:31], v18 offset:16384\n"
"ds_read_b64_tr_b16 v[32:33], v18 offset:17408\n"
"v_mfma_f32_16x16x32_bf16 v[70:73], v[130:133], v[204:207], v[70:73]\n"
"ds_read_b64_tr_b16 v[34:35], v19 offset:16384\n"
"ds_read_b64_tr_b16 v[36:37], v19 offset:17408\n"
"s_waitcnt lgkmcnt(8)\n"
"v_mfma_f32_16x16x32_bf16 v[74:77], v[204:207], v[134:137], v[74:77]\n"
"ds_read_b64_tr_b16 v[126:127], v16 offset:30720\n"
"ds_read_b64_tr_b16 v[128:129], v16 offset:31744\n"
"v_mfma_f32_16x16x32_bf16 v[78:81], v[204:207], v[138:141], v[78:81]\n"
"ds_read_b64_tr_b16 v[130:131], v17 offset:30720\n"
"ds_read_b64_tr_b16 v[132:133], v17 offset:31744\n"
"s_waitcnt lgkmcnt(8) vmcnt(7)\n"
"v_mfma_f32_16x16x32_bf16 v[152:155], v[22:25], v[208:211], v[152:155]\n"
"ds_read_b64_tr_b16 v[134:135], v18 offset:18432\n"
"ds_read_b64_tr_b16 v[136:137], v18 offset:19456\n"
"v_mfma_f32_16x16x32_bf16 v[70:73], v[26:29], v[208:211], v[70:73]\n"
"ds_read_b64_tr_b16 v[138:139], v19 offset:18432\n"
"ds_read_b64_tr_b16 v[140:141], v19 offset:19456\n"
"s_waitcnt lgkmcnt(8)\n"
"v_mfma_f32_16x16x32_bf16 v[74:77], v[208:211], v[30:33], v[74:77]\n"
"v_mfma_f32_16x16x32_bf16 v[78:81], v[208:211], v[34:37], v[78:81]\n"
"v_mfma_f32_16x16x16_bf16 v[152:155], a[242:243], v[146:147], v[152:155]\n"
"s_nop 3\n"
"v_mfma_f32_16x16x16_bf16 v[74:77], v[146:147], a[246:247], v[74:77]\n"
"v_mfma_f32_16x16x16_bf16 v[70:73], a[244:245], v[146:147], v[70:73]\n"
"v_mfma_f32_16x16x16_bf16 v[78:81], v[146:147], a[248:249], v[78:81]\n"
"s_waitcnt lgkmcnt(4) vmcnt(6)\n"
"v_mfma_f32_16x16x32_bf16 v[152:155], v[126:129], v[212:215], v[152:155]\n"
"s_nop 2\n"
"v_mfma_f32_16x16x32_bf16 v[70:73], v[130:133], v[212:215], v[70:73]\n"
"s_waitcnt lgkmcnt(0)\n"
"v_mfma_f32_16x16x32_bf16 v[74:77], v[212:215], v[134:137], v[74:77]\n"
"v_mfma_f32_16x16x32_bf16 v[78:81], v[212:215], v[138:141], v[78:81]\n"
"s_nop 0\n"
"v_cvt_pk_bf16_f32 v248, v152, v153\n"
"v_cvt_pk_bf16_f32 v249, v154, v155\n"
"s_nop 1\n"
"v_cvt_pk_bf16_f32 v216, v70, v71\n"
"v_cvt_pk_bf16_f32 v217, v72, v73\n"
"v_mfma_f32_16x16x16_bf16 v[152:155], a[224:225], v[248:249], 0\n"
"s_nop 0\n"
"v_mfma_f32_16x16x16_bf16 v[70:73], a[226:227], v[216:217], 0\n"
"v_pk_mul_f32 v[74:75], v[74:75], v[148:149] op_sel:[0,0] op_sel_hi:[1,1]\n"
"v_pk_mul_f32 v[76:77], v[76:77], v[150:151] op_sel:[0,0] op_sel_hi:[1,1]\n"
"v_pk_mul_f32 v[78:79], v[78:79], v[148:149] op_sel:[0,0] op_sel_hi:[1,1]\n"
"v_pk_mul_f32 v[80:81], v[80:81], v[150:151] op_sel:[0,0] op_sel_hi:[1,1]\n"
"v_cvt_pk_bf16_f32 v226, v74, v75\n"
"v_cvt_pk_bf16_f32 v227, v76, v77\n"
"v_cvt_pk_bf16_f32 v230, v78, v79\n"
"v_cvt_pk_bf16_f32 v231, v80, v81\n"
"s_barrier\n"
"v_max3_f32 v20, v152, v153, v154\n"
"v_max3_f32 v21, -v152, -v153, -v154\n"
"v_max3_f32 v252, v70, v71, v72\n"
"v_max3_f32 v253, -v70, -v71, -v72\n"
"v_max_f32 v20, v20, v155\n"
"v_max_f32 v21, v21, -v155\n"
"v_max_f32 v252, v252, v73\n"
"v_max_f32 v253, v253, -v73\n"
"v_permlane32_swap_b32 v20, v21\n"
"s_nop 0\n"
"v_permlane32_swap_b32 v252, v253\n"
"v_max_f32 v20, v20, v21\n"
"v_max_f32 v252, v252, v253\n"
"s_nop 1\n"
"v_permlane16_swap_b32 v20, v252\n"
"v_max_f32 v20, v20, v252\n"
"v_mul_f32 v20, -v20, v143\n"
"v_mov_b32 v252, v20\n"
"s_nop 1\n"
"v_permlane16_swap_b32 v20, v252\n"
"v_mov_b32 v21, v20\n"
"v_mov_b32 v253, v252\n"
"s_nop 0\n"
"v_permlane32_swap_b32 v20, v21\n"
"v_permlane32_swap_b32 v252, v253\n"
"v_pk_fma_f32 v[220:221], v[152:153], v[142:143], v[20:21] op_sel:[0,1,1] op_sel_hi:[1,1,1] neg_lo:[1,0,0] neg_hi:[1,0,0]\n"
"v_pk_fma_f32 v[152:153], v[152:153], v[142:143], v[20:21] op_sel:[0,1,0] op_sel_hi:[1,1,0]\n"
"v_pk_fma_f32 v[222:223], v[154:155], v[142:143], v[20:21] op_sel:[0,1,1] op_sel_hi:[1,1,1] neg_lo:[1,0,0] neg_hi:[1,0,0]\n"
"v_pk_fma_f32 v[154:155], v[154:155], v[142:143], v[20:21] op_sel:[0,1,0] op_sel_hi:[1,1,0]\n"
"v_pk_fma_f32 v[232:233], v[70:71], v[142:143], v[252:253] op_sel:[0,1,1] op_sel_hi:[1,1,1] neg_lo:[1,0,0] neg_hi:[1,0,0]\n"
"v_pk_fma_f32 v[70:71], v[70:71], v[142:143], v[252:253] op_sel:[0,1,0] op_sel_hi:[1,1,0]\n"
"v_pk_fma_f32 v[234:235], v[72:73], v[142:143], v[252:253] op_sel:[0,1,1] op_sel_hi:[1,1,1] neg_lo:[1,0,0] neg_hi:[1,0,0]\n"
"v_pk_fma_f32 v[72:73], v[72:73], v[142:143], v[252:253] op_sel:[0,1,0] op_sel_hi:[1,1,0]\n"
"v_exp_f32 v220, v220\n"
"v_exp_f32 v152, v152\n"
"v_exp_f32 v221, v221\n"
"v_exp_f32 v153, v153\n"
"v_exp_f32 v222, v222\n"
"v_exp_f32 v154, v154\n"
"v_exp_f32 v223, v223\n"
"v_exp_f32 v155, v155\n"
"v_exp_f32 v232, v232\n"
"v_exp_f32 v70, v70\n"
"v_exp_f32 v233, v233\n"
"v_exp_f32 v71, v71\n"
"v_exp_f32 v234, v234\n"
"v_exp_f32 v72, v72\n"
"v_exp_f32 v235, v235\n"
"v_exp_f32 v73, v73\n"
"v_pk_add_f32 v[248:249], v[152:153], v[154:155] op_sel:[0,0] op_sel_hi:[1,1]\n"
"v_pk_add_f32 v[250:251], v[220:221], v[222:223] op_sel:[0,0] op_sel_hi:[1,1]\n"
"v_pk_add_f32 v[216:217], v[70:71], v[72:73] op_sel:[0,0] op_sel_hi:[1,1]\n"
"v_pk_add_f32 v[218:219], v[232:233], v[234:235] op_sel:[0,0] op_sel_hi:[1,1]\n"
"v_add_f32 v20, v248, v249\n"
"v_add_f32 v21, v250, v251\n"
"v_add_f32 v252, v216, v217\n"
"v_add_f32 v253, v218, v219\n"
"v_permlane32_swap_b32 v20, v21\n"
"s_nop 0\n"
"v_permlane32_swap_b32 v252, v253\n"
"v_add_f32 v20, v20, v21\n"
"v_add_f32 v252, v252, v253\n"
"s_nop 1\n"
"v_permlane16_swap_b32 v20, v252\n"
"v_add_f32 v20, v20, v252\n"
"v_rcp_f32 v20, v20\n"
"s_nop 0\n"
"v_mov_b32 v252, v20\n"
"s_nop 1\n"
"v_permlane16_swap_b32 v20, v252\n"
"v_mov_b32 v21, v20\n"
"v_mov_b32 v253, v252\n"
"s_nop 0\n"
"v_permlane32_swap_b32 v20, v21\n"
"v_permlane32_swap_b32 v252, v253\n"
"v_pk_mul_f32 v[152:153], v[152:153], v[20:21] op_sel:[0,0] op_sel_hi:[1,0]\n"
"v_pk_mul_f32 v[220:221], v[220:221], v[20:21] op_sel:[0,1] op_sel_hi:[1,1]\n"
"v_pk_mul_f32 v[154:155], v[154:155], v[20:21] op_sel:[0,0] op_sel_hi:[1,0]\n"
"v_pk_mul_f32 v[222:223], v[222:223], v[20:21] op_sel:[0,1] op_sel_hi:[1,1]\n"
"v_pk_mul_f32 v[70:71], v[70:71], v[252:253] op_sel:[0,0] op_sel_hi:[1,0]\n"
"v_pk_mul_f32 v[232:233], v[232:233], v[252:253] op_sel:[0,1] op_sel_hi:[1,1]\n"
"v_pk_mul_f32 v[72:73], v[72:73], v[252:253] op_sel:[0,0] op_sel_hi:[1,0]\n"
"v_pk_mul_f32 v[234:235], v[234:235], v[252:253] op_sel:[0,1] op_sel_hi:[1,1]\n"
"v_cvt_pk_bf16_f32 v248, v152, v153\n"
"v_cvt_pk_bf16_f32 v249, v154, v155\n"
"v_cvt_pk_bf16_f32 v250, v220, v221\n"
"v_cvt_pk_bf16_f32 v251, v222, v223\n"
"v_cvt_pk_bf16_f32 v216, v70, v71\n"
"v_cvt_pk_bf16_f32 v217, v72, v73\n"
"v_cvt_pk_bf16_f32 v218, v232, v233\n"
"v_cvt_pk_bf16_f32 v219, v234, v235\n"
"v_mfma_f32_16x16x16_bf16 v[152:155], v[248:249], a[232:233], 0\n"
"v_mfma_f32_16x16x16_bf16 v[70:73], v[250:251], a[232:233], 0\n"
"v_mfma_f32_16x16x16_bf16 v[74:77], v[216:217], a[232:233], 0\n"
"v_mfma_f32_16x16x16_bf16 v[78:81], v[218:219], a[232:233], 0\n"
"s_nop 4\n"
"v_cvt_pk_bf16_f32 v84, v152, v153\n"
"v_cvt_pk_bf16_f32 v85, v154, v155\n"
"v_cvt_pk_bf16_f32 v88, v70, v71\n"
"v_cvt_pk_bf16_f32 v89, v72, v73\n"
"v_cvt_pk_bf16_f32 v242, v74, v75\n"
"v_cvt_pk_bf16_f32 v243, v76, v77\n"
"v_cvt_pk_bf16_f32 v246, v78, v79\n"
"v_cvt_pk_bf16_f32 v247, v80, v81\n"
"s_waitcnt vmcnt(0)\n"
"s_lshl_b32 s63, s34, 12\n"
"s_add_u32 s63, s63, 61440\n"
"v_add_u32 v20, s63, v3\n"
"s_add_u32 s63, s91, 77824\n"
"v_add_u32 v21, s63, v3\n"
"v_mfma_f32_16x16x32_bf16 v[152:155], v[82:85], v[224:227], 0\n"
"v_mfma_f32_16x16x32_bf16 v[70:73], v[86:89], v[224:227], 0\n"
"v_mfma_f32_16x16x32_bf16 v[74:77], v[240:243], v[228:231], 0\n"
"v_mfma_f32_16x16x32_bf16 v[78:81], v[244:247], v[228:231], 0\n"
"v_mfma_f32_16x16x16_bf16 v[232:235], v[82:83], a[234:235], 0\n"
"v_mfma_f32_16x16x16_bf16 v[232:235], v[84:85], a[234:235], v[232:235]\n"
"v_mfma_f32_16x16x16_bf16 v[232:235], v[86:87], a[236:237], v[232:235]\n"
"v_mfma_f32_16x16x16_bf16 v[232:235], v[88:89], a[236:237], v[232:235]\n"
"v_mfma_f32_16x16x16_bf16 v[232:235], v[240:241], a[238:239], v[232:235]\n"
"v_mfma_f32_16x16x16_bf16 v[232:235], v[242:243], a[238:239], v[232:235]\n"
"v_mfma_f32_16x16x16_bf16 v[232:235], v[244:245], a[240:241], v[232:235]\n"
"v_mfma_f32_16x16x16_bf16 v[232:235], v[246:247], a[240:241], v[232:235]\n"
"ds_write_b128 v20, v[152:155]\n"
"ds_write_b128 v20, v[70:73] offset:1024\n"
"ds_write_b128 v20, v[74:77] offset:2048\n"
"ds_write_b128 v20, v[78:81] offset:3072\n"
"s_nop 3\n"
"ds_write_b128 v21, v[232:235]\n"
"s_add_u32 s63, s91, 61440\n"
"v_add_u32 v20, s63, v3\n"
"s_lshl_b32 s63, s34, 4\n"
"s_add_u32 s63, s63, 77824\n"
"v_lshl_add_u32 v21, v1, 8, s63\n"
"s_lshl2_add_u32 s63, s56, s34\n"
"s_lshl4_add_u32 s63, s63, s35\n"
"s_lshl_b32 s68, s63, 6\n"
"s_add_u32 s68, s68, 131072\n"
"s_lshl_b32 s63, s63, 9\n"
"s_add_u32 s63, s63, 2097152\n"
"v_lshrrev_b32 v252, 1, v1\n"
"v_lshl_add_u32 v252, v252, 4, v2\n"
"v_lshl_add_u32 v252, v252, 1, v1\n"
"v_lshlrev_b32 v252, 3, v252\n"
"v_and_b32 v248, 2, v1\n"
"v_lshlrev_b32 v248, 3, v248\n"
"v_sub_u32 v252, v252, v248\n"
"s_waitcnt lgkmcnt(0)\n"
"s_barrier\n"
"ds_read_b128 v[152:155], v20\n"
"ds_read_b128 v[70:73], v20 offset:4096\n"
"ds_read_b128 v[74:77], v20 offset:8192\n"
"ds_read_b128 v[78:81], v20 offset:12288\n"
"ds_read_b128 v[248:251], v21\n"
"ds_read_b128 v[216:219], v21 offset:1024\n"
"ds_read_b128 v[148:151], v21 offset:2048\n"
"ds_read_b128 v[220:223], v21 offset:3072\n"
"v_lshlrev_b32 v21, 4, v1\n"
"v_mov_b32 v20, 2147483392\n"
"v_cmp_eq_u32 vcc, 0, v2\n"
"s_nop 1\n"
"v_cndmask_b32 v21, v20, v21, vcc\n"
"s_waitcnt lgkmcnt(6)\n"
"v_pk_add_f32 v[152:153], v[152:153], v[70:71] op_sel:[0,0] op_sel_hi:[1,1]\n"
"s_waitcnt lgkmcnt(4)\n"
"v_pk_add_f32 v[74:75], v[74:75], v[78:79] op_sel:[0,0] op_sel_hi:[1,1]\n"
"v_pk_add_f32 v[154:155], v[154:155], v[72:73] op_sel:[0,0] op_sel_hi:[1,1]\n"
"v_pk_add_f32 v[76:77], v[76:77], v[80:81] op_sel:[0,0] op_sel_hi:[1,1]\n"
"v_pk_add_f32 v[152:153], v[152:153], v[74:75] op_sel:[0,0] op_sel_hi:[1,1]\n"
"v_pk_add_f32 v[154:155], v[154:155], v[76:77] op_sel:[0,0] op_sel_hi:[1,1]\n"
"v_cvt_pk_bf16_f32 v70, v152, v153\n"
"v_cvt_pk_bf16_f32 v71, v154, v155\n"
"buffer_store_dwordx2 v[70:71], v252, s[52:55], s63 offen  sc1\n"
"s_waitcnt lgkmcnt(2)\n"
"v_pk_add_f32 v[248:249], v[248:249], v[216:217] op_sel:[0,0] op_sel_hi:[1,1]\n"
"s_waitcnt lgkmcnt(0)\n"
"v_pk_add_f32 v[148:149], v[148:149], v[220:221] op_sel:[0,0] op_sel_hi:[1,1]\n"
"v_pk_add_f32 v[250:251], v[250:251], v[218:219] op_sel:[0,0] op_sel_hi:[1,1]\n"
"v_pk_add_f32 v[150:151], v[150:151], v[222:223] op_sel:[0,0] op_sel_hi:[1,1]\n"
"v_pk_add_f32 v[248:249], v[248:249], v[148:149] op_sel:[0,0] op_sel_hi:[1,1]\n"
"v_pk_add_f32 v[250:251], v[250:251], v[150:151] op_sel:[0,0] op_sel_hi:[1,1]\n"
"buffer_store_dwordx4 v[248:251], v21, s[52:55], s68 offen  sc1\n"
"s_mul_i32 s63, s58, 72\n"
"s_lshl2_add_u32 s63, s34, s63\n"
"s_lshl_b32 s63, s63, 12\n"
"s_add_u32 s63, s63, 5242880\n"
"s_add_u32 s12, s63, s82\n"
"s_add_u32 s13, s63, s83\n"
"ds_read_b64_tr_b16 v[22:23], v18 offset:20480\n"
"ds_read_b64_tr_b16 v[24:25], v18 offset:21504\n"
"ds_read_b64_tr_b16 v[26:27], v19 offset:20480\n"
"ds_read_b64_tr_b16 v[28:29], v19 offset:21504\n"
"ds_read_b64_tr_b16 v[30:31], v18 offset:22528\n"
"ds_read_b64_tr_b16 v[32:33], v18 offset:23552\n"
"ds_read_b64_tr_b16 v[34:35], v19 offset:22528\n"
"ds_read_b64_tr_b16 v[36:37], v19 offset:23552\n"
"ds_read_b64_tr_b16 v[126:127], v18 offset:24576\n"
"ds_read_b64_tr_b16 v[128:129], v18 offset:25600\n"
"ds_read_b64_tr_b16 v[130:131], v19 offset:24576\n"
"ds_read_b64_tr_b16 v[132:133], v19 offset:25600\n"
"s_waitcnt lgkmcnt(8)\n"
"v_mfma_f32_16x16x32_bf16 v[152:155], v[22:25], v[114:117], 0\n"
"ds_read_b64_tr_b16 v[134:135], v18 offset:26624\n"
"v_mfma_f32_16x16x32_bf16 v[74:77], v[22:25], v[90:93], 0\n"
"ds_read_b64_tr_b16 v[136:137], v18 offset:27648\n"
"v_mfma_f32_16x16x32_bf16 v[70:73], v[26:29], v[114:117], 0\n"
"ds_read_b64_tr_b16 v[138:139], v19 offset:26624\n"
"v_mfma_f32_16x16x32_bf16 v[78:81], v[26:29], v[90:93], 0\n"
"ds_read_b64_tr_b16 v[140:141], v19 offset:27648\n"
"s_waitcnt lgkmcnt(8)\n"
"v_mfma_f32_16x16x32_bf16 v[152:155], v[30:33], v[118:121], v[152:155]\n"
"ds_read_b64_tr_b16 v[22:23], v18 offset:28672\n"
"v_mfma_f32_16x16x32_bf16 v[74:77], v[30:33], v[94:97], v[74:77]\n"
"ds_read_b64_tr_b16 v[24:25], v18 offset:29696\n"
"buffer_load_dwordx4 v[114:117], v3, s[52:55], s12 offen  sc1\n"
"v_mfma_f32_16x16x32_bf16 v[70:73], v[34:37], v[118:121], v[70:73]\n"
"ds_read_b64_tr_b16 v[26:27], v19 offset:28672\n"
"v_mfma_f32_16x16x32_bf16 v[78:81], v[34:37], v[94:97], v[78:81]\n"
"ds_read_b64_tr_b16 v[28:29], v19 offset:29696\n"
"s_waitcnt lgkmcnt(8)\n"
"v_mfma_f32_16x16x32_bf16 v[152:155], v[126:129], v[122:125], v[152:155]\n"
"ds_read_b64_tr_b16 v[30:31], v18 offset:30720\n"
"v_mfma_f32_16x16x32_bf16 v[74:77], v[126:129], v[98:101], v[74:77]\n"
"ds_read_b64_tr_b16 v[32:33], v18 offset:31744\n"
"buffer_load_dwordx4 v[118:121], v3, s[52:55], s12 offen offset:1024 sc1\n"
"v_mfma_f32_16x16x32_bf16 v[70:73], v[130:133], v[122:125], v[70:73]\n"
"ds_read_b64_tr_b16 v[34:35], v19 offset:30720\n"
"v_mfma_f32_16x16x32_bf16 v[78:81], v[130:133], v[98:101], v[78:81]\n"
"ds_read_b64_tr_b16 v[36:37], v19 offset:31744\n"
"s_waitcnt lgkmcnt(8)\n"
"v_mfma_f32_16x16x32_bf16 v[152:155], v[134:137], v[38:41], v[152:155]\n"
"ds_read_b64_tr_b16 v[126:127], v18 offset:32768\n"
"v_mfma_f32_16x16x32_bf16 v[74:77], v[134:137], v[102:105], v[74:77]\n"
"ds_read_b64_tr_b16 v[128:129], v18 offset:33792\n"
"buffer_load_dwordx4 v[122:125], v3, s[52:55], s13 offen  sc1\n"
"v_mfma_f32_16x16x32_bf16 v[70:73], v[138:141], v[38:41], v[70:73]\n"
"ds_read_b64_tr_b16 v[130:131], v19 offset:32768\n"
"v_mfma_f32_16x16x32_bf16 v[78:81], v[138:141], v[102:105], v[78:81]\n"
"ds_read_b64_tr_b16 v[132:133], v19 offset:33792\n"
"s_waitcnt lgkmcnt(8)\n"
"v_mfma_f32_16x16x32_bf16 v[152:155], v[22:25], v[42:45], v[152:155]\n"
"L_pad10_%=:\n"
"s_cbranch_execz L_ret_%=\n"
"ds_read_b64_tr_b16 v[134:135], v18 offset:34816\n"
"v_mfma_f32_16x16x32_bf16 v[74:77], v[22:25], v[106:109], v[74:77]\n"
"ds_read_b64_tr_b16 v[136:137], v18 offset:35840\n"
"buffer_load_dwordx4 v[38:41], v3, s[52:55], s13 offen offset:1024 sc1\n"
"v_mfma_f32_16x16x32_bf16 v[70:73], v[26:29], v[42:45], v[70:73]\n"
"ds_read_b64_tr_b16 v[138:139], v19 offset:34816\n"
"v_mfma_f32_16x16x32_bf16 v[78:81], v[26:29], v[106:109], v[78:81]\n"
"ds_read_b64_tr_b16 v[140:141], v19 offset:35840\n"
"s_waitcnt lgkmcnt(8)\n"
"v_mfma_f32_16x16x32_bf16 v[152:155], v[30:33], v[46:49], v[152:155]\n"
"ds_read_b64_tr_b16 v[22:23], v18 offset:36864\n"
"v_mfma_f32_16x16x32_bf16 v[74:77], v[30:33], v[110:113], v[74:77]\n"
"ds_read_b64_tr_b16 v[24:25], v18 offset:37888\n"
"s_add_u32 s10, s12, 4096\n"
"buffer_load_dwordx4 v[42:45], v3, s[52:55], s10 offen  sc1\n"
"v_mfma_f32_16x16x32_bf16 v[70:73], v[34:37], v[46:49], v[70:73]\n"
"ds_read_b64_tr_b16 v[26:27], v19 offset:36864\n"
"v_mfma_f32_16x16x32_bf16 v[78:81], v[34:37], v[110:113], v[78:81]\n"
"ds_read_b64_tr_b16 v[28:29], v19 offset:37888\n"
"s_waitcnt vmcnt(5)\n"
"v_mov_b32 v20, s60\n"
"v_mov_b32 v21, s61\n"
"s_add_u32 s11, s79, 524304\n"
"s_barrier\n"
"s_mov_b64 exec, 1\n"
"buffer_store_dwordx2 v[20:21], off, s[52:55], s11 sc1\n"
"s_mov_b64 exec, -1\n"
"s_waitcnt lgkmcnt(8)\n"
"v_mfma_f32_16x16x32_bf16 v[152:155], v[126:129], v[50:53], v[152:155]\n"
"ds_read_b64_tr_b16 v[30:31], v18 offset:38912\n"
"v_mfma_f32_16x16x32_bf16 v[74:77], v[126:129], v[176:179], v[74:77]\n"
"ds_read_b64_tr_b16 v[32:33], v18 offset:39936\n"
"s_add_u32 s8, s12, 4096\n"
"buffer_load_dwordx4 v[46:49], v3, s[52:55], s8 offen offset:1024 sc1\n"
"v_mfma_f32_16x16x32_bf16 v[70:73], v[130:133], v[50:53], v[70:73]\n"
"ds_read_b64_tr_b16 v[34:35], v19 offset:38912\n"
"v_mfma_f32_16x16x32_bf16 v[78:81], v[130:133], v[176:179], v[78:81]\n"
"ds_read_b64_tr_b16 v[36:37], v19 offset:39936\n"
"s_waitcnt lgkmcnt(8)\n"
"v_mfma_f32_16x16x32_bf16 v[152:155], v[134:137], v[54:57], v[152:155]\n"
"ds_read_b64_tr_b16 v[126:127], v18 offset:40960\n"
"v_mfma_f32_16x16x32_bf16 v[74:77], v[134:137], v[180:183], v[74:77]\n"
"ds_read_b64_tr_b16 v[128:129], v18 offset:41984\n"
"s_add_u32 s9, s13, 4096\n"
"buffer_load_dwordx4 v[50:53], v3, s[52:55], s9 offen  sc1\n"
"v_mfma_f32_16x16x32_bf16 v[70:73], v[138:141], v[54:57], v[70:73]\n"
"ds_read_b64_tr_b16 v[130:131], v19 offset:40960\n"
"v_mfma_f32_16x16x32_bf16 v[78:81], v[138:141], v[180:183], v[78:81]\n"
"ds_read_b64_tr_b16 v[132:133], v19 offset:41984\n"
"s_waitcnt lgkmcnt(8)\n"
"v_mfma_f32_16x16x32_bf16 v[152:155], v[22:25], v[58:61], v[152:155]\n"
"ds_read_b64_tr_b16 v[134:135], v18 offset:43008\n"
"v_mfma_f32_16x16x32_bf16 v[74:77], v[22:25], v[184:187], v[74:77]\n"
"ds_read_b64_tr_b16 v[136:137], v18 offset:44032\n"
"s_add_u32 s10, s13, 4096\n"
"buffer_load_dwordx4 v[54:57], v3, s[52:55], s10 offen offset:1024 sc1\n"
"v_mfma_f32_16x16x32_bf16 v[70:73], v[26:29], v[58:61], v[70:73]\n"
"ds_read_b64_tr_b16 v[138:139], v19 offset:43008\n"
"v_mfma_f32_16x16x32_bf16 v[78:81], v[26:29], v[184:187], v[78:81]\n"
"ds_read_b64_tr_b16 v[140:141], v19 offset:44032\n"
"s_waitcnt lgkmcnt(8)\n"
"v_mfma_f32_16x16x32_bf16 v[152:155], v[30:33], v[62:65], v[152:155]\n"
"ds_read_b64_tr_b16 v[22:23], v18 offset:45056\n"
"v_mfma_f32_16x16x32_bf16 v[74:77], v[30:33], v[188:191], v[74:77]\n"
"ds_read_b64_tr_b16 v[24:25], v18 offset:46080\n"
"s_add_u32 s11, s12, 8192\n"
"buffer_load_dwordx4 v[58:61], v3, s[52:55], s11 offen  sc1\n"
"v_mfma_f32_16x16x32_bf16 v[70:73], v[34:37], v[62:65], v[70:73]\n"
"ds_read_b64_tr_b16 v[26:27], v19 offset:45056\n"
"v_mfma_f32_16x16x32_bf16 v[78:81], v[34:37], v[188:191], v[78:81]\n"
"ds_read_b64_tr_b16 v[28:29], v19 offset:46080\n"
"v_mfma_f32_16x16x16_bf16 v[152:155], a[250:251], v[144:145], v[152:155]\n"
"s_nop 1\n"
"v_mfma_f32_16x16x16_bf16 v[70:73], a[252:253], v[144:145], v[70:73]\n"
"v_mfma_f32_16x16x16_bf16 v[74:77], a[250:251], v[146:147], v[74:77]\n"
"v_mfma_f32_16x16x16_bf16 v[78:81], a[252:253], v[146:147], v[78:81]\n"
"s_waitcnt lgkmcnt(8)\n"
"v_mfma_f32_16x16x32_bf16 v[152:155], v[126:129], v[66:69], v[152:155]\n"
"ds_read_b64_tr_b16 v[30:31], v18 offset:47104\n"
"s_nop 1\n"
"v_mfma_f32_16x16x32_bf16 v[74:77], v[126:129], v[192:195], v[74:77]\n"
"ds_read_b64_tr_b16 v[32:33], v18 offset:48128\n"
"s_add_u32 s8, s12, 8192\n"
"buffer_load_dwordx4 v[62:65], v3, s[52:55], s8 offen offset:1024 sc1\n"
"v_mfma_f32_16x16x32_bf16 v[70:73], v[130:133], v[66:69], v[70:73]\n"
"ds_read_b64_tr_b16 v[34:35], v19 offset:47104\n"
"v_mfma_f32_16x16x32_bf16 v[78:81], v[130:133], v[192:195], v[78:81]\n"
"ds_read_b64_tr_b16 v[36:37], v19 offset:48128\n"
"s_waitcnt lgkmcnt(8)\n"
"v_mfma_f32_16x16x32_bf16 v[152:155], v[134:137], v[156:159], v[152:155]\n"
"ds_read_b64_tr_b16 v[126:127], v18 offset:49152\n"
"v_mfma_f32_16x16x32_bf16 v[74:77], v[134:137], v[196:199], v[74:77]\n"
"ds_read_b64_tr_b16 v[128:129], v18 offset:50176\n"
"s_add_u32 s9, s13, 8192\n"
"buffer_load_dwordx4 v[66:69], v3, s[52:55], s9 offen  sc1\n"
"v_mfma_f32_16x16x32_bf16 v[70:73], v[138:141], v[156:159], v[70:73]\n"
"ds_read_b64_tr_b16 v[130:131], v19 offset:49152\n"
"v_mfma_f32_16x16x32_bf16 v[78:81], v[138:141], v[196:199], v[78:81]\n"
"ds_read_b64_tr_b16 v[132:133], v19 offset:50176\n"
"s_waitcnt lgkmcnt(8)\n"
"v_mfma_f32_16x16x32_bf16 v[152:155], v[22:25], v[160:163], v[152:155]\n"
"ds_read_b64_tr_b16 v[134:135], v18 offset:51200\n"
"v_mfma_f32_16x16x32_bf16 v[74:77], v[22:25], v[200:203], v[74:77]\n"
"ds_read_b64_tr_b16 v[136:137], v18 offset:52224\n"
"s_add_u32 s10, s13, 8192\n"
"buffer_load_dwordx4 v[156:159], v3, s[52:55], s10 offen offset:1024 sc1\n"
"v_mfma_f32_16x16x32_bf16 v[70:73], v[26:29], v[160:163], v[70:73]\n"
"ds_read_b64_tr_b16 v[138:139], v19 offset:51200\n"
"v_mfma_f32_16x16x32_bf16 v[78:81], v[26:29], v[200:203], v[78:81]\n"
"ds_read_b64_tr_b16 v[140:141], v19 offset:52224\n"
"s_waitcnt lgkmcnt(8)\n"
"v_mfma_f32_16x16x32_bf16 v[152:155], v[30:33], v[164:167], v[152:155]\n"
"v_mfma_f32_16x16x32_bf16 v[74:77], v[30:33], v[204:207], v[74:77]\n"
"s_add_u32 s11, s12, 12288\n"
"buffer_load_dwordx4 v[160:163], v3, s[52:55], s11 offen  sc1\n"
"v_mfma_f32_16x16x32_bf16 v[70:73], v[34:37], v[164:167], v[70:73]\n"
"v_mfma_f32_16x16x32_bf16 v[78:81], v[34:37], v[204:207], v[78:81]\n"
"s_waitcnt lgkmcnt(4)\n"
"v_mfma_f32_16x16x32_bf16 v[152:155], v[126:129], v[168:171], v[152:155]\n"
"v_mfma_f32_16x16x32_bf16 v[74:77], v[126:129], v[208:211], v[74:77]\n"
"s_add_u32 s8, s12, 12288\n"
"buffer_load_dwordx4 v[164:167], v3, s[52:55], s8 offen offset:1024 sc1\n"
"v_mfma_f32_16x16x32_bf16 v[70:73], v[130:133], v[168:171], v[70:73]\n"
"v_mfma_f32_16x16x32_bf16 v[78:81], v[130:133], v[208:211], v[78:81]\n"
"s_waitcnt lgkmcnt(0)\n"
"v_mfma_f32_16x16x32_bf16 v[152:155], v[134:137], v[172:175], v[152:155]\n"
"v_mfma_f32_16x16x32_bf16 v[74:77], v[134:137], v[212:215], v[74:77]\n"
"s_add_u32 s9, s13, 12288\n"
"buffer_load_dwordx4 v[168:171], v3, s[52:55], s9 offen  sc1\n"
"v_mfma_f32_16x16x32_bf16 v[70:73], v[138:141], v[172:175], v[70:73]\n"
"v_mfma_f32_16x16x32_bf16 v[78:81], v[138:141], v[212:215], v[78:81]\n"
"v_lshrrev_b32 v145, 4, v3\n"
"v_mul_u32_u24 v253, 4682, v145\n"
"v_lshrrev_b32 v253, 16, v253\n"
"v_mul_u32_u24 v144, 14, v253\n"
"v_sub_u32 v144, v145, v144\n"
"v_mul_u32_u24 v253, 3584, v253\n"
"v_lshl_add_u32 v253, v144, 4, v253\n"
"s_mul_i32 s94, s34, 896\n"
"s_lshl2_add_u32 s96, s56, s34\n"
"s_lshl_b32 s97, s96, 10\n"
"s_add_u32 s97, s97, 131072\n"
"s_lshl_b32 s96, s96, 13\n"
"s_add_u32 s96, s96, 2097152\n"
"v_lshlrev_b32 v28, 6, v2\n"
"v_lshl_add_u32 v28, v1, 4, v28\n"
"s_lshr_b32 s63, s34, 1\n"
"s_lshl_b32 s63, s63, 10\n"
"s_and_b32 s68, s34, 1\n"
"s_lshl3_add_u32 s63, s68, s63\n"
"s_add_u32 s63, s63, 161792\n"
"v_add_u32 v29, s63, v3\n"
"s_lshl_b32 s63, s34, 5\n"
"s_and_b32 s63, s63, 64\n"
"s_lshl3_add_u32 s63, s68, s63\n"
"s_add_u32 s63, s63, 160128\n"
"v_lshl_add_u32 v30, v1, 4, s63\n"
"v_and_b32 v24, 1, v2\n"
"v_lshlrev_b32 v24, 4, v24\n"
"v_mov_b32 v25, 0x3f80\n"
"v_lshlrev_b32 v24, v24, v25\n"
"v_and_b32 v25, 1, v1\n"
"v_lshlrev_b32 v25, 2, v25\n"
"v_lshrrev_b32 v32, 1, v2\n"
"v_sub_u32 v25, v32, v25\n"
"v_cmp_eq_u32 vcc, 0, v25\n"
"s_nop 1\n"
"v_cndmask_b32 v32, 0, v24, vcc\n"
"v_cmp_eq_u32 vcc, 1, v25\n"
"s_nop 1\n"
"v_cndmask_b32 v33, 0, v24, vcc\n"
"v_cmp_eq_u32 vcc, 2, v25\n"
"s_nop 1\n"
"v_cndmask_b32 v34, 0, v24, vcc\n"
"v_cmp_eq_u32 vcc, 3, v25\n"
"s_nop 1\n"
"v_cndmask_b32 v35, 0, v24, vcc\n"
"s_add_u32 s98, s96, 4096\n"
"v_lshlrev_b32 v31, 7, v2\n"
"s_add_u32 s99, s77, 524304\n"
"v_cvt_pk_bf16_f32 v88, v152, v153\n"
"v_cvt_pk_bf16_f32 v89, v154, v155\n"
"v_cvt_pk_bf16_f32 v92, v70, v71\n"
"v_cvt_pk_bf16_f32 v93, v72, v73\n"
"v_cvt_pk_bf16_f32 v96, v74, v75\n"
"v_cvt_pk_bf16_f32 v97, v76, v77\n"
"v_cvt_pk_bf16_f32 v100, v78, v79\n"
"v_cvt_pk_bf16_f32 v101, v80, v81\n"
"v_mfma_f32_16x16x16_bf16 v[152:155], a[224:225], v[88:89], 0\n"
"v_mfma_f32_16x16x16_bf16 v[70:73], a[226:227], v[92:93], 0\n"
"v_mfma_f32_16x16x16_bf16 v[74:77], a[224:225], v[96:97], 0\n"
"v_mfma_f32_16x16x16_bf16 v[78:81], a[226:227], v[100:101], 0\n"
"s_add_u32 s10, s13, 12288\n"
"buffer_load_dwordx4 v[172:175], v3, s[52:55], s10 offen offset:1024 sc1\n"
"s_nop 2\n"
"v_max3_f32 v112, v152, v153, v154\n"
"v_max3_f32 v113, -v152, -v153, -v154\n"
"v_max3_f32 v184, v70, v71, v72\n"
"v_max3_f32 v185, -v70, -v71, -v72\n"
"v_max3_f32 v186, v74, v75, v76\n"
"v_max3_f32 v187, -v74, -v75, -v76\n"
"v_max3_f32 v188, v78, v79, v80\n"
"v_max3_f32 v189, -v78, -v79, -v80\n"
"v_max_f32 v112, v112, v155\n"
"v_max_f32 v113, v113, -v155\n"
"v_max_f32 v184, v184, v73\n"
"v_max_f32 v185, v185, -v73\n"
"v_max_f32 v186, v186, v77\n"
"v_max_f32 v187, v187, -v77\n"
"v_max_f32 v188, v188, v81\n"
"v_max_f32 v189, v189, -v81\n"
"v_permlane32_swap_b32 v112, v113\n"
"v_permlane32_swap_b32 v186, v187\n"
"v_permlane32_swap_b32 v184, v185\n"
"v_permlane32_swap_b32 v188, v189\n"
"v_max_f32 v112, v112, v113\n"
"v_max_f32 v186, v186, v187\n"
"v_max_f32 v184, v184, v185\n"
"v_max_f32 v188, v188, v189\n"
"s_nop 0\n"
"v_permlane16_swap_b32 v112, v184\n"
"v_permlane16_swap_b32 v186, v188\n"
"v_max_f32 v112, v112, v184\n"
"v_max_f32 v186, v186, v188\n"
"v_mul_f32 v112, -v112, v142\n"
"v_mul_f32 v186, -v186, v143\n"
"v_mov_b32 v184, v112\n"
"v_mov_b32 v188, v186\n"
"s_nop 0\n"
"v_permlane16_swap_b32 v112, v184\n"
"v_permlane16_swap_b32 v186, v188\n"
"v_mov_b32 v113, v112\n"
"v_mov_b32 v187, v186\n"
"v_mov_b32 v185, v184\n"
"v_mov_b32 v189, v188\n"
"v_permlane32_swap_b32 v112, v113\n"
"v_permlane32_swap_b32 v186, v187\n"
"v_permlane32_swap_b32 v184, v185\n"
"v_permlane32_swap_b32 v188, v189\n"
"v_pk_fma_f32 v[104:105], v[152:153], v[142:143], v[112:113] op_sel:[0,0,1] op_sel_hi:[1,0,1] neg_lo:[1,0,0] neg_hi:[1,0,0]\n"
"v_pk_fma_f32 v[152:153], v[152:153], v[142:143], v[112:113] op_sel:[0,0,0] op_sel_hi:[1,0,0]\n"
"v_pk_fma_f32 v[106:107], v[154:155], v[142:143], v[112:113] op_sel:[0,0,1] op_sel_hi:[1,0,1] neg_lo:[1,0,0] neg_hi:[1,0,0]\n"
"v_pk_fma_f32 v[154:155], v[154:155], v[142:143], v[112:113] op_sel:[0,0,0] op_sel_hi:[1,0,0]\n"
"v_pk_fma_f32 v[108:109], v[70:71], v[142:143], v[184:185] op_sel:[0,0,1] op_sel_hi:[1,0,1] neg_lo:[1,0,0] neg_hi:[1,0,0]\n"
"v_pk_fma_f32 v[70:71], v[70:71], v[142:143], v[184:185] op_sel:[0,0,0] op_sel_hi:[1,0,0]\n"
"v_pk_fma_f32 v[110:111], v[72:73], v[142:143], v[184:185] op_sel:[0,0,1] op_sel_hi:[1,0,1] neg_lo:[1,0,0] neg_hi:[1,0,0]\n"
"v_pk_fma_f32 v[72:73], v[72:73], v[142:143], v[184:185] op_sel:[0,0,0] op_sel_hi:[1,0,0]\n"
"v_pk_fma_f32 v[176:177], v[74:75], v[142:143], v[186:187] op_sel:[0,1,1] op_sel_hi:[1,1,1] neg_lo:[1,0,0] neg_hi:[1,0,0]\n"
"v_pk_fma_f32 v[74:75], v[74:75], v[142:143], v[186:187] op_sel:[0,1,0] op_sel_hi:[1,1,0]\n"
"v_pk_fma_f32 v[178:179], v[76:77], v[142:143], v[186:187] op_sel:[0,1,1] op_sel_hi:[1,1,1] neg_lo:[1,0,0] neg_hi:[1,0,0]\n"
"v_pk_fma_f32 v[76:77], v[76:77], v[142:143], v[186:187] op_sel:[0,1,0] op_sel_hi:[1,1,0]\n"
"v_pk_fma_f32 v[180:181], v[78:79], v[142:143], v[188:189] op_sel:[0,1,1] op_sel_hi:[1,1,1] neg_lo:[1,0,0] neg_hi:[1,0,0]\n"
"v_pk_fma_f32 v[78:79], v[78:79], v[142:143], v[188:189] op_sel:[0,1,0] op_sel_hi:[1,1,0]\n"
"v_pk_fma_f32 v[182:183], v[80:81], v[142:143], v[188:189] op_sel:[0,1,1] op_sel_hi:[1,1,1] neg_lo:[1,0,0] neg_hi:[1,0,0]\n"
"v_pk_fma_f32 v[80:81], v[80:81], v[142:143], v[188:189] op_sel:[0,1,0] op_sel_hi:[1,1,0]\n"
"v_exp_f32 v104, v104\n"
"v_exp_f32 v152, v152\n"
"v_exp_f32 v105, v105\n"
"v_exp_f32 v153, v153\n"
"v_exp_f32 v106, v106\n"
"v_exp_f32 v154, v154\n"
"v_exp_f32 v107, v107\n"
"v_exp_f32 v155, v155\n"
"buffer_load_dwordx2 v[36:37], v31, s[52:55], s99 offen  sc1\n"
"v_exp_f32 v108, v108\n"
"v_exp_f32 v70, v70\n"
"v_exp_f32 v109, v109\n"
"v_exp_f32 v71, v71\n"
"v_exp_f32 v110, v110\n"
"v_exp_f32 v72, v72\n"
"v_exp_f32 v111, v111\n"
"v_exp_f32 v73, v73\n"
"v_exp_f32 v176, v176\n"
"v_exp_f32 v74, v74\n"
"v_exp_f32 v177, v177\n"
"v_exp_f32 v75, v75\n"
"v_exp_f32 v178, v178\n"
"v_exp_f32 v76, v76\n"
"v_exp_f32 v179, v179\n"
"v_exp_f32 v77, v77\n"
"v_exp_f32 v180, v180\n"
"v_exp_f32 v78, v78\n"
"v_exp_f32 v181, v181\n"
"v_exp_f32 v79, v79\n"
"v_exp_f32 v182, v182\n"
"v_exp_f32 v80, v80\n"
"v_exp_f32 v183, v183\n"
"v_exp_f32 v81, v81\n"
"v_pk_add_f32 v[88:89], v[152:153], v[154:155] op_sel:[0,0] op_sel_hi:[1,1]\n"
"buffer_load_dwordx2 v[82:83], v31, s[52:55], s99 offen  sc1\n"
"v_pk_add_f32 v[90:91], v[104:105], v[106:107] op_sel:[0,0] op_sel_hi:[1,1]\n"
"v_pk_add_f32 v[92:93], v[70:71], v[72:73] op_sel:[0,0] op_sel_hi:[1,1]\n"
"v_pk_add_f32 v[94:95], v[108:109], v[110:111] op_sel:[0,0] op_sel_hi:[1,1]\n"
"v_pk_add_f32 v[96:97], v[74:75], v[76:77] op_sel:[0,0] op_sel_hi:[1,1]\n"
"v_pk_add_f32 v[98:99], v[176:177], v[178:179] op_sel:[0,0] op_sel_hi:[1,1]\n"
"v_pk_add_f32 v[100:101], v[78:79], v[80:81] op_sel:[0,0] op_sel_hi:[1,1]\n"
"v_pk_add_f32 v[102:103], v[180:181], v[182:183] op_sel:[0,0] op_sel_hi:[1,1]\n"
"v_add_f32 v112, v88, v89\n"
"v_add_f32 v113, v90, v91\n"
"v_add_f32 v184, v92, v93\n"
"v_add_f32 v185, v94, v95\n"
"v_add_f32 v186, v96, v97\n"
"v_add_f32 v187, v98, v99\n"
"v_add_f32 v188, v100, v101\n"
"v_add_f32 v189, v102, v103\n"
"v_permlane32_swap_b32 v112, v113\n"
"v_permlane32_swap_b32 v186, v187\n"
"v_permlane32_swap_b32 v184, v185\n"
"v_permlane32_swap_b32 v188, v189\n"
"v_add_f32 v112, v112, v113\n"
"v_add_f32 v186, v186, v187\n"
"v_add_f32 v184, v184, v185\n"
"v_add_f32 v188, v188, v189\n"
"s_nop 0\n"
"v_permlane16_swap_b32 v112, v184\n"
"buffer_load_dwordx2 v[84:85], v31, s[52:55], s99 offen  sc1\n"
"v_permlane16_swap_b32 v186, v188\n"
"v_add_f32 v112, v112, v184\n"
"v_add_f32 v186, v186, v188\n"
"v_rcp_f32 v112, v112\n"
"v_rcp_f32 v186, v186\n"
"v_mov_b32 v184, v112\n"
"v_mov_b32 v188, v186\n"
"s_nop 0\n"
"v_permlane16_swap_b32 v112, v184\n"
"v_permlane16_swap_b32 v186, v188\n"
"v_mov_b32 v113, v112\n"
"v_mov_b32 v187, v186\n"
"v_mov_b32 v185, v184\n"
"v_mov_b32 v189, v188\n"
"v_permlane32_swap_b32 v112, v113\n"
"v_permlane32_swap_b32 v186, v187\n"
"v_permlane32_swap_b32 v184, v185\n"
"v_permlane32_swap_b32 v188, v189\n"
"v_pk_mul_f32 v[152:153], v[152:153], v[112:113] op_sel:[0,0] op_sel_hi:[1,0]\n"
"v_pk_mul_f32 v[104:105], v[104:105], v[112:113] op_sel:[0,1] op_sel_hi:[1,1]\n"
"v_pk_mul_f32 v[154:155], v[154:155], v[112:113] op_sel:[0,0] op_sel_hi:[1,0]\n"
"v_pk_mul_f32 v[106:107], v[106:107], v[112:113] op_sel:[0,1] op_sel_hi:[1,1]\n"
"v_pk_mul_f32 v[70:71], v[70:71], v[184:185] op_sel:[0,0] op_sel_hi:[1,0]\n"
"v_pk_mul_f32 v[108:109], v[108:109], v[184:185] op_sel:[0,1] op_sel_hi:[1,1]\n"
"v_pk_mul_f32 v[72:73], v[72:73], v[184:185] op_sel:[0,0] op_sel_hi:[1,0]\n"
"v_pk_mul_f32 v[110:111], v[110:111], v[184:185] op_sel:[0,1] op_sel_hi:[1,1]\n"
"buffer_load_dwordx2 v[86:87], v31, s[52:55], s99 offen  sc1\n"
"v_pk_mul_f32 v[74:75], v[74:75], v[186:187] op_sel:[0,0] op_sel_hi:[1,0]\n"
"v_pk_mul_f32 v[176:177], v[176:177], v[186:187] op_sel:[0,1] op_sel_hi:[1,1]\n"
"s_waitcnt vmcnt(3)\n"
"v_cmp_ne_u64 vcc, s[60:61], v[36:37]\n"
"s_nop 4\n"
"s_cbranch_vccz L_23_%=\n"
"s_waitcnt vmcnt(2)\n"
"v_cmp_ne_u64 vcc, s[60:61], v[82:83]\n"
"s_nop 4\n"
"s_cbranch_vccz L_23_%=\n"
"s_waitcnt vmcnt(1)\n"
"v_cmp_ne_u64 vcc, s[60:61], v[84:85]\n"
"s_nop 4\n"
"s_cbranch_vccz L_23_%=\n"
"s_waitcnt vmcnt(0)\n"
"v_cmp_ne_u64 vcc, s[60:61], v[86:87]\n"
"s_nop 4\n"
"s_cbranch_vccz L_23_%=\n"
"s_mov_b32 s62, 40000\n"
"buffer_load_dwordx2 v[12:13], v31, s[52:55], s99 offen  sc1\n"
"s_sleep 4\n"
"buffer_load_dwordx2 v[14:15], v31, s[52:55], s99 offen  sc1\n"
"L_24_%=:\n"
"s_waitcnt vmcnt(1)\n"
"v_cmp_ne_u64 vcc, s[60:61], v[12:13]\n"
"s_nop 4\n"
"s_cbranch_vccz L_23_%=\n"
"buffer_load_dwordx2 v[12:13], v31, s[52:55], s99 offen  sc1\n"
"s_waitcnt vmcnt(1)\n"
"v_cmp_ne_u64 vcc, s[60:61], v[14:15]\n"
"s_nop 4\n"
"s_cbranch_vccz L_23_%=\n"
"buffer_load_dwordx2 v[14:15], v31, s[52:55], s99 offen  sc1\n"
"s_sub_u32 s62, s62, 1\n"
"s_cbranch_scc0 L_24_%=\n"
"L_23_%=:\n"
"buffer_load_dwordx4 v[24:27], v28, s[52:55], s97 offen  sc1\n"
"v_pk_mul_f32 v[76:77], v[76:77], v[186:187] op_sel:[0,0] op_sel_hi:[1,0]\n"
"v_pk_mul_f32 v[178:179], v[178:179], v[186:187] op_sel:[0,1] op_sel_hi:[1,1]\n"
"buffer_load_dwordx4 v[144:147], v3, s[52:55], s96 offen  sc1\n"
"v_pk_mul_f32 v[78:79], v[78:79], v[188:189] op_sel:[0,0] op_sel_hi:[1,0]\n"
"v_pk_mul_f32 v[180:181], v[180:181], v[188:189] op_sel:[0,1] op_sel_hi:[1,1]\n"
"v_pk_mul_f32 v[80:81], v[80:81], v[188:189] op_sel:[0,0] op_sel_hi:[1,0]\n"
"buffer_load_dwordx4 v[148:151], v3, s[52:55], s96 offen offset:1024 sc1\n"
"v_pk_mul_f32 v[182:183], v[182:183], v[188:189] op_sel:[0,1] op_sel_hi:[1,1]\n"
"v_cvt_pk_bf16_f32 v88, v152, v153\n"
"buffer_load_dwordx4 v[126:129], v3, s[52:55], s96 offen offset:2048 sc1\n"
"v_cvt_pk_bf16_f32 v89, v154, v155\n"
"v_cvt_pk_bf16_f32 v90, v104, v105\n"
"v_cvt_pk_bf16_f32 v91, v106, v107\n"
"buffer_load_dwordx4 v[130:133], v3, s[52:55], s96 offen offset:3072 sc1\n"
"v_cvt_pk_bf16_f32 v92, v70, v71\n"
"v_cvt_pk_bf16_f32 v93, v72, v73\n"
"buffer_load_dwordx4 v[134:137], v3, s[52:55], s98 offen  sc1\n"
"v_cvt_pk_bf16_f32 v94, v108, v109\n"
"v_cvt_pk_bf16_f32 v95, v110, v111\n"
"v_cvt_pk_bf16_f32 v96, v74, v75\n"
"buffer_load_dwordx4 v[138:141], v3, s[52:55], s98 offen offset:1024 sc1\n"
"v_cvt_pk_bf16_f32 v97, v76, v77\n"
"v_cvt_pk_bf16_f32 v98, v176, v177\n"
"buffer_load_dwordx4 v[16:19], v3, s[52:55], s98 offen offset:2048 sc1\n"
"v_cvt_pk_bf16_f32 v99, v178, v179\n"
"v_cvt_pk_bf16_f32 v100, v78, v79\n"
"v_cvt_pk_bf16_f32 v101, v80, v81\n"
"buffer_load_dwordx4 v[20:23], v3, s[52:55], s98 offen offset:3072 sc1\n"
"v_cvt_pk_bf16_f32 v102, v180, v181\n"
"v_cvt_pk_bf16_f32 v103, v182, v183\n"
"s_waitcnt vmcnt(8)\n"
"v_add_f32_dpp v24, v24, v24 row_ror:8 row_mask:0xf bank_mask:0xf\n"
"v_add_f32_dpp v25, v25, v25 row_ror:8 row_mask:0xf bank_mask:0xf\n"
"v_add_f32_dpp v26, v26, v26 row_ror:8 row_mask:0xf bank_mask:0xf\n"
"v_add_f32_dpp v27, v27, v27 row_ror:8 row_mask:0xf bank_mask:0xf\n"
"v_add_f32_dpp v24, v24, v24 row_ror:4 row_mask:0xf bank_mask:0xf\n"
"v_add_f32_dpp v25, v25, v25 row_ror:4 row_mask:0xf bank_mask:0xf\n"
"v_add_f32_dpp v26, v26, v26 row_ror:4 row_mask:0xf bank_mask:0xf\n"
"v_add_f32_dpp v27, v27, v27 row_ror:4 row_mask:0xf bank_mask:0xf\n"
"v_add_f32_dpp v24, v24, v24 row_ror:2 row_mask:0xf bank_mask:0xf\n"
"v_add_f32_dpp v25, v25, v25 row_ror:2 row_mask:0xf bank_mask:0xf\n"
"v_add_f32_dpp v26, v26, v26 row_ror:2 row_mask:0xf bank_mask:0xf\n"
"v_add_f32_dpp v27, v27, v27 row_ror:2 row_mask:0xf bank_mask:0xf\n"
"v_add_f32_dpp v24, v24, v24 row_ror:1 row_mask:0xf bank_mask:0xf\n"
"v_add_f32_dpp v25, v25, v25 row_ror:1 row_mask:0xf bank_mask:0xf\n"
"v_add_f32_dpp v26, v26, v26 row_ror:1 row_mask:0xf bank_mask:0xf\n"
"v_add_f32_dpp v27, v27, v27 row_ror:1 row_mask:0xf bank_mask:0xf\n"
"v_cvt_pk_bf16_f32 v24, v24, v25\n"
"v_cvt_pk_bf16_f32 v25, v26, v27\n"
"ds_write_b64 v30, v[24:25]\n"
"v_add_u32 v28, 161792, v3\n"
"v_lshlrev_b32 v30, 4, v1\n"
"v_add_u32 v30, 160128, v30\n"
"s_lshr_b32 s68, s56, 1\n"
"s_lshl3_add_u32 s63, s34, s68\n"
"s_mul_i32 s68, s59, 3\n"
"s_lshl4_add_u32 s63, s63, s68\n"
"s_lshl_b32 s63, s63, 10\n"
"s_add_u32 s68, s63, 524288\n"
"s_waitcnt vmcnt(7)\n"
"v_mfma_f32_16x16x32_bf16 v[152:155], v[32:35], v[144:147], 0\n"
"s_waitcnt vmcnt(6)\n"
"v_mfma_f32_16x16x32_bf16 v[152:155], v[32:35], v[148:151], v[152:155]\n"
"s_waitcnt vmcnt(5)\n"
"v_mfma_f32_16x16x32_bf16 v[152:155], v[32:35], v[126:129], v[152:155]\n"
"s_waitcnt vmcnt(4)\n"
"v_mfma_f32_16x16x32_bf16 v[152:155], v[32:35], v[130:133], v[152:155]\n"
"s_waitcnt vmcnt(3)\n"
"v_mfma_f32_16x16x32_bf16 v[152:155], v[32:35], v[134:137], v[152:155]\n"
"s_waitcnt vmcnt(2)\n"
"v_mfma_f32_16x16x32_bf16 v[152:155], v[32:35], v[138:141], v[152:155]\n"
"s_waitcnt vmcnt(1)\n"
"v_mfma_f32_16x16x32_bf16 v[152:155], v[32:35], v[16:19], v[152:155]\n"
"s_waitcnt vmcnt(0)\n"
"v_mfma_f32_16x16x32_bf16 v[152:155], v[32:35], v[20:23], v[152:155]\n"
"s_nop 7\n"
"v_cvt_pk_bf16_f32 v148, v152, v153\n"
"v_cvt_pk_bf16_f32 v149, v154, v155\n"
"ds_write_b64 v29, v[148:149]\n"
"s_waitcnt lgkmcnt(0)\n"
"s_barrier\n"
"ds_read_b128 v[134:137], v30\n"
"ds_read_b128 v[138:141], v30 offset:64\n"
"ds_read_b128 v[126:129], v28\n"
"ds_read_b128 v[130:133], v28 offset:1024\n"
"s_waitcnt lgkmcnt(3)\n"
"v_mfma_f32_16x16x32_bf16 v[74:77], v[134:137], v[88:91], 0\n"
"s_waitcnt lgkmcnt(2)\n"
"v_mfma_f32_16x16x32_bf16 v[82:85], v[138:141], v[92:95], 0\n"
"v_mfma_f32_16x16x32_bf16 v[78:81], v[134:137], v[96:99], 0\n"
"v_mfma_f32_16x16x32_bf16 v[176:179], v[138:141], v[100:103], 0\n"
"s_waitcnt lgkmcnt(1)\n"
"v_mfma_f32_16x16x32_bf16 v[152:155], v[126:129], v[88:91], 0\n"
"s_waitcnt lgkmcnt(0)\n"
"v_mfma_f32_16x16x32_bf16 v[108:111], v[130:133], v[92:95], 0\n"
"v_mfma_f32_16x16x32_bf16 v[104:107], v[126:129], v[96:99], 0\n"
"v_mfma_f32_16x16x32_bf16 v[70:73], v[130:133], v[100:103], 0\n"
"v_add_f32 v74, 0x358637bd, v74\n"
"v_add_f32 v82, 0x358637bd, v82\n"
"v_add_f32 v78, 0x358637bd, v78\n"
"v_add_f32 v176, 0x358637bd, v176\n"
"v_rcp_f32 v74, v74\n"
"v_rcp_f32 v82, v82\n"
"v_rcp_f32 v78, v78\n"
"v_rcp_f32 v176, v176\n"
"v_pk_mul_f32 v[152:153], v[152:153], v[74:75] op_sel:[0,0] op_sel_hi:[1,0]\n"
"v_pk_mul_f32 v[154:155], v[154:155], v[74:75] op_sel:[0,0] op_sel_hi:[1,0]\n"
"v_pk_mul_f32 v[108:109], v[108:109], v[82:83] op_sel:[0,0] op_sel_hi:[1,0]\n"
"v_pk_mul_f32 v[110:111], v[110:111], v[82:83] op_sel:[0,0] op_sel_hi:[1,0]\n"
"v_cvt_pk_bf16_f32 v16, v152, v153\n"
"v_cvt_pk_bf16_f32 v17, v154, v155\n"
"v_cvt_pk_bf16_f32 v18, v108, v109\n"
"v_cvt_pk_bf16_f32 v19, v110, v111\n"
"buffer_store_dwordx4 v[16:19], v3, s[84:87], s63 offen  sc1\n"
"v_pk_mul_f32 v[104:105], v[104:105], v[78:79] op_sel:[0,0] op_sel_hi:[1,0]\n"
"v_pk_mul_f32 v[106:107], v[106:107], v[78:79] op_sel:[0,0] op_sel_hi:[1,0]\n"
"v_pk_mul_f32 v[70:71], v[70:71], v[176:177] op_sel:[0,0] op_sel_hi:[1,0]\n"
"v_pk_mul_f32 v[72:73], v[72:73], v[176:177] op_sel:[0,0] op_sel_hi:[1,0]\n"
"v_cvt_pk_bf16_f32 v20, v104, v105\n"
"v_cvt_pk_bf16_f32 v21, v106, v107\n"
"v_cvt_pk_bf16_f32 v22, v70, v71\n"
"v_cvt_pk_bf16_f32 v23, v72, v73\n"
"buffer_store_dwordx4 v[20:23], v3, s[84:87], s68 offen  sc1\n"
"v_lshrrev_b32 v19, 2, v2\n"
"v_mul_u32_u24 v19, 224, v19\n"
"v_mul_u32_u24 v252, 896, v1\n"
"v_add_u32 v19, v19, v252\n"
"v_and_b32 v252, 3, v2\n"
"v_lshl_add_u32 v19, v252, 3, v19\n"
"s_lshl_b32 s63, s59, 5\n"
"v_add_u32 v252, s63, v19\n"
"v_add_u32 v16, 57344, v252\n"
"s_cmp_eq_u32 s59, 0\n"
"s_cselect_b32 s63, 192, 0\n"
"v_add_u32 v17, s63, v19\n"
"v_add_u32 v18, 57344, v17\n"
"v_lshlrev_b32 v20, 16, v4\n"
"v_and_b32 v21, 0xffff0000, v4\n"
"v_lshlrev_b32 v22, 16, v5\n"
"v_and_b32 v23, 0xffff0000, v5\n"
"v_lshlrev_b32 v24, 16, v6\n"
"v_and_b32 v25, 0xffff0000, v6\n"
"v_lshlrev_b32 v26, 16, v7\n"
"v_and_b32 v27, 0xffff0000, v7\n"
"v_add_u32 v19, s73, v3\n"
"v_add_u32 v36, s74, v3\n"
"v_add_u32 v37, s75, v3\n"
"s_mul_i32 s63, s34, 6144\n"
"s_add_u32 s63, s63, 90112\n"
"v_add_u32 v142, s63, v3\n"
"v_and_b32 v143, 3, v2\n"
"v_lshlrev_b32 v143, 7, v143\n"
"s_lshl_b32 s96, s34, 9\n"
"s_add_u32 s96, s96, 1343552\n"
"v_lshrrev_b32 v146, 4, v3\n"
"v_mul_u32_u24 v144, 4682, v146\n"
"v_lshrrev_b32 v144, 16, v144\n"
"v_mul_u32_u24 v145, 14, v144\n"
"v_sub_u32 v145, v146, v145\n"
"v_mul_u32_u24 v144, 3584, v144\n"
"v_lshl_add_u32 v144, v145, 4, v144\n"
"s_mul_i32 s97, s34, 896\n"
"s_getpc_b64 s[20:21]\n"
"L_ts17_%=:\n"
"s_mov_b32 vcc_lo, L_pad17_%=-L_ts17_%=\n"
"s_cmp_eq_u32 s34, 1\n"
"s_cselect_b32 vcc_lo, L_pad18_%=-L_ts17_%=, vcc_lo\n"
"s_cmp_eq_u32 s34, 2\n"
"s_cselect_b32 vcc_lo, L_pad19_%=-L_ts17_%=, vcc_lo\n"
"s_cmp_eq_u32 s34, 3\n"
"s_cselect_b32 vcc_lo, L_pad20_%=-L_ts17_%=, vcc_lo\n"
"s_ashr_i32 vcc_hi, vcc_lo, 31\n"
"s_add_u32 s20, s20, vcc_lo\n"
"s_addc_u32 s21, s21, vcc_hi\n"
"s_mov_b64 exec, 0\n"
"s_swappc_b64 vcc, s[20:21]\n"
"s_mov_b64 exec, -1\n"
"s_waitcnt vmcnt(0)\n"
"v_mov_b32 v146, s60\n"
"v_mov_b32 v147, s61\n"
"s_add_u32 s11, s78, 1343552\n"
"s_barrier\n"
"s_mov_b64 exec, 1\n"
"buffer_store_dwordx2 v[146:147], off, s[84:87], s11 sc1\n"
"s_mov_b64 exec, -1\n"
"s_mov_b32 s62, 40000\n"
"buffer_load_dwordx2 v[12:13], v143, s[84:87], s96 offen  sc1\n"
"L_25_%=:\n"
"s_waitcnt vmcnt(0)\n"
"v_cmp_ne_u64 vcc, s[60:61], v[12:13]\n"
"s_nop 4\n"
"s_cbranch_vccz L_26_%=\n"
"buffer_load_dwordx2 v[12:13], v143, s[84:87], s96 offen  sc1\n"
"s_sub_u32 s62, s62, 1\n"
"s_cbranch_scc0 L_25_%=\n"
"L_26_%=:\n"
"buffer_load_dwordx4 v[74:77], v3, s[84:87], s72 offen  sc1\n"
"buffer_load_dwordx4 v[78:81], v3, s[84:87], s72 offen offset:3072 sc1\n"
"s_add_u32 s8, s72, 16384\n"
"buffer_load_dwordx4 v[82:85], v3, s[84:87], s8 offen  sc1\n"
"s_add_u32 s9, s72, 16384\n"
"buffer_load_dwordx4 v[86:89], v3, s[84:87], s9 offen offset:3072 sc1\n"
"buffer_load_dwordx4 v[144:147], v3, s[84:87], s71 offen  sc1\n"
"buffer_load_dwordx4 v[148:151], v3, s[84:87], s71 offen offset:3072 sc1\n"
"s_add_u32 s10, s71, 16384\n"
"buffer_load_dwordx4 v[152:155], v3, s[84:87], s10 offen  sc1\n"
"s_add_u32 s11, s71, 16384\n"
"buffer_load_dwordx4 v[70:73], v3, s[84:87], s11 offen offset:3072 sc1\n"
"s_add_u32 s8, s72, 524288\n"
"buffer_load_dwordx4 v[106:109], v3, s[84:87], s8 offen  sc1\n"
"s_add_u32 s9, s72, 524288\n"
"buffer_load_dwordx4 v[110:113], v3, s[84:87], s9 offen offset:3072 sc1\n"
"s_add_u32 s10, s72, 540672\n"
"buffer_load_dwordx4 v[176:179], v3, s[84:87], s10 offen  sc1\n"
"s_add_u32 s11, s72, 540672\n"
"buffer_load_dwordx4 v[180:183], v3, s[84:87], s11 offen offset:3072 sc1\n"
"s_add_u32 s8, s71, 524288\n"
"buffer_load_dwordx4 v[90:93], v3, s[84:87], s8 offen  sc1\n"
"s_add_u32 s9, s71, 524288\n"
"buffer_load_dwordx4 v[94:97], v3, s[84:87], s9 offen offset:3072 sc1\n"
"s_add_u32 s10, s71, 540672\n"
"buffer_load_dwordx4 v[98:101], v3, s[84:87], s10 offen  sc1\n"
"s_add_u32 s11, s71, 540672\n"
"buffer_load_dwordx4 v[102:105], v3, s[84:87], s11 offen offset:3072 sc1\n"
"s_waitcnt vmcnt(15)\n"
"v_mfma_f32_16x16x32_bf16 v[126:129], v[114:117], v[74:77], 0\n"
"v_mfma_f32_16x16x32_bf16 v[130:133], v[118:121], v[74:77], 0\n"
"s_add_u32 m0, s94, 60928\n"
"s_xor_b32 s8, s93, 512\n"
"s_mul_i32 s8, s8, 1792\n"
"s_add_u32 s8, s8, s89\n"
"s_add_u32 s95, s8, 14336\n"
"s_bfm_b64 exec, 56, 0\n"
"buffer_load_dwordx4 v253, s[28:31], s95 offen lds\n"
"s_mov_b64 exec, -1\n"
"v_mfma_f32_16x16x32_bf16 v[134:137], v[122:125], v[74:77], 0\n"
"v_mfma_f32_16x16x32_bf16 v[138:141], v[38:41], v[74:77], 0\n"
"s_waitcnt vmcnt(15)\n"
"v_mfma_f32_16x16x32_bf16 v[126:129], v[42:45], v[78:81], v[126:129]\n"
"s_add_u32 m0, s94, 64512\n"
"s_add_u32 s95, s95, 14336\n"
"s_bfm_b64 exec, 56, 0\n"
"buffer_load_dwordx4 v253, s[28:31], s95 offen lds\n"
"s_mov_b64 exec, -1\n"
"v_mfma_f32_16x16x32_bf16 v[130:133], v[46:49], v[78:81], v[130:133]\n"
"v_mfma_f32_16x16x32_bf16 v[134:137], v[50:53], v[78:81], v[134:137]\n"
"v_mfma_f32_16x16x32_bf16 v[138:141], v[54:57], v[78:81], v[138:141]\n"
"s_add_u32 m0, s94, 68096\n"
"s_add_u32 s95, s95, 14336\n"
"s_bfm_b64 exec, 56, 0\n"
"buffer_load_dwordx4 v253, s[28:31], s95 offen lds\n"
"s_mov_b64 exec, -1\n"
"s_waitcnt vmcnt(16)\n"
"v_mfma_f32_16x16x32_bf16 v[126:129], v[58:61], v[82:85], v[126:129]\n"
"v_mfma_f32_16x16x32_bf16 v[130:133], v[62:65], v[82:85], v[130:133]\n"
"v_mfma_f32_16x16x32_bf16 v[134:137], v[66:69], v[82:85], v[134:137]\n"
"s_add_u32 m0, s94, 71680\n"
"s_xor_b32 s9, s93, 640\n"
"s_mul_i32 s9, s9, 1792\n"
"s_add_u32 s9, s9, s89\n"
"s_add_u32 s95, s9, 0\n"
"s_bfm_b64 exec, 56, 0\n"
"buffer_load_dwordx4 v253, s[28:31], s95 offen lds\n"
"s_mov_b64 exec, -1\n"
"v_mfma_f32_16x16x32_bf16 v[138:141], v[156:159], v[82:85], v[138:141]\n"
"s_waitcnt vmcnt(16)\n"
"v_mfma_f32_16x16x32_bf16 v[126:129], v[160:163], v[86:89], v[126:129]\n"
"v_mfma_f32_16x16x32_bf16 v[130:133], v[164:167], v[86:89], v[130:133]\n"
"s_add_u32 m0, s94, 75264\n"
"L_pad11_%=:\n"
"s_cbranch_execz L_ret_%=\n"
"s_add_u32 s95, s95, 14336\n"
"s_bfm_b64 exec, 56, 0\n"
"buffer_load_dwordx4 v253, s[28:31], s95 offen lds\n"
"s_mov_b64 exec, -1\n"
"v_mfma_f32_16x16x32_bf16 v[134:137], v[168:171], v[86:89], v[134:137]\n"
"v_mfma_f32_16x16x32_bf16 v[138:141], v[172:175], v[86:89], v[138:141]\n"
"s_waitcnt vmcnt(16)\n"
"v_mfma_f32_16x16x32_bf16 v[28:31], v[122:125], v[144:147], 0\n"
"s_add_u32 m0, s94, 78848\n"
"s_add_u32 s95, s95, 14336\n"
"s_bfm_b64 exec, 56, 0\n"
"buffer_load_dwordx4 v253, s[28:31], s95 offen lds\n"
"s_mov_b64 exec, -1\n"
"v_mfma_f32_16x16x32_bf16 v[32:35], v[38:41], v[144:147], 0\n"
"ds_write_b128 v36, v[126:129]\n"
"s_waitcnt vmcnt(16)\n"
"v_mfma_f32_16x16x32_bf16 v[28:31], v[50:53], v[148:151], v[28:31]\n"
"v_mfma_f32_16x16x32_bf16 v[32:35], v[54:57], v[148:151], v[32:35]\n"
"ds_write_b128 v36, v[130:133] offset:1024\n"
"s_add_u32 m0, s94, 82432\n"
"s_add_u32 s95, s95, 14336\n"
"s_bfm_b64 exec, 56, 0\n"
"buffer_load_dwordx4 v253, s[28:31], s95 offen lds\n"
"s_mov_b64 exec, -1\n"
"s_waitcnt vmcnt(16)\n"
"v_mfma_f32_16x16x32_bf16 v[28:31], v[66:69], v[152:155], v[28:31]\n"
"v_mfma_f32_16x16x32_bf16 v[32:35], v[156:159], v[152:155], v[32:35]\n"
"ds_write_b128 v37, v[134:137]\n"
"s_waitcnt vmcnt(15)\n"
"v_mfma_f32_16x16x32_bf16 v[28:31], v[168:171], v[70:73], v[28:31]\n"
"s_add_u32 m0, s94, 86016\n"
"s_xor_b32 s10, s93, 768\n"
"s_mul_i32 s10, s10, 1792\n"
"s_add_u32 s10, s10, s89\n"
"s_add_u32 s95, s10, 0\n"
"s_bfm_b64 exec, 56, 0\n"
"buffer_load_dwordx4 v253, s[28:31], s95 offen lds\n"
"s_mov_b64 exec, -1\n"
"v_mfma_f32_16x16x32_bf16 v[32:35], v[172:175], v[70:73], v[32:35]\n"
"ds_write_b128 v37, v[138:141] offset:1024\n"
"v_mfma_f32_16x16x32_bf16 v[20:23], v[114:117], v[144:147], v[20:23]\n"
"v_mfma_f32_16x16x32_bf16 v[24:27], v[118:121], v[144:147], v[24:27]\n"
"ds_write_b128 v19, v[28:31]\n"
"v_mfma_f32_16x16x32_bf16 v[20:23], v[42:45], v[148:151], v[20:23]\n"
"s_nop 2\n"
"ds_write_b128 v19, v[32:35] offset:1024\n"
"v_mfma_f32_16x16x32_bf16 v[24:27], v[46:49], v[148:151], v[24:27]\n"
"s_waitcnt lgkmcnt(0)\n"
"s_barrier\n"
"ds_read_b128 v[28:31], v142\n"
"ds_read_b128 v[32:35], v142 offset:1024\n"
"v_mfma_f32_16x16x32_bf16 v[20:23], v[58:61], v[152:155], v[20:23]\n"
"ds_read_b128 v[126:129], v142 offset:2048\n"
"ds_read_b128 v[130:133], v142 offset:3072\n"
"v_mfma_f32_16x16x32_bf16 v[24:27], v[62:65], v[152:155], v[24:27]\n"
"ds_read_b128 v[134:137], v142 offset:4096\n"
"ds_read_b128 v[138:141], v142 offset:5120\n"
"v_mfma_f32_16x16x32_bf16 v[20:23], v[160:163], v[70:73], v[20:23]\n"
"v_mfma_f32_16x16x32_bf16 v[24:27], v[164:167], v[70:73], v[24:27]\n"
"s_waitcnt lgkmcnt(5)\n"
"s_nop 5\n"
"v_pk_add_f32 v[28:29], v[28:29], v[20:21] op_sel:[0,0] op_sel_hi:[1,1]\n"
"s_waitcnt lgkmcnt(1)\n"
"v_pk_add_f32 v[126:127], v[126:127], v[134:135] op_sel:[0,0] op_sel_hi:[1,1]\n"
"v_pk_add_f32 v[30:31], v[30:31], v[22:23] op_sel:[0,0] op_sel_hi:[1,1]\n"
"v_pk_add_f32 v[128:129], v[128:129], v[136:137] op_sel:[0,0] op_sel_hi:[1,1]\n"
"v_pk_add_f32 v[32:33], v[32:33], v[24:25] op_sel:[0,0] op_sel_hi:[1,1]\n"
"s_waitcnt lgkmcnt(0)\n"
"v_pk_add_f32 v[130:131], v[130:131], v[138:139] op_sel:[0,0] op_sel_hi:[1,1]\n"
"v_pk_add_f32 v[34:35], v[34:35], v[26:27] op_sel:[0,0] op_sel_hi:[1,1]\n"
"v_pk_add_f32 v[132:133], v[132:133], v[140:141] op_sel:[0,0] op_sel_hi:[1,1]\n"
"v_pk_add_f32 v[28:29], v[28:29], v[126:127] op_sel:[0,0] op_sel_hi:[1,1]\n"
"v_pk_add_f32 v[30:31], v[30:31], v[128:129] op_sel:[0,0] op_sel_hi:[1,1]\n"
"v_pk_add_f32 v[32:33], v[32:33], v[130:131] op_sel:[0,0] op_sel_hi:[1,1]\n"
"v_pk_add_f32 v[34:35], v[34:35], v[132:133] op_sel:[0,0] op_sel_hi:[1,1]\n"
"v_cvt_pk_bf16_f32 v4, v28, v29\n"
"v_cvt_pk_bf16_f32 v5, v30, v31\n"
"v_cvt_pk_bf16_f32 v6, v32, v33\n"
"v_cvt_pk_bf16_f32 v7, v34, v35\n"
"buffer_store_dwordx4 v[4:7], v3, s[84:87], s80 offen  sc1\n"
"v_mov_b32 v184, 0\n"
"v_mov_b32 v185, 0\n"
"v_lshlrev_b32 v143, 4, v2\n"
"v_mov_b32 v186, 2147483392\n"
"v_cmp_eq_u32 vcc, 0, v1\n"
"v_dot2c_f32_bf16 v184, s90, v4\n"
"v_dot2c_f32_bf16 v185, v4, v4\n"
"v_dot2c_f32_bf16 v184, s90, v5\n"
"v_dot2c_f32_bf16 v185, v5, v5\n"
"v_dot2c_f32_bf16 v184, s90, v6\n"
"v_dot2c_f32_bf16 v185, v6, v6\n"
"v_dot2c_f32_bf16 v184, s90, v7\n"
"v_dot2c_f32_bf16 v185, v7, v7\n"
"v_cndmask_b32 v143, v186, v143, vcc\n"
"s_nop 1\n"
"v_permlane32_swap_b32 v184, v185\n"
"v_add_f32 v184, v184, v185\n"
"v_mov_b32 v185, v184\n"
"s_nop 1\n"
"v_permlane16_swap_b32 v184, v185\n"
"v_add_f32 v184, v184, v185\n"
"v_mov_b32 v185, v184\n"
"s_nop 1\n"
"v_permlane32_swap_b32 v184, v185\n"
"buffer_store_dwordx2 v[184:185], v143, s[84:87], s81 offen  sc1\n"
"v_lshlrev_b32 v20, 16, v8\n"
"v_and_b32 v21, 0xffff0000, v8\n"
"v_lshlrev_b32 v22, 16, v9\n"
"v_and_b32 v23, 0xffff0000, v9\n"
"v_lshlrev_b32 v24, 16, v10\n"
"v_and_b32 v25, 0xffff0000, v10\n"
"v_lshlrev_b32 v26, 16, v11\n"
"v_and_b32 v27, 0xffff0000, v11\n"
"s_waitcnt vmcnt(17)\n"
"v_mfma_f32_16x16x32_bf16 v[126:129], v[114:117], v[106:109], 0\n"
"v_mfma_f32_16x16x32_bf16 v[130:133], v[118:121], v[106:109], 0\n"
"v_mfma_f32_16x16x32_bf16 v[134:137], v[122:125], v[106:109], 0\n"
"v_mfma_f32_16x16x32_bf16 v[138:141], v[38:41], v[106:109], 0\n"
"s_waitcnt vmcnt(16)\n"
"v_mfma_f32_16x16x32_bf16 v[126:129], v[42:45], v[110:113], v[126:129]\n"
"v_mfma_f32_16x16x32_bf16 v[130:133], v[46:49], v[110:113], v[130:133]\n"
"v_mfma_f32_16x16x32_bf16 v[134:137], v[50:53], v[110:113], v[134:137]\n"
"v_mfma_f32_16x16x32_bf16 v[138:141], v[54:57], v[110:113], v[138:141]\n"
"s_waitcnt vmcnt(15)\n"
"v_mfma_f32_16x16x32_bf16 v[126:129], v[58:61], v[176:179], v[126:129]\n"
"v_mfma_f32_16x16x32_bf16 v[130:133], v[62:65], v[176:179], v[130:133]\n"
"v_mfma_f32_16x16x32_bf16 v[134:137], v[66:69], v[176:179], v[134:137]\n"
"v_mfma_f32_16x16x32_bf16 v[138:141], v[156:159], v[176:179], v[138:141]\n"
"s_waitcnt vmcnt(14)\n"
"v_mfma_f32_16x16x32_bf16 v[126:129], v[160:163], v[180:183], v[126:129]\n"
"v_mfma_f32_16x16x32_bf16 v[130:133], v[164:167], v[180:183], v[130:133]\n"
"v_mfma_f32_16x16x32_bf16 v[134:137], v[168:171], v[180:183], v[134:137]\n"
"v_mfma_f32_16x16x32_bf16 v[138:141], v[172:175], v[180:183], v[138:141]\n"
"s_waitcnt vmcnt(13)\n"
"v_mfma_f32_16x16x32_bf16 v[28:31], v[122:125], v[90:93], 0\n"
"v_mfma_f32_16x16x32_bf16 v[32:35], v[38:41], v[90:93], 0\n"
"s_waitcnt vmcnt(12)\n"
"v_mfma_f32_16x16x32_bf16 v[28:31], v[50:53], v[94:97], v[28:31]\n"
"v_mfma_f32_16x16x32_bf16 v[32:35], v[54:57], v[94:97], v[32:35]\n"
"s_waitcnt vmcnt(11)\n"
"v_mfma_f32_16x16x32_bf16 v[28:31], v[66:69], v[98:101], v[28:31]\n"
"v_mfma_f32_16x16x32_bf16 v[32:35], v[156:159], v[98:101], v[32:35]\n"
"s_waitcnt vmcnt(10)\n"
"v_mfma_f32_16x16x32_bf16 v[28:31], v[168:171], v[102:105], v[28:31]\n"
"v_mfma_f32_16x16x32_bf16 v[32:35], v[172:175], v[102:105], v[32:35]\n"
"v_mfma_f32_16x16x32_bf16 v[20:23], v[114:117], v[90:93], v[20:23]\n"
"v_mfma_f32_16x16x32_bf16 v[24:27], v[118:121], v[90:93], v[24:27]\n"
"v_mfma_f32_16x16x32_bf16 v[20:23], v[42:45], v[94:97], v[20:23]\n"
"v_mfma_f32_16x16x32_bf16 v[24:27], v[46:49], v[94:97], v[24:27]\n"
"v_mfma_f32_16x16x32_bf16 v[20:23], v[58:61], v[98:101], v[20:23]\n"
"v_mfma_f32_16x16x32_bf16 v[24:27], v[62:65], v[98:101], v[24:27]\n"
"v_mfma_f32_16x16x32_bf16 v[20:23], v[160:163], v[102:105], v[20:23]\n"
"v_mfma_f32_16x16x32_bf16 v[24:27], v[164:167], v[102:105], v[24:27]\n"
"v_lshlrev_b32 v106, 4, v2\n"
"v_lshl_add_u32 v106, v1, 9, v106\n"
"s_lshl_b32 s12, s34, 11\n"
"s_add_u32 s12, s12, 1310720\n"
"s_lshl_b32 s13, s34, 15\n"
"s_add_u32 s13, s13, 1048576\n"
"s_and_b32 s63, s93, 512\n"
"s_add_u32 s63, s63, s93\n"
"s_lshl4_add_u32 s13, s63, s13\n"
"v_and_b32 v143, 7, v2\n"
"v_lshlrev_b32 v143, 7, v143\n"
"s_lshr_b32 s95, s34, 1\n"
"s_lshl_b32 s95, s95, 10\n"
"s_add_u32 s95, s95, 1343560\n"
"s_waitcnt vmcnt(0)\n"
"v_mov_b32 v110, s60\n"
"v_mov_b32 v111, s61\n"
"s_add_u32 s11, s78, 1343560\n"
"s_barrier\n"
"s_mov_b64 exec, 1\n"
"buffer_store_dwordx2 v[110:111], off, s[84:87], s11 sc1\n"
"s_mov_b64 exec, -1\n"
"ds_write_b128 v36, v[126:129]\n"
"ds_write_b128 v36, v[130:133] offset:1024\n"
"ds_write_b128 v37, v[134:137]\n"
"ds_write_b128 v37, v[138:141] offset:1024\n"
"ds_write_b128 v19, v[28:31]\n"
"ds_write_b128 v19, v[32:35] offset:1024\n"
"s_waitcnt lgkmcnt(0)\n"
"s_barrier\n"
"ds_read_b128 v[28:31], v142\n"
"ds_read_b128 v[32:35], v142 offset:1024\n"
"ds_read_b128 v[126:129], v142 offset:2048\n"
"ds_read_b128 v[130:133], v142 offset:3072\n"
"ds_read_b128 v[134:137], v142 offset:4096\n"
"ds_read_b128 v[138:141], v142 offset:5120\n"
"s_waitcnt lgkmcnt(5)\n"
"v_pk_add_f32 v[28:29], v[28:29], v[20:21] op_sel:[0,0] op_sel_hi:[1,1]\n"
"s_waitcnt lgkmcnt(1)\n"
"v_pk_add_f32 v[126:127], v[126:127], v[134:135] op_sel:[0,0] op_sel_hi:[1,1]\n"
"v_pk_add_f32 v[30:31], v[30:31], v[22:23] op_sel:[0,0] op_sel_hi:[1,1]\n"
"v_pk_add_f32 v[128:129], v[128:129], v[136:137] op_sel:[0,0] op_sel_hi:[1,1]\n"
"v_pk_add_f32 v[32:33], v[32:33], v[24:25] op_sel:[0,0] op_sel_hi:[1,1]\n"
"s_waitcnt lgkmcnt(0)\n"
"v_pk_add_f32 v[130:131], v[130:131], v[138:139] op_sel:[0,0] op_sel_hi:[1,1]\n"
"v_pk_add_f32 v[34:35], v[34:35], v[26:27] op_sel:[0,0] op_sel_hi:[1,1]\n"
"v_pk_add_f32 v[132:133], v[132:133], v[140:141] op_sel:[0,0] op_sel_hi:[1,1]\n"
"v_pk_add_f32 v[28:29], v[28:29], v[126:127] op_sel:[0,0] op_sel_hi:[1,1]\n"
"v_pk_add_f32 v[30:31], v[30:31], v[128:129] op_sel:[0,0] op_sel_hi:[1,1]\n"
"v_pk_add_f32 v[32:33], v[32:33], v[130:131] op_sel:[0,0] op_sel_hi:[1,1]\n"
"v_pk_add_f32 v[34:35], v[34:35], v[132:133] op_sel:[0,0] op_sel_hi:[1,1]\n"
"v_cvt_pk_bf16_f32 v8, v28, v29\n"
"v_cvt_pk_bf16_f32 v9, v30, v31\n"
"v_cvt_pk_bf16_f32 v10, v32, v33\n"
"v_cvt_pk_bf16_f32 v11, v34, v35\n"
"s_add_u32 s8, s80, 131072\n"
"buffer_store_dwordx4 v[8:11], v3, s[84:87], s8 offen  sc1\n"
"v_mov_b32 v110, 0\n"
"v_mov_b32 v111, 0\n"
"v_lshlrev_b32 v253, 4, v2\n"
"v_mov_b32 v112, 2147483392\n"
"v_cmp_eq_u32 vcc, 0, v1\n"
"v_dot2c_f32_bf16 v110, s90, v8\n"
"v_dot2c_f32_bf16 v111, v8, v8\n"
"v_dot2c_f32_bf16 v110, s90, v9\n"
"v_dot2c_f32_bf16 v111, v9, v9\n"
"v_dot2c_f32_bf16 v110, s90, v10\n"
"v_dot2c_f32_bf16 v111, v10, v10\n"
"v_dot2c_f32_bf16 v110, s90, v11\n"
"v_dot2c_f32_bf16 v111, v11, v11\n"
"v_cndmask_b32 v253, v112, v253, vcc\n"
"s_nop 1\n"
"v_permlane32_swap_b32 v110, v111\n"
"v_add_f32 v110, v110, v111\n"
"v_mov_b32 v111, v110\n"
"s_nop 1\n"
"v_permlane16_swap_b32 v110, v111\n"
"v_add_f32 v110, v110, v111\n"
"v_mov_b32 v111, v110\n"
"s_nop 1\n"
"v_permlane32_swap_b32 v110, v111\n"
"s_add_u32 s9, s81, 16384\n"
"buffer_store_dwordx2 v[110:111], v253, s[84:87], s9 offen  sc1\n"
"v_lshrrev_b32 v253, 4, v3\n"
"v_mul_u32_u24 v19, 4682, v253\n"
"v_lshrrev_b32 v19, 16, v19\n"
"v_mul_u32_u24 v142, 14, v19\n"
"v_sub_u32 v142, v253, v142\n"
"v_mul_u32_u24 v19, 3584, v19\n"
"v_lshl_add_u32 v19, v142, 4, v19\n"
"s_mul_i32 s96, s34, 896\n"
"v_and_b32 v142, 1, v1\n"
"v_lshlrev_b32 v142, 6, v142\n"
"v_lshl_add_u32 v142, v2, 2, v142\n"
"v_add_u32 v142, 160256, v142\n"
"s_lshr_b32 s63, s56, 1\n"
"s_lshl3_add_u32 s63, s34, s63\n"
"s_lshl_b32 s63, s63, 14\n"
"s_lshl_b32 s68, s59, 12\n"
"s_add_u32 s98, s63, s68\n"
"s_lshl3_add_u32 s99, s59, s63\n"
"ds_read2_b32 a[242:243], v142 offset0:0 offset1:0\n"
"ds_read2_b32 a[244:245], v142 offset0:32 offset1:32\n"
"ds_read2_b32 a[246:247], v142 offset0:64 offset1:64\n"
"ds_read2_b32 a[248:249], v142 offset0:96 offset1:96\n"
"ds_read2_b32 a[250:251], v142 offset0:128 offset1:128\n"
"ds_read2_b32 a[252:253], v142 offset0:160 offset1:160\n"
"ds_read2_b32 a[254:255], v142 offset0:192 offset1:192\n"
"v_lshlrev_b32 v234, 4, v2\n"
"v_lshl_add_u32 v234, v1, 9, v234\n"
"s_lshl_b32 s20, s34, 11\n"
"s_add_u32 s20, s20, 1327104\n"
"s_lshl_b32 s21, s34, 15\n"
"s_add_u32 s21, s21, 1179648\n"
"s_and_b32 s63, s93, 512\n"
"s_add_u32 s63, s63, s93\n"
"s_lshl4_add_u32 s21, s63, s21\n"
"v_and_b32 v165, 7, v2\n"
"v_lshlrev_b32 v165, 7, v165\n"
"s_lshr_b32 s23, s34, 1\n"
"s_lshl_b32 s23, s23, 10\n"
"s_add_u32 s23, s23, 1343568\n"
"ds_read_b64_tr_b16 v[20:21], v252\n"
"ds_read_b64_tr_b16 v[22:23], v252 offset:3584\n"
"ds_read_b64_tr_b16 v[24:25], v252 offset:32\n"
"ds_read_b64_tr_b16 v[26:27], v252 offset:3616\n"
"ds_read_b64_tr_b16 v[28:29], v252 offset:64\n"
"ds_read_b64_tr_b16 v[30:31], v252 offset:3648\n"
"ds_read_b64_tr_b16 v[32:33], v252 offset:96\n"
"ds_read_b64_tr_b16 v[34:35], v252 offset:3680\n"
"s_waitcnt lgkmcnt(14)\n"
"ds_read_b64_tr_b16 v[110:111], v252 offset:128\n"
"s_waitcnt lgkmcnt(14)\n"
"ds_read_b64_tr_b16 v[112:113], v252 offset:3712\n"
"s_waitcnt lgkmcnt(14)\n"
"ds_read_b64_tr_b16 v[114:115], v252 offset:160\n"
"s_waitcnt lgkmcnt(14)\n"
"ds_read_b64_tr_b16 v[116:117], v252 offset:3744\n"
"s_mov_b32 s62, 40000\n"
"buffer_load_dwordx2 v[12:13], v143, s[84:87], s95 offen  sc1\n"
"L_27_%=:\n"
"s_waitcnt vmcnt(0)\n"
"v_cmp_ne_u64 vcc, s[60:61], v[12:13]\n"
"s_nop 4\n"
"s_cbranch_vccz L_28_%=\n"
"buffer_load_dwordx2 v[12:13], v143, s[84:87], s95 offen  sc1\n"
"s_sub_u32 s62, s62, 1\n"
"s_cbranch_scc0 L_27_%=\n"
"L_28_%=:\n"
"buffer_load_dwordx4 v[38:41], v3, s[84:87], s13 offen  sc1\n"
"buffer_load_dwordx4 v[42:45], v3, s[84:87], s13 offen offset:1024 sc1\n"
"buffer_load_dwordx4 v[102:105], v106, s[84:87], s12 offen  sc1\n"
"buffer_load_dwordx4 v[106:109], v106, s[84:87], s12 offen offset:256 sc1\n"
"s_xor_b32 s94, s13, 2048\n"
"buffer_load_dwordx4 v[46:49], v3, s[84:87], s94 offen  sc1\n"
"buffer_load_dwordx4 v[50:53], v3, s[84:87], s94 offen offset:1024 sc1\n"
"s_waitcnt vmcnt(6)\n"
"v_mov_b32 v238, s60\n"
"v_mov_b32 v239, s61\n"
"s_add_u32 s10, s78, 1343568\n"
"s_barrier\n"
"s_mov_b64 exec, 1\n"
"buffer_store_dwordx2 v[238:239], off, s[84:87], s10 sc1\n"
"s_mov_b64 exec, -1\n"
"s_xor_b32 s94, s13, 4096\n"
"buffer_load_dwordx4 v[54:57], v3, s[84:87], s94 offen  sc1\n"
"buffer_load_dwordx4 v[58:61], v3, s[84:87], s94 offen offset:1024 sc1\n"
"s_xor_b32 s94, s13, 6144\n"
"buffer_load_dwordx4 v[62:65], v3, s[84:87], s94 offen  sc1\n"
"buffer_load_dwordx4 v[66:69], v3, s[84:87], s94 offen offset:1024 sc1\n"
"s_xor_b32 s94, s13, 16384\n"
"buffer_load_dwordx4 v[70:73], v3, s[84:87], s94 offen  sc1\n"
"buffer_load_dwordx4 v[74:77], v3, s[84:87], s94 offen offset:1024 sc1\n"
"s_xor_b32 s94, s13, 18432\n"
"buffer_load_dwordx4 v[78:81], v3, s[84:87], s94 offen  sc1\n"
"s_waitcnt lgkmcnt(8) vmcnt(13)\n"
"v_mfma_f32_16x16x32_bf16 v[130:133], v[20:23], v[38:41], 0\n"
"ds_read_b64_tr_b16 v[118:119], v17\n"
"ds_read_b64_tr_b16 v[120:121], v17 offset:3584\n"
"buffer_load_dwordx4 v[82:85], v3, s[84:87], s94 offen offset:1024 sc1\n"
"v_mfma_f32_16x16x32_bf16 v[134:137], v[24:27], v[38:41], 0\n"
"ds_read_b64_tr_b16 v[122:123], v252 offset:7168\n"
"ds_read_b64_tr_b16 v[124:125], v252 offset:10752\n"
"s_waitcnt lgkmcnt(8)\n"
"v_mfma_f32_16x16x32_bf16 v[138:141], v[28:31], v[38:41], 0\n"
"ds_read_b64_tr_b16 v[20:21], v252 offset:7200\n"
"ds_read_b64_tr_b16 v[22:23], v252 offset:10784\n"
"v_mfma_f32_16x16x32_bf16 v[144:147], v[32:35], v[38:41], 0\n"
"ds_read_b64_tr_b16 v[24:25], v252 offset:7232\n"
"ds_read_b64_tr_b16 v[26:27], v252 offset:10816\n"
"s_xor_b32 s94, s13, 20480\n"
"buffer_load_dwordx4 v[86:89], v3, s[84:87], s94 offen  sc1\n"
"s_waitcnt lgkmcnt(8)\n"
"v_mfma_f32_16x16x32_bf16 v[148:151], v[110:113], v[38:41], 0\n"
"ds_read_b64_tr_b16 v[28:29], v252 offset:7264\n"
"ds_read_b64_tr_b16 v[30:31], v252 offset:10848\n"
"s_waitcnt vmcnt(13)\n"
"v_add_f32 v102, v102, v104\n"
"v_mfma_f32_16x16x32_bf16 v[152:155], v[114:117], v[38:41], 0\n"
"ds_read_b64_tr_b16 v[32:33], v252 offset:7296\n"
"ds_read_b64_tr_b16 v[34:35], v252 offset:10880\n"
"v_add_f32 v103, v103, v105\n"
"s_waitcnt lgkmcnt(8)\n"
"v_mfma_f32_16x16x32_bf16 v[156:159], v[118:121], v[38:41], 0\n"
"ds_read_b64_tr_b16 v[110:111], v252 offset:7328\n"
"ds_read_b64_tr_b16 v[112:113], v252 offset:10912\n"
"buffer_load_dwordx4 v[90:93], v3, s[84:87], s94 offen offset:1024 sc1\n"
"s_waitcnt vmcnt(13)\n"
"v_add_f32 v106, v106, v108\n"
"v_mfma_f32_16x16x32_bf16 v[130:133], v[122:125], v[42:45], v[130:133]\n"
"ds_read_b64_tr_b16 v[114:115], v17 offset:7168\n"
"ds_read_b64_tr_b16 v[116:117], v17 offset:10752\n"
"v_add_f32 v107, v107, v109\n"
"s_waitcnt lgkmcnt(8)\n"
"v_mfma_f32_16x16x32_bf16 v[134:137], v[20:23], v[42:45], v[134:137]\n"
"ds_read_b64_tr_b16 v[118:119], v252 offset:14336\n"
"ds_read_b64_tr_b16 v[120:121], v252 offset:17920\n"
"v_add_f32 v102, v102, v106\n"
"v_mfma_f32_16x16x32_bf16 v[138:141], v[24:27], v[42:45], v[138:141]\n"
"ds_read_b64_tr_b16 v[122:123], v252 offset:14368\n"
"ds_read_b64_tr_b16 v[124:125], v252 offset:17952\n"
"s_xor_b32 s94, s13, 22528\n"
"buffer_load_dwordx4 v[94:97], v3, s[84:87], s94 offen  sc1\n"
"v_add_f32 v103, v103, v107\n"
"s_waitcnt lgkmcnt(8)\n"
"v_mfma_f32_16x16x32_bf16 v[144:147], v[28:31], v[42:45], v[144:147]\n"
"ds_read_b64_tr_b16 v[20:21], v252 offset:14400\n"
"ds_read_b64_tr_b16 v[22:23], v252 offset:17984\n"
"v_permlane32_swap_b32 v102, v103\n"
"v_mfma_f32_16x16x32_bf16 v[148:151], v[32:35], v[42:45], v[148:151]\n"
"ds_read_b64_tr_b16 v[24:25], v252 offset:14432\n"
"ds_read_b64_tr_b16 v[26:27], v252 offset:18016\n"
"v_add_f32 v102, v102, v103\n"
"s_waitcnt lgkmcnt(8)\n"
"v_mfma_f32_16x16x32_bf16 v[152:155], v[110:113], v[42:45], v[152:155]\n"
"ds_read_b64_tr_b16 v[28:29], v252 offset:14464\n"
"ds_read_b64_tr_b16 v[30:31], v252 offset:18048\n"
"buffer_load_dwordx4 v[98:101], v3, s[84:87], s94 offen offset:1024 sc1\n"
"v_mov_b32 v103, v102\n"
"v_mfma_f32_16x16x32_bf16 v[156:159], v[114:117], v[42:45], v[156:159]\n"
"ds_read_b64_tr_b16 v[32:33], v252 offset:14496\n"
"ds_read_b64_tr_b16 v[34:35], v252 offset:18080\n"
"v_permlane16_swap_b32 v102, v103\n"
"s_waitcnt lgkmcnt(8) vmcnt(14)\n"
"v_mfma_f32_16x16x32_bf16 v[130:133], v[118:121], v[46:49], v[130:133]\n"
"ds_read_b64_tr_b16 v[110:111], v17 offset:14336\n"
"ds_read_b64_tr_b16 v[112:113], v17 offset:17920\n"
"v_add_f32 v102, v102, v103\n"
"v_mfma_f32_16x16x32_bf16 v[134:137], v[122:125], v[46:49], v[134:137]\n"
"ds_read_b64_tr_b16 v[114:115], v252 offset:21504\n"
"ds_read_b64_tr_b16 v[116:117], v252 offset:25088\n"
"s_add_u32 m0, s96, 89600\n"
"s_xor_b32 s11, s93, 768\n"
"s_mul_i32 s11, s11, 1792\n"
"s_add_u32 s11, s11, s89\n"
"s_add_u32 s97, s11, 14336\n"
"s_bfm_b64 exec, 56, 0\n"
"buffer_load_dwordx4 v19, s[28:31], s97 offen lds\n"
"s_mov_b64 exec, -1\n"
"v_mov_b32 v103, v102\n"
"buffer_load_dwordx2 v[238:239], v165, s[84:87], s23 offen  sc1\n"
"s_waitcnt lgkmcnt(8)\n"
"v_mfma_f32_16x16x32_bf16 v[138:141], v[20:23], v[46:49], v[138:141]\n"
"ds_read_b64_tr_b16 v[118:119], v252 offset:21536\n"
"ds_read_b64_tr_b16 v[120:121], v252 offset:25120\n"
"v_permlane32_swap_b32 v102, v103\n"
"v_mfma_f32_16x16x32_bf16 v[144:147], v[24:27], v[46:49], v[144:147]\n"
"ds_read_b64_tr_b16 v[122:123], v252 offset:21568\n"
"ds_read_b64_tr_b16 v[124:125], v252 offset:25152\n"
"v_mul_f32 v102, 0x3b000000, v102\n"
"s_waitcnt lgkmcnt(8)\n"
"v_mfma_f32_16x16x32_bf16 v[148:151], v[28:31], v[46:49], v[148:151]\n"
"ds_read_b64_tr_b16 v[20:21], v252 offset:21600\n"
"ds_read_b64_tr_b16 v[22:23], v252 offset:25184\n"
"s_add_u32 m0, s96, 93184\n"
"s_add_u32 s97, s97, 14336\n"
"s_bfm_b64 exec, 56, 0\n"
"buffer_load_dwordx4 v19, s[28:31], s97 offen lds\n"
"s_mov_b64 exec, -1\n"
"v_mul_f32 v103, 0x3b000000, v103\n"
"v_mfma_f32_16x16x32_bf16 v[152:155], v[32:35], v[46:49], v[152:155]\n"
"ds_read_b64_tr_b16 v[24:25], v252 offset:21632\n"
"ds_read_b64_tr_b16 v[26:27], v252 offset:25216\n"
"v_fma_f32 v103, -v102, v102, v103\n"
"s_waitcnt lgkmcnt(8)\n"
"v_mfma_f32_16x16x32_bf16 v[156:159], v[110:113], v[46:49], v[156:159]\n"
"ds_read_b64_tr_b16 v[28:29], v252 offset:21664\n"
"ds_read_b64_tr_b16 v[30:31], v252 offset:25248\n"
"v_max_f32 v103, 0, v103\n"
"s_waitcnt vmcnt(16)\n"
"v_mfma_f32_16x16x32_bf16 v[130:133], v[114:117], v[50:53], v[130:133]\n"
"ds_read_b64_tr_b16 v[32:33], v17 offset:21504\n"
"ds_read_b64_tr_b16 v[34:35], v17 offset:25088\n"
"s_add_u32 m0, s96, 96768\n"
"s_add_u32 s97, s97, 14336\n"
"s_bfm_b64 exec, 56, 0\n"
"buffer_load_dwordx4 v19, s[28:31], s97 offen lds\n"
"s_mov_b64 exec, -1\n"
"v_add_f32 v160, 0x3727c5ac, v103\n"
"s_waitcnt lgkmcnt(8)\n"
"v_mfma_f32_16x16x32_bf16 v[134:137], v[118:121], v[50:53], v[134:137]\n"
"ds_read_b64_tr_b16 v[110:111], v252 offset:28672\n"
"ds_read_b64_tr_b16 v[112:113], v252 offset:32256\n"
"v_rsq_f32 v160, v160\n"
"v_mfma_f32_16x16x32_bf16 v[138:141], v[122:125], v[50:53], v[138:141]\n"
"ds_read_b64_tr_b16 v[114:115], v252 offset:28704\n"
"ds_read_b64_tr_b16 v[116:117], v252 offset:32288\n"
"v_mul_f32 v103, v103, v160\n"
"s_waitcnt lgkmcnt(8)\n"
"v_mfma_f32_16x16x32_bf16 v[144:147], v[20:23], v[50:53], v[144:147]\n"
"ds_read_b64_tr_b16 v[118:119], v252 offset:28736\n"
"ds_read_b64_tr_b16 v[120:121], v252 offset:32320\n"
"s_add_u32 m0, s96, 100352\n"
"s_xor_b32 s8, s93, 896\n"
"s_mul_i32 s8, s8, 1792\n"
"s_add_u32 s8, s8, s89\n"
"s_add_u32 s97, s8, 0\n"
"s_bfm_b64 exec, 56, 0\n"
"buffer_load_dwordx4 v19, s[28:31], s97 offen lds\n"
"s_mov_b64 exec, -1\n"
"v_fmaak_f32 v103, v103, v160, 0x3727c5ac\n"
"v_mfma_f32_16x16x32_bf16 v[148:151], v[24:27], v[50:53], v[148:151]\n"
"ds_read_b64_tr_b16 v[122:123], v252 offset:28768\n"
"ds_read_b64_tr_b16 v[124:125], v252 offset:32352\n"
"v_rsq_f32 v103, v103\n"
"s_waitcnt lgkmcnt(8)\n"
"v_mfma_f32_16x16x32_bf16 v[152:155], v[28:31], v[50:53], v[152:155]\n"
"ds_read_b64_tr_b16 v[20:21], v252 offset:28800\n"
"ds_read_b64_tr_b16 v[22:23], v252 offset:32384\n"
"v_mul_f32 v103, v103, v160\n"
"v_mfma_f32_16x16x32_bf16 v[156:159], v[32:35], v[50:53], v[156:159]\n"
"ds_read_b64_tr_b16 v[24:25], v252 offset:28832\n"
"ds_read_b64_tr_b16 v[26:27], v252 offset:32416\n"
"s_add_u32 m0, s96, 103936\n"
"s_add_u32 s97, s97, 14336\n"
"s_bfm_b64 exec, 56, 0\n"
"buffer_load_dwordx4 v19, s[28:31], s97 offen lds\n"
"s_mov_b64 exec, -1\n"
"v_rcp_f32 v142, v103\n"
"s_waitcnt lgkmcnt(8) vmcnt(17)\n"
"v_mfma_f32_16x16x32_bf16 v[130:133], v[110:113], v[54:57], v[130:133]\n"
"ds_read_b64_tr_b16 v[28:29], v17 offset:28672\n"
"ds_read_b64_tr_b16 v[30:31], v17 offset:32256\n"
"v_mul_f32 v102, -1.0, v102\n"
"v_mfma_f32_16x16x32_bf16 v[134:137], v[114:117], v[54:57], v[134:137]\n"
"ds_read_b64_tr_b16 v[32:33], v252 offset:35840\n"
"ds_read_b64_tr_b16 v[34:35], v252 offset:39424\n"
"v_mul_f32 v37, 0xc0135761, v103\n"
"s_waitcnt lgkmcnt(8)\n"
"v_mfma_f32_16x16x32_bf16 v[138:141], v[118:121], v[54:57], v[138:141]\n"
"ds_read_b64_tr_b16 v[110:111], v252 offset:35872\n"
"ds_read_b64_tr_b16 v[112:113], v252 offset:39456\n"
"s_add_u32 m0, s96, 107520\n"
"s_add_u32 s97, s97, 14336\n"
"s_bfm_b64 exec, 56, 0\n"
"buffer_load_dwordx4 v19, s[28:31], s97 offen lds\n"
"s_mov_b64 exec, -1\n"
"v_mul_f32 v162, v103, v103\n"
"v_mfma_f32_16x16x32_bf16 v[144:147], v[122:125], v[54:57], v[144:147]\n"
"ds_read_b64_tr_b16 v[114:115], v252 offset:35904\n"
"ds_read_b64_tr_b16 v[116:117], v252 offset:39488\n"
"v_cvt_pk_bf16_f32 v128, v102, v102\n"
"s_waitcnt lgkmcnt(8)\n"
"v_mfma_f32_16x16x32_bf16 v[148:151], v[20:23], v[54:57], v[148:151]\n"
"ds_read_b64_tr_b16 v[118:119], v252 offset:35936\n"
"ds_read_b64_tr_b16 v[120:121], v252 offset:39520\n"
"v_and_b32 v164, 0xffff0000, v128\n"
"v_mfma_f32_16x16x32_bf16 v[152:155], v[24:27], v[54:57], v[152:155]\n"
"ds_read_b64_tr_b16 v[122:123], v252 offset:35968\n"
"ds_read_b64_tr_b16 v[124:125], v252 offset:39552\n"
"s_add_u32 m0, s96, 111104\n"
"s_add_u32 s97, s97, 14336\n"
"s_bfm_b64 exec, 56, 0\n"
"buffer_load_dwordx4 v19, s[28:31], s97 offen lds\n"
"s_mov_b64 exec, -1\n"
"v_sub_f32 v164, v102, v164\n"
"s_waitcnt lgkmcnt(8)\n"
"v_mfma_f32_16x16x32_bf16 v[156:159], v[28:31], v[54:57], v[156:159]\n"
"ds_read_b64_tr_b16 v[20:21], v252 offset:36000\n"
"ds_read_b64_tr_b16 v[22:23], v252 offset:39584\n"
"v_cvt_pk_bf16_f32 v129, v164, v164\n"
"s_waitcnt vmcnt(18)\n"
"v_mfma_f32_16x16x32_bf16 v[130:133], v[32:35], v[58:61], v[130:133]\n"
"ds_read_b64_tr_b16 v[24:25], v17 offset:35840\n"
"ds_read_b64_tr_b16 v[26:27], v17 offset:39424\n"
"v_mul_f32 v162, 0x3d372713, v162\n"
"s_waitcnt lgkmcnt(8)\n"
"v_mfma_f32_16x16x32_bf16 v[134:137], v[110:113], v[58:61], v[134:137]\n"
"ds_read_b64_tr_b16 v[28:29], v252 offset:43008\n"
"ds_read_b64_tr_b16 v[30:31], v252 offset:46592\n"
"v_mul_f32 v36, v162, v37\n"
"v_mfma_f32_16x16x32_bf16 v[138:141], v[114:117], v[58:61], v[138:141]\n"
"ds_read_b64_tr_b16 v[32:33], v252 offset:43040\n"
"ds_read_b64_tr_b16 v[34:35], v252 offset:46624\n"
"v_and_b32 v162, 0xffff0000, v142\n"
"s_waitcnt lgkmcnt(8)\n"
"v_mfma_f32_16x16x32_bf16 v[144:147], v[118:121], v[58:61], v[144:147]\n"
"ds_read_b64_tr_b16 v[110:111], v252 offset:43072\n"
"ds_read_b64_tr_b16 v[112:113], v252 offset:46656\n"
"v_sub_f32 v163, v142, v162\n"
"v_mfma_f32_16x16x32_bf16 v[148:151], v[122:125], v[58:61], v[148:151]\n"
"ds_read_b64_tr_b16 v[114:115], v252 offset:43104\n"
"ds_read_b64_tr_b16 v[116:117], v252 offset:46688\n"
"v_cvt_pk_bf16_f32 v163, v162, v163\n"
"s_waitcnt vmcnt(6)\n"
"v_cmp_ne_u64 vcc, s[60:61], v[238:239]\n"
"s_nop 4\n"
"s_cbranch_vccz L_29_%=\n"
"s_mov_b32 s62, 40000\n"
"buffer_load_dwordx2 v[12:13], v165, s[84:87], s23 offen  sc1\n"
"L_30_%=:\n"
"s_waitcnt vmcnt(0)\n"
"v_cmp_ne_u64 vcc, s[60:61], v[12:13]\n"
"s_nop 4\n"
"s_cbranch_vccz L_29_%=\n"
"buffer_load_dwordx2 v[12:13], v165, s[84:87], s23 offen  sc1\n"
"s_sub_u32 s62, s62, 1\n"
"s_cbranch_scc0 L_30_%=\n"
"L_29_%=:\n"
"s_waitcnt lgkmcnt(8)\n"
"v_mfma_f32_16x16x32_bf16 v[152:155], v[20:23], v[58:61], v[152:155]\n"
"ds_read_b64_tr_b16 v[118:119], v252 offset:43136\n"
"ds_read_b64_tr_b16 v[120:121], v252 offset:46720\n"
"v_cmp_eq_u32 vcc, 1, v1\n"
"s_nop 1\n"
"v_cndmask_b32 v163, 0, v163, vcc\n"
"v_cmp_eq_u32 vcc, 0, v1\n"
"s_nop 1\n"
"v_cndmask_b32 v128, v163, v128, vcc\n"
"v_cndmask_b32 v129, 0, v129, vcc\n"
"buffer_load_dwordx4 v[166:169], v3, s[84:87], s21 offen  sc1\n"
"v_mfma_f32_16x16x32_bf16 v[156:159], v[24:27], v[58:61], v[156:159]\n"
"ds_read_b64_tr_b16 v[122:123], v252 offset:43168\n"
"ds_read_b64_tr_b16 v[124:125], v252 offset:46752\n"
"s_waitcnt lgkmcnt(8)\n"
"v_mfma_f32_16x16x32_bf16 v[130:133], v[28:31], v[62:65], v[130:133]\n"
"ds_read_b64_tr_b16 v[20:21], v17 offset:43008\n"
"ds_read_b64_tr_b16 v[22:23], v17 offset:46592\n"
"v_mfma_f32_16x16x32_bf16 v[134:137], v[32:35], v[62:65], v[134:137]\n"
"ds_read_b64_tr_b16 v[24:25], v252 offset:50176\n"
"ds_read_b64_tr_b16 v[26:27], v252 offset:53760\n"
"buffer_load_dwordx4 v[170:173], v3, s[84:87], s21 offen offset:1024 sc1\n"
"s_waitcnt lgkmcnt(8)\n"
"v_mfma_f32_16x16x32_bf16 v[138:141], v[110:113], v[62:65], v[138:141]\n"
"ds_read_b64_tr_b16 v[28:29], v252 offset:50208\n"
"ds_read_b64_tr_b16 v[30:31], v252 offset:53792\n"
"v_mfma_f32_16x16x32_bf16 v[144:147], v[114:117], v[62:65], v[144:147]\n"
"ds_read_b64_tr_b16 v[32:33], v252 offset:50240\n"
"ds_read_b64_tr_b16 v[34:35], v252 offset:53824\n"
"s_waitcnt lgkmcnt(8)\n"
"v_mfma_f32_16x16x32_bf16 v[148:151], v[118:121], v[62:65], v[148:151]\n"
"ds_read_b64_tr_b16 v[110:111], v252 offset:50272\n"
"ds_read_b64_tr_b16 v[112:113], v252 offset:53856\n"
"buffer_load_dwordx4 v[230:233], v234, s[84:87], s20 offen  sc1\n"
"v_mfma_f32_16x16x32_bf16 v[152:155], v[122:125], v[62:65], v[152:155]\n"
"ds_read_b64_tr_b16 v[114:115], v252 offset:50304\n"
"ds_read_b64_tr_b16 v[116:117], v252 offset:53888\n"
"s_waitcnt lgkmcnt(8)\n"
"v_mfma_f32_16x16x32_bf16 v[156:159], v[20:23], v[62:65], v[156:159]\n"
"ds_read_b64_tr_b16 v[118:119], v252 offset:50336\n"
"ds_read_b64_tr_b16 v[120:121], v252 offset:53920\n"
"v_mfma_f32_16x16x32_bf16 v[130:133], v[24:27], v[66:69], v[130:133]\n"
"ds_read_b64_tr_b16 v[122:123], v17 offset:50176\n"
"ds_read_b64_tr_b16 v[124:125], v17 offset:53760\n"
"buffer_load_dwordx4 v[234:237], v234, s[84:87], s20 offen offset:256 sc1\n"
"s_waitcnt lgkmcnt(8)\n"
"v_mfma_f32_16x16x32_bf16 v[134:137], v[28:31], v[66:69], v[134:137]\n"
"ds_read_b64_tr_b16 v[20:21], v16\n"
"ds_read_b64_tr_b16 v[22:23], v16 offset:3584\n"
"v_mfma_f32_16x16x32_bf16 v[138:141], v[32:35], v[66:69], v[138:141]\n"
"ds_read_b64_tr_b16 v[24:25], v16 offset:32\n"
"ds_read_b64_tr_b16 v[26:27], v16 offset:3616\n"
"s_waitcnt lgkmcnt(8)\n"
"v_mfma_f32_16x16x32_bf16 v[144:147], v[110:113], v[66:69], v[144:147]\n"
"ds_read_b64_tr_b16 v[28:29], v16 offset:64\n"
"L_pad12_%=:\n"
"s_cbranch_execz L_ret_%=\n"
"ds_read_b64_tr_b16 v[30:31], v16 offset:3648\n"
"s_xor_b32 s22, s21, 2048\n"
"buffer_load_dwordx4 v[174:177], v3, s[84:87], s22 offen  sc1\n"
"v_mfma_f32_16x16x32_bf16 v[148:151], v[114:117], v[66:69], v[148:151]\n"
"ds_read_b64_tr_b16 v[32:33], v16 offset:96\n"
"ds_read_b64_tr_b16 v[34:35], v16 offset:3680\n"
"s_waitcnt lgkmcnt(8)\n"
"v_mfma_f32_16x16x32_bf16 v[152:155], v[118:121], v[66:69], v[152:155]\n"
"ds_read_b64_tr_b16 v[110:111], v16 offset:128\n"
"ds_read_b64_tr_b16 v[112:113], v16 offset:3712\n"
"v_mfma_f32_16x16x32_bf16 v[156:159], v[122:125], v[66:69], v[156:159]\n"
"ds_read_b64_tr_b16 v[114:115], v16 offset:160\n"
"ds_read_b64_tr_b16 v[116:117], v16 offset:3744\n"
"buffer_load_dwordx4 v[178:181], v3, s[84:87], s22 offen offset:1024 sc1\n"
"s_waitcnt lgkmcnt(8)\n"
"v_mfma_f32_16x16x32_bf16 v[130:133], v[20:23], v[70:73], v[130:133]\n"
"ds_read_b64_tr_b16 v[118:119], v18\n"
"ds_read_b64_tr_b16 v[120:121], v18 offset:3584\n"
"v_mfma_f32_16x16x32_bf16 v[134:137], v[24:27], v[70:73], v[134:137]\n"
"ds_read_b64_tr_b16 v[122:123], v16 offset:7168\n"
"ds_read_b64_tr_b16 v[124:125], v16 offset:10752\n"
"s_waitcnt lgkmcnt(8)\n"
"v_mfma_f32_16x16x32_bf16 v[138:141], v[28:31], v[70:73], v[138:141]\n"
"ds_read_b64_tr_b16 v[20:21], v16 offset:7200\n"
"ds_read_b64_tr_b16 v[22:23], v16 offset:10784\n"
"s_xor_b32 s22, s21, 4096\n"
"buffer_load_dwordx4 v[182:185], v3, s[84:87], s22 offen  sc1\n"
"v_mfma_f32_16x16x32_bf16 v[144:147], v[32:35], v[70:73], v[144:147]\n"
"ds_read_b64_tr_b16 v[24:25], v16 offset:7232\n"
"ds_read_b64_tr_b16 v[26:27], v16 offset:10816\n"
"s_waitcnt lgkmcnt(8)\n"
"v_mfma_f32_16x16x32_bf16 v[148:151], v[110:113], v[70:73], v[148:151]\n"
"ds_read_b64_tr_b16 v[28:29], v16 offset:7264\n"
"ds_read_b64_tr_b16 v[30:31], v16 offset:10848\n"
"v_mfma_f32_16x16x32_bf16 v[152:155], v[114:117], v[70:73], v[152:155]\n"
"ds_read_b64_tr_b16 v[32:33], v16 offset:7296\n"
"ds_read_b64_tr_b16 v[34:35], v16 offset:10880\n"
"buffer_load_dwordx4 v[186:189], v3, s[84:87], s22 offen offset:1024 sc1\n"
"s_waitcnt lgkmcnt(8)\n"
"v_mfma_f32_16x16x32_bf16 v[156:159], v[118:121], v[70:73], v[156:159]\n"
"ds_read_b64_tr_b16 v[110:111], v16 offset:7328\n"
"ds_read_b64_tr_b16 v[112:113], v16 offset:10912\n"
"v_mfma_f32_16x16x32_bf16 v[130:133], v[122:125], v[74:77], v[130:133]\n"
"ds_read_b64_tr_b16 v[114:115], v18 offset:7168\n"
"ds_read_b64_tr_b16 v[116:117], v18 offset:10752\n"
"s_waitcnt lgkmcnt(8)\n"
"v_mfma_f32_16x16x32_bf16 v[134:137], v[20:23], v[74:77], v[134:137]\n"
"ds_read_b64_tr_b16 v[118:119], v16 offset:14336\n"
"ds_read_b64_tr_b16 v[120:121], v16 offset:17920\n"
"s_xor_b32 s22, s21, 6144\n"
"buffer_load_dwordx4 v[190:193], v3, s[84:87], s22 offen  sc1\n"
"v_mfma_f32_16x16x32_bf16 v[138:141], v[24:27], v[74:77], v[138:141]\n"
"ds_read_b64_tr_b16 v[122:123], v16 offset:14368\n"
"ds_read_b64_tr_b16 v[124:125], v16 offset:17952\n"
"s_waitcnt lgkmcnt(8)\n"
"v_mfma_f32_16x16x32_bf16 v[144:147], v[28:31], v[74:77], v[144:147]\n"
"ds_read_b64_tr_b16 v[20:21], v16 offset:14400\n"
"ds_read_b64_tr_b16 v[22:23], v16 offset:17984\n"
"v_mfma_f32_16x16x32_bf16 v[148:151], v[32:35], v[74:77], v[148:151]\n"
"ds_read_b64_tr_b16 v[24:25], v16 offset:14432\n"
"ds_read_b64_tr_b16 v[26:27], v16 offset:18016\n"
"buffer_load_dwordx4 v[194:197], v3, s[84:87], s22 offen offset:1024 sc1\n"
"s_waitcnt lgkmcnt(8)\n"
"v_mfma_f32_16x16x32_bf16 v[152:155], v[110:113], v[74:77], v[152:155]\n"
"ds_read_b64_tr_b16 v[28:29], v16 offset:14464\n"
"ds_read_b64_tr_b16 v[30:31], v16 offset:18048\n"
"v_mfma_f32_16x16x32_bf16 v[156:159], v[114:117], v[74:77], v[156:159]\n"
"ds_read_b64_tr_b16 v[32:33], v16 offset:14496\n"
"ds_read_b64_tr_b16 v[34:35], v16 offset:18080\n"
"s_waitcnt lgkmcnt(8)\n"
"v_mfma_f32_16x16x32_bf16 v[130:133], v[118:121], v[78:81], v[130:133]\n"
"ds_read_b64_tr_b16 v[110:111], v18 offset:14336\n"
"ds_read_b64_tr_b16 v[112:113], v18 offset:17920\n"
"s_xor_b32 s22, s21, 16384\n"
"buffer_load_dwordx4 v[198:201], v3, s[84:87], s22 offen  sc1\n"
"v_mfma_f32_16x16x32_bf16 v[134:137], v[122:125], v[78:81], v[134:137]\n"
"ds_read_b64_tr_b16 v[114:115], v16 offset:21504\n"
"ds_read_b64_tr_b16 v[116:117], v16 offset:25088\n"
"s_waitcnt lgkmcnt(8)\n"
"v_mfma_f32_16x16x32_bf16 v[138:141], v[20:23], v[78:81], v[138:141]\n"
"ds_read_b64_tr_b16 v[118:119], v16 offset:21536\n"
"ds_read_b64_tr_b16 v[120:121], v16 offset:25120\n"
"v_mfma_f32_16x16x32_bf16 v[144:147], v[24:27], v[78:81], v[144:147]\n"
"ds_read_b64_tr_b16 v[122:123], v16 offset:21568\n"
"ds_read_b64_tr_b16 v[124:125], v16 offset:25152\n"
"buffer_load_dwordx4 v[202:205], v3, s[84:87], s22 offen offset:1024 sc1\n"
"s_waitcnt lgkmcnt(8)\n"
"v_mfma_f32_16x16x32_bf16 v[148:151], v[28:31], v[78:81], v[148:151]\n"
"ds_read_b64_tr_b16 v[20:21], v16 offset:21600\n"
"ds_read_b64_tr_b16 v[22:23], v16 offset:25184\n"
"v_mfma_f32_16x16x32_bf16 v[152:155], v[32:35], v[78:81], v[152:155]\n"
"ds_read_b64_tr_b16 v[24:25], v16 offset:21632\n"
"ds_read_b64_tr_b16 v[26:27], v16 offset:25216\n"
"s_waitcnt lgkmcnt(8)\n"
"v_mfma_f32_16x16x32_bf16 v[156:159], v[110:113], v[78:81], v[156:159]\n"
"ds_read_b64_tr_b16 v[28:29], v16 offset:21664\n"
"ds_read_b64_tr_b16 v[30:31], v16 offset:25248\n"
"s_xor_b32 s22, s21, 18432\n"
"buffer_load_dwordx4 v[206:209], v3, s[84:87], s22 offen  sc1\n"
"v_mfma_f32_16x16x32_bf16 v[130:133], v[114:117], v[82:85], v[130:133]\n"
"ds_read_b64_tr_b16 v[32:33], v18 offset:21504\n"
"ds_read_b64_tr_b16 v[34:35], v18 offset:25088\n"
"s_waitcnt vmcnt(13)\n"
"s_barrier\n"
"s_waitcnt lgkmcnt(8)\n"
"v_mfma_f32_16x16x32_bf16 v[134:137], v[118:121], v[82:85], v[134:137]\n"
"ds_read_b64_tr_b16 v[110:111], v16 offset:28672\n"
"ds_read_b64_tr_b16 v[112:113], v16 offset:32256\n"
"v_mfma_f32_16x16x32_bf16 v[138:141], v[122:125], v[82:85], v[138:141]\n"
"ds_read_b64_tr_b16 v[114:115], v16 offset:28704\n"
"ds_read_b64_tr_b16 v[116:117], v16 offset:32288\n"
"buffer_load_dwordx4 v[210:213], v3, s[84:87], s22 offen offset:1024 sc1\n"
"s_waitcnt lgkmcnt(8)\n"
"v_mfma_f32_16x16x32_bf16 v[144:147], v[20:23], v[82:85], v[144:147]\n"
"ds_read_b64_tr_b16 v[118:119], v16 offset:28736\n"
"ds_read_b64_tr_b16 v[120:121], v16 offset:32320\n"
"v_mfma_f32_16x16x32_bf16 v[148:151], v[24:27], v[82:85], v[148:151]\n"
"ds_read_b64_tr_b16 v[122:123], v16 offset:28768\n"
"ds_read_b64_tr_b16 v[124:125], v16 offset:32352\n"
"s_waitcnt lgkmcnt(8)\n"
"v_mfma_f32_16x16x32_bf16 v[152:155], v[28:31], v[82:85], v[152:155]\n"
"ds_read_b64_tr_b16 v[20:21], v16 offset:28800\n"
"ds_read_b64_tr_b16 v[22:23], v16 offset:32384\n"
"s_xor_b32 s22, s21, 20480\n"
"buffer_load_dwordx4 v[214:217], v3, s[84:87], s22 offen  sc1\n"
"v_mfma_f32_16x16x32_bf16 v[156:159], v[32:35], v[82:85], v[156:159]\n"
"ds_read_b64_tr_b16 v[24:25], v16 offset:28832\n"
"ds_read_b64_tr_b16 v[26:27], v16 offset:32416\n"
"s_waitcnt lgkmcnt(8)\n"
"v_mfma_f32_16x16x32_bf16 v[130:133], v[110:113], v[86:89], v[130:133]\n"
"ds_read_b64_tr_b16 v[28:29], v18 offset:28672\n"
"ds_read_b64_tr_b16 v[30:31], v18 offset:32256\n"
"v_mfma_f32_16x16x32_bf16 v[134:137], v[114:117], v[86:89], v[134:137]\n"
"ds_read_b64_tr_b16 v[32:33], v16 offset:35840\n"
"ds_read_b64_tr_b16 v[34:35], v16 offset:39424\n"
"buffer_load_dwordx4 v[218:221], v3, s[84:87], s22 offen offset:1024 sc1\n"
"s_waitcnt lgkmcnt(8)\n"
"v_mfma_f32_16x16x32_bf16 v[138:141], v[118:121], v[86:89], v[138:141]\n"
"ds_read_b64_tr_b16 v[110:111], v16 offset:35872\n"
"ds_read_b64_tr_b16 v[112:113], v16 offset:39456\n"
"v_mfma_f32_16x16x32_bf16 v[144:147], v[122:125], v[86:89], v[144:147]\n"
"ds_read_b64_tr_b16 v[114:115], v16 offset:35904\n"
"ds_read_b64_tr_b16 v[116:117], v16 offset:39488\n"
"s_waitcnt lgkmcnt(8)\n"
"v_mfma_f32_16x16x32_bf16 v[148:151], v[20:23], v[86:89], v[148:151]\n"
"ds_read_b64_tr_b16 v[118:119], v16 offset:35936\n"
"ds_read_b64_tr_b16 v[120:121], v16 offset:39520\n"
"s_xor_b32 s22, s21, 22528\n"
"buffer_load_dwordx4 v[222:225], v3, s[84:87], s22 offen  sc1\n"
"v_mfma_f32_16x16x32_bf16 v[152:155], v[24:27], v[86:89], v[152:155]\n"
"ds_read_b64_tr_b16 v[122:123], v16 offset:35968\n"
"ds_read_b64_tr_b16 v[124:125], v16 offset:39552\n"
"s_waitcnt lgkmcnt(8)\n"
"v_mfma_f32_16x16x32_bf16 v[156:159], v[28:31], v[86:89], v[156:159]\n"
"ds_read_b64_tr_b16 v[20:21], v16 offset:36000\n"
"ds_read_b64_tr_b16 v[22:23], v16 offset:39584\n"
"v_mfma_f32_16x16x32_bf16 v[130:133], v[32:35], v[90:93], v[130:133]\n"
"ds_read_b64_tr_b16 v[24:25], v18 offset:35840\n"
"ds_read_b64_tr_b16 v[26:27], v18 offset:39424\n"
"buffer_load_dwordx4 v[226:229], v3, s[84:87], s22 offen offset:1024 sc1\n"
"s_waitcnt lgkmcnt(8)\n"
"v_mfma_f32_16x16x32_bf16 v[134:137], v[110:113], v[90:93], v[134:137]\n"
"ds_read_b64_tr_b16 v[28:29], v16 offset:43008\n"
"ds_read_b64_tr_b16 v[30:31], v16 offset:46592\n"
"v_mfma_f32_16x16x32_bf16 v[138:141], v[114:117], v[90:93], v[138:141]\n"
"ds_read_b64_tr_b16 v[32:33], v16 offset:43040\n"
"ds_read_b64_tr_b16 v[34:35], v16 offset:46624\n"
"s_waitcnt lgkmcnt(8)\n"
"v_mfma_f32_16x16x32_bf16 v[144:147], v[118:121], v[90:93], v[144:147]\n"
"ds_read_b64_tr_b16 v[110:111], v16 offset:43072\n"
"ds_read_b64_tr_b16 v[112:113], v16 offset:46656\n"
"v_mfma_f32_16x16x32_bf16 v[148:151], v[122:125], v[90:93], v[148:151]\n"
"ds_read_b64_tr_b16 v[114:115], v16 offset:43104\n"
"ds_read_b64_tr_b16 v[116:117], v16 offset:46688\n"
"s_waitcnt lgkmcnt(8)\n"
"v_mfma_f32_16x16x32_bf16 v[152:155], v[20:23], v[90:93], v[152:155]\n"
"ds_read_b64_tr_b16 v[118:119], v16 offset:43136\n"
"ds_read_b64_tr_b16 v[120:121], v16 offset:46720\n"
"v_mfma_f32_16x16x32_bf16 v[156:159], v[24:27], v[90:93], v[156:159]\n"
"ds_read_b64_tr_b16 v[122:123], v16 offset:43168\n"
"ds_read_b64_tr_b16 v[124:125], v16 offset:46752\n"
"s_waitcnt lgkmcnt(8)\n"
"v_mfma_f32_16x16x32_bf16 v[130:133], v[28:31], v[94:97], v[130:133]\n"
"ds_read_b64_tr_b16 v[20:21], v18 offset:43008\n"
"ds_read_b64_tr_b16 v[22:23], v18 offset:46592\n"
"v_mfma_f32_16x16x32_bf16 v[134:137], v[32:35], v[94:97], v[134:137]\n"
"ds_read_b64_tr_b16 v[24:25], v16 offset:50176\n"
"ds_read_b64_tr_b16 v[26:27], v16 offset:53760\n"
"s_waitcnt lgkmcnt(8)\n"
"v_mfma_f32_16x16x32_bf16 v[138:141], v[110:113], v[94:97], v[138:141]\n"
"ds_read_b64_tr_b16 v[28:29], v16 offset:50208\n"
"ds_read_b64_tr_b16 v[30:31], v16 offset:53792\n"
"v_mfma_f32_16x16x32_bf16 v[144:147], v[114:117], v[94:97], v[144:147]\n"
"ds_read_b64_tr_b16 v[32:33], v16 offset:50240\n"
"ds_read_b64_tr_b16 v[34:35], v16 offset:53824\n"
"s_waitcnt lgkmcnt(8)\n"
"v_mfma_f32_16x16x32_bf16 v[148:151], v[118:121], v[94:97], v[148:151]\n"
"ds_read_b64_tr_b16 v[110:111], v16 offset:50272\n"
"ds_read_b64_tr_b16 v[112:113], v16 offset:53856\n"
"v_mfma_f32_16x16x32_bf16 v[152:155], v[122:125], v[94:97], v[152:155]\n"
"ds_read_b64_tr_b16 v[114:115], v16 offset:50304\n"
"ds_read_b64_tr_b16 v[116:117], v16 offset:53888\n"
"v_mfma_f32_16x16x16_bf16 v[130:133], a[242:243], v[128:129], v[130:133]\n"
"v_mfma_f32_16x16x16_bf16 v[134:137], a[244:245], v[128:129], v[134:137]\n"
"v_mfma_f32_16x16x16_bf16 v[138:141], a[246:247], v[128:129], v[138:141]\n"
"v_mfma_f32_16x16x16_bf16 v[144:147], a[248:249], v[128:129], v[144:147]\n"
"v_mfma_f32_16x16x16_bf16 v[148:151], a[250:251], v[128:129], v[148:151]\n"
"v_mfma_f32_16x16x16_bf16 v[152:155], a[252:253], v[128:129], v[152:155]\n"
"v_mfma_f32_16x16x16_bf16 v[156:159], a[254:255], v[128:129], v[156:159]\n"
"s_waitcnt lgkmcnt(8)\n"
"s_nop 4\n"
"v_mfma_f32_16x16x32_bf16 v[156:159], v[20:23], v[94:97], v[156:159]\n"
"ds_read_b64_tr_b16 v[118:119], v16 offset:50336\n"
"ds_read_b64_tr_b16 v[120:121], v16 offset:53920\n"
"v_mfma_f32_16x16x32_bf16 v[130:133], v[24:27], v[98:101], v[130:133]\n"
"ds_read_b64_tr_b16 v[122:123], v18 offset:50176\n"
"ds_read_b64_tr_b16 v[124:125], v18 offset:53760\n"
"s_waitcnt lgkmcnt(8)\n"
"v_mfma_f32_16x16x32_bf16 v[134:137], v[28:31], v[98:101], v[134:137]\n"
"v_mfma_f32_16x16x32_bf16 v[138:141], v[32:35], v[98:101], v[138:141]\n"
"s_waitcnt lgkmcnt(4)\n"
"v_mfma_f32_16x16x32_bf16 v[144:147], v[110:113], v[98:101], v[144:147]\n"
"v_mfma_f32_16x16x32_bf16 v[148:151], v[114:117], v[98:101], v[148:151]\n"
"s_waitcnt lgkmcnt(0)\n"
"v_mfma_f32_16x16x32_bf16 v[152:155], v[118:121], v[98:101], v[152:155]\n"
"v_mfma_f32_16x16x32_bf16 v[156:159], v[122:125], v[98:101], v[156:159]\n"
"ds_read_b64_tr_b16 v[20:21], v252\n"
"ds_read_b64_tr_b16 v[22:23], v252 offset:3584\n"
"ds_read_b64_tr_b16 v[24:25], v252 offset:32\n"
"ds_read_b64_tr_b16 v[26:27], v252 offset:3616\n"
"ds_read_b64_tr_b16 v[28:29], v252 offset:64\n"
"ds_read_b64_tr_b16 v[30:31], v252 offset:3648\n"
"ds_read_b64_tr_b16 v[32:33], v252 offset:96\n"
"ds_read_b64_tr_b16 v[34:35], v252 offset:3680\n"
"ds_read_b64_tr_b16 v[110:111], v252 offset:7168\n"
"ds_read_b64_tr_b16 v[112:113], v252 offset:10752\n"
"ds_read_b64_tr_b16 v[114:115], v252 offset:7200\n"
"ds_read_b64_tr_b16 v[116:117], v252 offset:10784\n"
"v_pk_mul_f32 v[160:161], v[130:131], v[130:131] op_sel:[0,0] op_sel_hi:[1,1]\n"
"v_pk_mul_f32 v[162:163], v[132:133], v[132:133] op_sel:[0,0] op_sel_hi:[1,1]\n"
"v_pk_mul_f32 v[238:239], v[134:135], v[134:135] op_sel:[0,0] op_sel_hi:[1,1]\n"
"v_pk_mul_f32 v[240:241], v[136:137], v[136:137] op_sel:[0,0] op_sel_hi:[1,1]\n"
"v_pk_mul_f32 v[242:243], v[138:139], v[138:139] op_sel:[0,0] op_sel_hi:[1,1]\n"
"v_pk_mul_f32 v[244:245], v[140:141], v[140:141] op_sel:[0,0] op_sel_hi:[1,1]\n"
"v_pk_mul_f32 v[246:247], v[144:145], v[144:145] op_sel:[0,0] op_sel_hi:[1,1]\n"
"v_pk_mul_f32 v[248:249], v[146:147], v[146:147] op_sel:[0,0] op_sel_hi:[1,1]\n"
"v_pk_fma_f32 v[160:161], v[160:161], v[36:37], v[36:37] op_sel:[0,0,1] op_sel_hi:[1,0,1]\n"
"v_pk_fma_f32 v[162:163], v[162:163], v[36:37], v[36:37] op_sel:[0,0,1] op_sel_hi:[1,0,1]\n"
"v_pk_fma_f32 v[238:239], v[238:239], v[36:37], v[36:37] op_sel:[0,0,1] op_sel_hi:[1,0,1]\n"
"v_pk_fma_f32 v[240:241], v[240:241], v[36:37], v[36:37] op_sel:[0,0,1] op_sel_hi:[1,0,1]\n"
"v_pk_fma_f32 v[242:243], v[242:243], v[36:37], v[36:37] op_sel:[0,0,1] op_sel_hi:[1,0,1]\n"
"v_pk_fma_f32 v[244:245], v[244:245], v[36:37], v[36:37] op_sel:[0,0,1] op_sel_hi:[1,0,1]\n"
"v_pk_fma_f32 v[246:247], v[246:247], v[36:37], v[36:37] op_sel:[0,0,1] op_sel_hi:[1,0,1]\n"
"v_pk_fma_f32 v[248:249], v[248:249], v[36:37], v[36:37] op_sel:[0,0,1] op_sel_hi:[1,0,1]\n"
"v_pk_mul_f32 v[160:161], v[160:161], v[130:131] op_sel:[0,0] op_sel_hi:[1,1]\n"
"v_pk_mul_f32 v[162:163], v[162:163], v[132:133] op_sel:[0,0] op_sel_hi:[1,1]\n"
"v_pk_mul_f32 v[238:239], v[238:239], v[134:135] op_sel:[0,0] op_sel_hi:[1,1]\n"
"v_pk_mul_f32 v[240:241], v[240:241], v[136:137] op_sel:[0,0] op_sel_hi:[1,1]\n"
"v_pk_mul_f32 v[242:243], v[242:243], v[138:139] op_sel:[0,0] op_sel_hi:[1,1]\n"
"v_pk_mul_f32 v[244:245], v[244:245], v[140:141] op_sel:[0,0] op_sel_hi:[1,1]\n"
"v_pk_mul_f32 v[246:247], v[246:247], v[144:145] op_sel:[0,0] op_sel_hi:[1,1]\n"
"v_pk_mul_f32 v[248:249], v[248:249], v[146:147] op_sel:[0,0] op_sel_hi:[1,1]\n"
"v_exp_f32 v160, v160\n"
"v_exp_f32 v161, v161\n"
"v_exp_f32 v162, v162\n"
"v_exp_f32 v163, v163\n"
"v_exp_f32 v238, v238\n"
"v_exp_f32 v239, v239\n"
"v_exp_f32 v240, v240\n"
"v_exp_f32 v241, v241\n"
"v_exp_f32 v242, v242\n"
"v_exp_f32 v243, v243\n"
"v_exp_f32 v244, v244\n"
"v_exp_f32 v245, v245\n"
"v_exp_f32 v246, v246\n"
"v_exp_f32 v247, v247\n"
"v_exp_f32 v248, v248\n"
"v_exp_f32 v249, v249\n"
"v_pk_fma_f32 v[160:161], v[160:161], v[142:143], v[142:143] op_sel:[0,0,0] op_sel_hi:[1,0,0]\n"
"v_pk_fma_f32 v[162:163], v[162:163], v[142:143], v[142:143] op_sel:[0,0,0] op_sel_hi:[1,0,0]\n"
"v_pk_fma_f32 v[238:239], v[238:239], v[142:143], v[142:143] op_sel:[0,0,0] op_sel_hi:[1,0,0]\n"
"v_pk_fma_f32 v[240:241], v[240:241], v[142:143], v[142:143] op_sel:[0,0,0] op_sel_hi:[1,0,0]\n"
"v_pk_fma_f32 v[242:243], v[242:243], v[142:143], v[142:143] op_sel:[0,0,0] op_sel_hi:[1,0,0]\n"
"v_pk_fma_f32 v[244:245], v[244:245], v[142:143], v[142:143] op_sel:[0,0,0] op_sel_hi:[1,0,0]\n"
"v_pk_fma_f32 v[246:247], v[246:247], v[142:143], v[142:143] op_sel:[0,0,0] op_sel_hi:[1,0,0]\n"
"v_pk_fma_f32 v[248:249], v[248:249], v[142:143], v[142:143] op_sel:[0,0,0] op_sel_hi:[1,0,0]\n"
"v_rcp_f32 v160, v160\n"
"v_rcp_f32 v161, v161\n"
"v_rcp_f32 v162, v162\n"
"v_rcp_f32 v163, v163\n"
"v_rcp_f32 v238, v238\n"
"v_rcp_f32 v239, v239\n"
"v_rcp_f32 v240, v240\n"
"v_rcp_f32 v241, v241\n"
"v_rcp_f32 v242, v242\n"
"v_rcp_f32 v243, v243\n"
"v_rcp_f32 v244, v244\n"
"v_rcp_f32 v245, v245\n"
"v_rcp_f32 v246, v246\n"
"v_rcp_f32 v247, v247\n"
"v_rcp_f32 v248, v248\n"
"v_rcp_f32 v249, v249\n"
"v_pk_mul_f32 v[130:131], v[130:131], v[160:161] op_sel:[0,0] op_sel_hi:[1,1]\n"
"v_pk_mul_f32 v[132:133], v[132:133], v[162:163] op_sel:[0,0] op_sel_hi:[1,1]\n"
"v_pk_mul_f32 v[134:135], v[134:135], v[238:239] op_sel:[0,0] op_sel_hi:[1,1]\n"
"v_pk_mul_f32 v[136:137], v[136:137], v[240:241] op_sel:[0,0] op_sel_hi:[1,1]\n"
"v_pk_mul_f32 v[138:139], v[138:139], v[242:243] op_sel:[0,0] op_sel_hi:[1,1]\n"
"v_pk_mul_f32 v[140:141], v[140:141], v[244:245] op_sel:[0,0] op_sel_hi:[1,1]\n"
"v_pk_mul_f32 v[144:145], v[144:145], v[246:247] op_sel:[0,0] op_sel_hi:[1,1]\n"
"v_pk_mul_f32 v[146:147], v[146:147], v[248:249] op_sel:[0,0] op_sel_hi:[1,1]\n"
"v_cvt_pk_bf16_f32 v130, v130, v131\n"
"v_cvt_pk_bf16_f32 v131, v132, v133\n"
"v_cvt_pk_bf16_f32 v132, v134, v135\n"
"v_cvt_pk_bf16_f32 v133, v136, v137\n"
"buffer_store_dwordx4 v[130:133], v3, s[84:87], s98 offen  sc1\n"
"v_cvt_pk_bf16_f32 v138, v138, v139\n"
"v_cvt_pk_bf16_f32 v139, v140, v141\n"
"v_cvt_pk_bf16_f32 v140, v144, v145\n"
"v_cvt_pk_bf16_f32 v141, v146, v147\n"
"buffer_store_dwordx4 v[138:141], v3, s[84:87], s98 offen offset:1024 sc1\n"
"v_pk_mul_f32 v[160:161], v[148:149], v[148:149] op_sel:[0,0] op_sel_hi:[1,1]\n"
"v_pk_mul_f32 v[162:163], v[150:151], v[150:151] op_sel:[0,0] op_sel_hi:[1,1]\n"
"v_pk_mul_f32 v[238:239], v[152:153], v[152:153] op_sel:[0,0] op_sel_hi:[1,1]\n"
"v_pk_mul_f32 v[240:241], v[154:155], v[154:155] op_sel:[0,0] op_sel_hi:[1,1]\n"
"v_pk_mul_f32 v[242:243], v[156:157], v[156:157] op_sel:[0,0] op_sel_hi:[1,1]\n"
"v_pk_mul_f32 v[244:245], v[158:159], v[158:159] op_sel:[0,0] op_sel_hi:[1,1]\n"
"v_pk_fma_f32 v[160:161], v[160:161], v[36:37], v[36:37] op_sel:[0,0,1] op_sel_hi:[1,0,1]\n"
"v_pk_fma_f32 v[162:163], v[162:163], v[36:37], v[36:37] op_sel:[0,0,1] op_sel_hi:[1,0,1]\n"
"v_pk_fma_f32 v[238:239], v[238:239], v[36:37], v[36:37] op_sel:[0,0,1] op_sel_hi:[1,0,1]\n"
"v_pk_fma_f32 v[240:241], v[240:241], v[36:37], v[36:37] op_sel:[0,0,1] op_sel_hi:[1,0,1]\n"
"v_pk_fma_f32 v[242:243], v[242:243], v[36:37], v[36:37] op_sel:[0,0,1] op_sel_hi:[1,0,1]\n"
"v_pk_fma_f32 v[244:245], v[244:245], v[36:37], v[36:37] op_sel:[0,0,1] op_sel_hi:[1,0,1]\n"
"v_pk_mul_f32 v[160:161], v[160:161], v[148:149] op_sel:[0,0] op_sel_hi:[1,1]\n"
"v_pk_mul_f32 v[162:163], v[162:163], v[150:151] op_sel:[0,0] op_sel_hi:[1,1]\n"
"v_pk_mul_f32 v[238:239], v[238:239], v[152:153] op_sel:[0,0] op_sel_hi:[1,1]\n"
"v_pk_mul_f32 v[240:241], v[240:241], v[154:155] op_sel:[0,0] op_sel_hi:[1,1]\n"
"v_pk_mul_f32 v[242:243], v[242:243], v[156:157] op_sel:[0,0] op_sel_hi:[1,1]\n"
"v_pk_mul_f32 v[244:245], v[244:245], v[158:159] op_sel:[0,0] op_sel_hi:[1,1]\n"
"v_exp_f32 v160, v160\n"
"v_exp_f32 v161, v161\n"
"v_exp_f32 v162, v162\n"
"v_exp_f32 v163, v163\n"
"v_exp_f32 v238, v238\n"
"v_exp_f32 v239, v239\n"
"v_exp_f32 v240, v240\n"
"v_exp_f32 v241, v241\n"
"v_exp_f32 v242, v242\n"
"v_exp_f32 v243, v243\n"
"v_exp_f32 v244, v244\n"
"v_exp_f32 v245, v245\n"
"v_pk_fma_f32 v[160:161], v[160:161], v[142:143], v[142:143] op_sel:[0,0,0] op_sel_hi:[1,0,0]\n"
"v_pk_fma_f32 v[162:163], v[162:163], v[142:143], v[142:143] op_sel:[0,0,0] op_sel_hi:[1,0,0]\n"
"v_pk_fma_f32 v[238:239], v[238:239], v[142:143], v[142:143] op_sel:[0,0,0] op_sel_hi:[1,0,0]\n"
"v_pk_fma_f32 v[240:241], v[240:241], v[142:143], v[142:143] op_sel:[0,0,0] op_sel_hi:[1,0,0]\n"
"v_pk_fma_f32 v[242:243], v[242:243], v[142:143], v[142:143] op_sel:[0,0,0] op_sel_hi:[1,0,0]\n"
"v_pk_fma_f32 v[244:245], v[244:245], v[142:143], v[142:143] op_sel:[0,0,0] op_sel_hi:[1,0,0]\n"
"v_rcp_f32 v160, v160\n"
"v_rcp_f32 v161, v161\n"
"v_rcp_f32 v162, v162\n"
"v_rcp_f32 v163, v163\n"
"v_rcp_f32 v238, v238\n"
"v_rcp_f32 v239, v239\n"
"v_rcp_f32 v240, v240\n"
"v_rcp_f32 v241, v241\n"
"v_rcp_f32 v242, v242\n"
"v_rcp_f32 v243, v243\n"
"v_rcp_f32 v244, v244\n"
"v_rcp_f32 v245, v245\n"
"v_pk_mul_f32 v[148:149], v[148:149], v[160:161] op_sel:[0,0] op_sel_hi:[1,1]\n"
"v_pk_mul_f32 v[150:151], v[150:151], v[162:163] op_sel:[0,0] op_sel_hi:[1,1]\n"
"v_pk_mul_f32 v[152:153], v[152:153], v[238:239] op_sel:[0,0] op_sel_hi:[1,1]\n"
"v_pk_mul_f32 v[154:155], v[154:155], v[240:241] op_sel:[0,0] op_sel_hi:[1,1]\n"
"v_pk_mul_f32 v[156:157], v[156:157], v[242:243] op_sel:[0,0] op_sel_hi:[1,1]\n"
"v_pk_mul_f32 v[158:159], v[158:159], v[244:245] op_sel:[0,0] op_sel_hi:[1,1]\n"
"v_cvt_pk_bf16_f32 v148, v148, v149\n"
"v_cvt_pk_bf16_f32 v149, v150, v151\n"
"v_cvt_pk_bf16_f32 v150, v152, v153\n"
"v_cvt_pk_bf16_f32 v151, v154, v155\n"
"buffer_store_dwordx4 v[148:151], v3, s[84:87], s98 offen offset:2048 sc1\n"
"v_cvt_pk_bf16_f32 v156, v156, v157\n"
"v_cvt_pk_bf16_f32 v157, v158, v159\n"
"buffer_store_dwordx2 v[156:157], v3, s[84:87], s99 offen offset:3072 sc1\n"
"s_waitcnt lgkmcnt(8) vmcnt(21)\n"
"v_mfma_f32_16x16x32_bf16 v[160:163], v[20:23], v[166:169], 0\n"
"ds_read_b64_tr_b16 v[118:119], v252 offset:7232\n"
"ds_read_b64_tr_b16 v[120:121], v252 offset:10816\n"
"s_waitcnt vmcnt(19)\n"
"v_add_f32 v230, v230, v232\n"
"v_mfma_f32_16x16x32_bf16 v[238:241], v[24:27], v[166:169], 0\n"
"ds_read_b64_tr_b16 v[122:123], v252 offset:7264\n"
"ds_read_b64_tr_b16 v[124:125], v252 offset:10848\n"
"v_add_f32 v231, v231, v233\n"
"s_waitcnt lgkmcnt(8)\n"
"v_mfma_f32_16x16x32_bf16 v[242:245], v[28:31], v[166:169], 0\n"
"ds_read_b64_tr_b16 v[20:21], v252 offset:14336\n"
"ds_read_b64_tr_b16 v[22:23], v252 offset:17920\n"
"s_waitcnt vmcnt(18)\n"
"v_add_f32 v234, v234, v236\n"
"v_mfma_f32_16x16x32_bf16 v[246:249], v[32:35], v[166:169], 0\n"
"ds_read_b64_tr_b16 v[24:25], v252 offset:14368\n"
"ds_read_b64_tr_b16 v[26:27], v252 offset:17952\n"
"v_add_f32 v235, v235, v237\n"
"s_waitcnt lgkmcnt(8)\n"
"v_mfma_f32_16x16x32_bf16 v[160:163], v[110:113], v[170:173], v[160:163]\n"
"ds_read_b64_tr_b16 v[28:29], v252 offset:14400\n"
"ds_read_b64_tr_b16 v[30:31], v252 offset:17984\n"
"v_add_f32 v230, v230, v234\n"
"v_mfma_f32_16x16x32_bf16 v[238:241], v[114:117], v[170:173], v[238:241]\n"
"ds_read_b64_tr_b16 v[32:33], v252 offset:14432\n"
"ds_read_b64_tr_b16 v[34:35], v252 offset:18016\n"
"v_add_f32 v231, v231, v235\n"
"s_waitcnt lgkmcnt(8)\n"
"v_mfma_f32_16x16x32_bf16 v[242:245], v[118:121], v[170:173], v[242:245]\n"
"ds_read_b64_tr_b16 v[110:111], v252 offset:21504\n"
"ds_read_b64_tr_b16 v[112:113], v252 offset:25088\n"
"v_permlane32_swap_b32 v230, v231\n"
"v_mfma_f32_16x16x32_bf16 v[246:249], v[122:125], v[170:173], v[246:249]\n"
"ds_read_b64_tr_b16 v[114:115], v252 offset:21536\n"
"ds_read_b64_tr_b16 v[116:117], v252 offset:25120\n"
"v_add_f32 v230, v230, v231\n"
"s_waitcnt lgkmcnt(8) vmcnt(17)\n"
"v_mfma_f32_16x16x32_bf16 v[160:163], v[20:23], v[174:177], v[160:163]\n"
"ds_read_b64_tr_b16 v[118:119], v252 offset:21568\n"
"ds_read_b64_tr_b16 v[120:121], v252 offset:25152\n"
"v_mov_b32 v231, v230\n"
"v_mfma_f32_16x16x32_bf16 v[238:241], v[24:27], v[174:177], v[238:241]\n"
"ds_read_b64_tr_b16 v[122:123], v252 offset:21600\n"
"ds_read_b64_tr_b16 v[124:125], v252 offset:25184\n"
"v_permlane16_swap_b32 v230, v231\n"
"s_waitcnt lgkmcnt(8)\n"
"v_mfma_f32_16x16x32_bf16 v[242:245], v[28:31], v[174:177], v[242:245]\n"
"ds_read_b64_tr_b16 v[20:21], v252 offset:28672\n"
"ds_read_b64_tr_b16 v[22:23], v252 offset:32256\n"
"v_add_f32 v230, v230, v231\n"
"v_mfma_f32_16x16x32_bf16 v[246:249], v[32:35], v[174:177], v[246:249]\n"
"ds_read_b64_tr_b16 v[24:25], v252 offset:28704\n"
"ds_read_b64_tr_b16 v[26:27], v252 offset:32288\n"
"v_mov_b32 v231, v230\n"
"s_waitcnt lgkmcnt(8) vmcnt(16)\n"
"v_mfma_f32_16x16x32_bf16 v[160:163], v[110:113], v[178:181], v[160:163]\n"
"ds_read_b64_tr_b16 v[28:29], v252 offset:28736\n"
"ds_read_b64_tr_b16 v[30:31], v252 offset:32320\n"
"v_permlane32_swap_b32 v230, v231\n"
"v_mfma_f32_16x16x32_bf16 v[238:241], v[114:117], v[178:181], v[238:241]\n"
"ds_read_b64_tr_b16 v[32:33], v252 offset:28768\n"
"ds_read_b64_tr_b16 v[34:35], v252 offset:32352\n"
"v_mul_f32 v230, 0x3b000000, v230\n"
"s_waitcnt lgkmcnt(8)\n"
"v_mfma_f32_16x16x32_bf16 v[242:245], v[118:121], v[178:181], v[242:245]\n"
"ds_read_b64_tr_b16 v[110:111], v252 offset:35840\n"
"ds_read_b64_tr_b16 v[112:113], v252 offset:39424\n"
"v_mul_f32 v231, 0x3b000000, v231\n"
"v_mfma_f32_16x16x32_bf16 v[246:249], v[122:125], v[178:181], v[246:249]\n"
"ds_read_b64_tr_b16 v[114:115], v252 offset:35872\n"
"ds_read_b64_tr_b16 v[116:117], v252 offset:39456\n"
"v_fma_f32 v231, -v230, v230, v231\n"
"s_waitcnt lgkmcnt(8) vmcnt(15)\n"
"v_mfma_f32_16x16x32_bf16 v[160:163], v[20:23], v[182:185], v[160:163]\n"
"ds_read_b64_tr_b16 v[118:119], v252 offset:35904\n"
"ds_read_b64_tr_b16 v[120:121], v252 offset:39488\n"
"v_max_f32 v231, 0, v231\n"
"v_mfma_f32_16x16x32_bf16 v[238:241], v[24:27], v[182:185], v[238:241]\n"
"ds_read_b64_tr_b16 v[122:123], v252 offset:35936\n"
"ds_read_b64_tr_b16 v[124:125], v252 offset:39520\n"
"v_add_f32 v19, 0x3727c5ac, v231\n"
"s_waitcnt lgkmcnt(8)\n"
"v_mfma_f32_16x16x32_bf16 v[242:245], v[28:31], v[182:185], v[242:245]\n"
"ds_read_b64_tr_b16 v[20:21], v252 offset:43008\n"
"ds_read_b64_tr_b16 v[22:23], v252 offset:46592\n"
"v_rsq_f32 v19, v19\n"
"v_mfma_f32_16x16x32_bf16 v[246:249], v[32:35], v[182:185], v[246:249]\n"
"ds_read_b64_tr_b16 v[24:25], v252 offset:43040\n"
"ds_read_b64_tr_b16 v[26:27], v252 offset:46624\n"
"v_mul_f32 v231, v231, v19\n"
"s_waitcnt lgkmcnt(8) vmcnt(14)\n"
"v_mfma_f32_16x16x32_bf16 v[160:163], v[110:113], v[186:189], v[160:163]\n"
"ds_read_b64_tr_b16 v[28:29], v252 offset:43072\n"
"ds_read_b64_tr_b16 v[30:31], v252 offset:46656\n"
"v_fmaak_f32 v231, v231, v19, 0x3727c5ac\n"
"v_mfma_f32_16x16x32_bf16 v[238:241], v[114:117], v[186:189], v[238:241]\n"
"ds_read_b64_tr_b16 v[32:33], v252 offset:43104\n"
"ds_read_b64_tr_b16 v[34:35], v252 offset:46688\n"
"v_rsq_f32 v231, v231\n"
"s_waitcnt lgkmcnt(8)\n"
"v_mfma_f32_16x16x32_bf16 v[242:245], v[118:121], v[186:189], v[242:245]\n"
"ds_read_b64_tr_b16 v[110:111], v252 offset:50176\n"
"ds_read_b64_tr_b16 v[112:113], v252 offset:53760\n"
"v_mul_f32 v231, v231, v19\n"
"v_mfma_f32_16x16x32_bf16 v[246:249], v[122:125], v[186:189], v[246:249]\n"
"ds_read_b64_tr_b16 v[114:115], v252 offset:50208\n"
"ds_read_b64_tr_b16 v[116:117], v252 offset:53792\n"
"v_rcp_f32 v253, v231\n"
"s_waitcnt lgkmcnt(8) vmcnt(13)\n"
"v_mfma_f32_16x16x32_bf16 v[160:163], v[20:23], v[190:193], v[160:163]\n"
"ds_read_b64_tr_b16 v[118:119], v252 offset:50240\n"
"ds_read_b64_tr_b16 v[120:121], v252 offset:53824\n"
"v_mul_f32 v230, -1.0, v230\n"
"v_mfma_f32_16x16x32_bf16 v[238:241], v[24:27], v[190:193], v[238:241]\n"
"ds_read_b64_tr_b16 v[122:123], v252 offset:50272\n"
"ds_read_b64_tr_b16 v[124:125], v252 offset:53856\n"
"v_mul_f32 v127, 0xc0135761, v231\n"
"s_waitcnt lgkmcnt(8)\n"
"v_mfma_f32_16x16x32_bf16 v[242:245], v[28:31], v[190:193], v[242:245]\n"
"ds_read_b64_tr_b16 v[20:21], v16\n"
"ds_read_b64_tr_b16 v[22:23], v16 offset:3584\n"
"v_mul_f32 v164, v231, v231\n"
"v_mfma_f32_16x16x32_bf16 v[246:249], v[32:35], v[190:193], v[246:249]\n"
"ds_read_b64_tr_b16 v[24:25], v16 offset:32\n"
"ds_read_b64_tr_b16 v[26:27], v16 offset:3616\n"
"v_cvt_pk_bf16_f32 v128, v230, v230\n"
"s_waitcnt lgkmcnt(8) vmcnt(12)\n"
"v_mfma_f32_16x16x32_bf16 v[160:163], v[110:113], v[194:197], v[160:163]\n"
"ds_read_b64_tr_b16 v[28:29], v16 offset:64\n"
"ds_read_b64_tr_b16 v[30:31], v16 offset:3648\n"
"v_and_b32 v250, 0xffff0000, v128\n"
"v_mfma_f32_16x16x32_bf16 v[238:241], v[114:117], v[194:197], v[238:241]\n"
"ds_read_b64_tr_b16 v[32:33], v16 offset:96\n"
"ds_read_b64_tr_b16 v[34:35], v16 offset:3680\n"
"v_sub_f32 v250, v230, v250\n"
"s_waitcnt lgkmcnt(8)\n"
"v_mfma_f32_16x16x32_bf16 v[242:245], v[118:121], v[194:197], v[242:245]\n"
"ds_read_b64_tr_b16 v[110:111], v16 offset:7168\n"
"ds_read_b64_tr_b16 v[112:113], v16 offset:10752\n"
"v_cvt_pk_bf16_f32 v129, v250, v250\n"
"v_mfma_f32_16x16x32_bf16 v[246:249], v[122:125], v[194:197], v[246:249]\n"
"ds_read_b64_tr_b16 v[114:115], v16 offset:7200\n"
"ds_read_b64_tr_b16 v[116:117], v16 offset:10784\n"
"v_mul_f32 v164, 0x3d372713, v164\n"
"s_waitcnt lgkmcnt(8) vmcnt(11)\n"
"v_mfma_f32_16x16x32_bf16 v[160:163], v[20:23], v[198:201], v[160:163]\n"
"ds_read_b64_tr_b16 v[118:119], v16 offset:7232\n"
"ds_read_b64_tr_b16 v[120:121], v16 offset:10816\n"
"v_mul_f32 v126, v164, v127\n"
"v_mfma_f32_16x16x32_bf16 v[238:241], v[24:27], v[198:201], v[238:241]\n"
"ds_read_b64_tr_b16 v[122:123], v16 offset:7264\n"
"ds_read_b64_tr_b16 v[124:125], v16 offset:10848\n"
"v_and_b32 v164, 0xffff0000, v253\n"
"s_waitcnt lgkmcnt(8)\n"
"v_mfma_f32_16x16x32_bf16 v[242:245], v[28:31], v[198:201], v[242:245]\n"
"ds_read_b64_tr_b16 v[20:21], v16 offset:14336\n"
"ds_read_b64_tr_b16 v[22:23], v16 offset:17920\n"
"v_sub_f32 v165, v253, v164\n"
"v_mfma_f32_16x16x32_bf16 v[246:249], v[32:35], v[198:201], v[246:249]\n"
"ds_read_b64_tr_b16 v[24:25], v16 offset:14368\n"
"ds_read_b64_tr_b16 v[26:27], v16 offset:17952\n"
"v_cvt_pk_bf16_f32 v165, v164, v165\n"
"s_waitcnt lgkmcnt(8) vmcnt(10)\n"
"v_mfma_f32_16x16x32_bf16 v[160:163], v[110:113], v[202:205], v[160:163]\n"
"ds_read_b64_tr_b16 v[28:29], v16 offset:14400\n"
"ds_read_b64_tr_b16 v[30:31], v16 offset:17984\n"
"v_cmp_eq_u32 vcc, 1, v1\n"
"s_nop 1\n"
"v_cndmask_b32 v165, 0, v165, vcc\n"
"v_cmp_eq_u32 vcc, 0, v1\n"
"s_nop 1\n"
"v_cndmask_b32 v128, v165, v128, vcc\n"
"v_cndmask_b32 v129, 0, v129, vcc\n"
"v_mfma_f32_16x16x32_bf16 v[238:241], v[114:117], v[202:205], v[238:241]\n"
"ds_read_b64_tr_b16 v[32:33], v16 offset:14432\n"
"ds_read_b64_tr_b16 v[34:35], v16 offset:18016\n"
"s_waitcnt lgkmcnt(8)\n"
"v_mfma_f32_16x16x32_bf16 v[242:245], v[118:121], v[202:205], v[242:245]\n"
"ds_read_b64_tr_b16 v[110:111], v16 offset:21504\n"
"ds_read_b64_tr_b16 v[112:113], v16 offset:25088\n"
"v_mfma_f32_16x16x32_bf16 v[246:249], v[122:125], v[202:205], v[246:249]\n"
"ds_read_b64_tr_b16 v[114:115], v16 offset:21536\n"
"ds_read_b64_tr_b16 v[116:117], v16 offset:25120\n"
"s_waitcnt vmcnt(0)\n"
"v_mov_b32 v50, s60\n"
"v_mov_b32 v51, s61\n"
"s_add_u32 s9, s78, 1343576\n"
"s_barrier\n"
"s_mov_b64 exec, 1\n"
"buffer_store_dwordx2 v[50:51], off, s[84:87], s9 sc1\n"
"s_mov_b64 exec, -1\n"
"s_waitcnt lgkmcnt(8)\n"
"v_mfma_f32_16x16x32_bf16 v[160:163], v[20:23], v[206:209], v[160:163]\n"
"ds_read_b64_tr_b16 v[118:119], v16 offset:21568\n"
"ds_read_b64_tr_b16 v[120:121], v16 offset:25152\n"
"v_mfma_f32_16x16x32_bf16 v[238:241], v[24:27], v[206:209], v[238:241]\n"
"ds_read_b64_tr_b16 v[122:123], v16 offset:21600\n"
"ds_read_b64_tr_b16 v[124:125], v16 offset:25184\n"
"s_waitcnt lgkmcnt(8)\n"
"v_mfma_f32_16x16x32_bf16 v[242:245], v[28:31], v[206:209], v[242:245]\n"
"ds_read_b64_tr_b16 v[20:21], v16 offset:28672\n"
"ds_read_b64_tr_b16 v[22:23], v16 offset:32256\n"
"L_pad13_%=:\n"
"s_cbranch_execz L_ret_%=\n"
"v_mfma_f32_16x16x32_bf16 v[246:249], v[32:35], v[206:209], v[246:249]\n"
"ds_read_b64_tr_b16 v[24:25], v16 offset:28704\n"
"ds_read_b64_tr_b16 v[26:27], v16 offset:32288\n"
"s_waitcnt lgkmcnt(8)\n"
"v_mfma_f32_16x16x32_bf16 v[160:163], v[110:113], v[210:213], v[160:163]\n"
"ds_read_b64_tr_b16 v[28:29], v16 offset:28736\n"
"ds_read_b64_tr_b16 v[30:31], v16 offset:32320\n"
"v_mfma_f32_16x16x32_bf16 v[238:241], v[114:117], v[210:213], v[238:241]\n"
"ds_read_b64_tr_b16 v[32:33], v16 offset:28768\n"
"ds_read_b64_tr_b16 v[34:35], v16 offset:32352\n"
"s_waitcnt lgkmcnt(8)\n"
"v_mfma_f32_16x16x32_bf16 v[242:245], v[118:121], v[210:213], v[242:245]\n"
"ds_read_b64_tr_b16 v[110:111], v16 offset:35840\n"
"ds_read_b64_tr_b16 v[112:113], v16 offset:39424\n"
"v_mfma_f32_16x16x32_bf16 v[246:249], v[122:125], v[210:213], v[246:249]\n"
"ds_read_b64_tr_b16 v[114:115], v16 offset:35872\n"
"ds_read_b64_tr_b16 v[116:117], v16 offset:39456\n"
"s_waitcnt lgkmcnt(8)\n"
"v_mfma_f32_16x16x32_bf16 v[160:163], v[20:23], v[214:217], v[160:163]\n"
"ds_read_b64_tr_b16 v[118:119], v16 offset:35904\n"
"ds_read_b64_tr_b16 v[120:121], v16 offset:39488\n"
"v_mfma_f32_16x16x32_bf16 v[238:241], v[24:27], v[214:217], v[238:241]\n"
"ds_read_b64_tr_b16 v[122:123], v16 offset:35936\n"
"ds_read_b64_tr_b16 v[124:125], v16 offset:39520\n"
"s_waitcnt lgkmcnt(8)\n"
"v_mfma_f32_16x16x32_bf16 v[242:245], v[28:31], v[214:217], v[242:245]\n"
"ds_read_b64_tr_b16 v[20:21], v16 offset:43008\n"
"ds_read_b64_tr_b16 v[22:23], v16 offset:46592\n"
"v_mfma_f32_16x16x32_bf16 v[246:249], v[32:35], v[214:217], v[246:249]\n"
"ds_read_b64_tr_b16 v[24:25], v16 offset:43040\n"
"ds_read_b64_tr_b16 v[26:27], v16 offset:46624\n"
"s_waitcnt lgkmcnt(8)\n"
"v_mfma_f32_16x16x32_bf16 v[160:163], v[110:113], v[218:221], v[160:163]\n"
"ds_read_b64_tr_b16 v[28:29], v16 offset:43072\n"
"ds_read_b64_tr_b16 v[30:31], v16 offset:46656\n"
"v_mfma_f32_16x16x32_bf16 v[238:241], v[114:117], v[218:221], v[238:241]\n"
"ds_read_b64_tr_b16 v[32:33], v16 offset:43104\n"
"ds_read_b64_tr_b16 v[34:35], v16 offset:46688\n"
"s_waitcnt lgkmcnt(8)\n"
"v_mfma_f32_16x16x32_bf16 v[242:245], v[118:121], v[218:221], v[242:245]\n"
"ds_read_b64_tr_b16 v[110:111], v16 offset:50176\n"
"ds_read_b64_tr_b16 v[112:113], v16 offset:53760\n"
"v_mfma_f32_16x16x32_bf16 v[246:249], v[122:125], v[218:221], v[246:249]\n"
"ds_read_b64_tr_b16 v[114:115], v16 offset:50208\n"
"ds_read_b64_tr_b16 v[116:117], v16 offset:53792\n"
"s_waitcnt lgkmcnt(8)\n"
"v_mfma_f32_16x16x32_bf16 v[160:163], v[20:23], v[222:225], v[160:163]\n"
"ds_read_b64_tr_b16 v[118:119], v16 offset:50240\n"
"ds_read_b64_tr_b16 v[120:121], v16 offset:53824\n"
"v_mfma_f32_16x16x32_bf16 v[238:241], v[24:27], v[222:225], v[238:241]\n"
"ds_read_b64_tr_b16 v[122:123], v16 offset:50272\n"
"ds_read_b64_tr_b16 v[124:125], v16 offset:53856\n"
"s_nop 0\n"
"v_mfma_f32_16x16x16_bf16 v[160:163], a[242:243], v[128:129], v[160:163]\n"
"s_nop 1\n"
"v_mfma_f32_16x16x16_bf16 v[238:241], a[244:245], v[128:129], v[238:241]\n"
"v_mfma_f32_16x16x16_bf16 v[242:245], a[246:247], v[128:129], v[242:245]\n"
"v_mfma_f32_16x16x16_bf16 v[246:249], a[248:249], v[128:129], v[246:249]\n"
"s_waitcnt lgkmcnt(8)\n"
"s_nop 3\n"
"v_mfma_f32_16x16x32_bf16 v[242:245], v[28:31], v[222:225], v[242:245]\n"
"v_mfma_f32_16x16x32_bf16 v[246:249], v[32:35], v[222:225], v[246:249]\n"
"s_waitcnt lgkmcnt(4)\n"
"v_mfma_f32_16x16x32_bf16 v[160:163], v[110:113], v[226:229], v[160:163]\n"
"v_mfma_f32_16x16x32_bf16 v[238:241], v[114:117], v[226:229], v[238:241]\n"
"s_waitcnt lgkmcnt(0)\n"
"v_mfma_f32_16x16x32_bf16 v[242:245], v[118:121], v[226:229], v[242:245]\n"
"v_mfma_f32_16x16x32_bf16 v[246:249], v[122:125], v[226:229], v[246:249]\n"
"v_and_b32 v19, 3, v2\n"
"v_lshlrev_b32 v19, 7, v19\n"
"s_lshl_b32 s12, s34, 9\n"
"s_add_u32 s12, s12, 1343576\n"
"ds_read_b64_tr_b16 v[20:21], v252 offset:128\n"
"ds_read_b64_tr_b16 v[22:23], v252 offset:3712\n"
"ds_read_b64_tr_b16 v[24:25], v252 offset:160\n"
"ds_read_b64_tr_b16 v[26:27], v252 offset:3744\n"
"ds_read_b64_tr_b16 v[28:29], v17\n"
"ds_read_b64_tr_b16 v[30:31], v17 offset:3584\n"
"ds_read_b64_tr_b16 v[32:33], v252 offset:7296\n"
"ds_read_b64_tr_b16 v[34:35], v252 offset:10880\n"
"ds_read_b64_tr_b16 v[110:111], v252 offset:7328\n"
"ds_read_b64_tr_b16 v[112:113], v252 offset:10912\n"
"ds_read_b64_tr_b16 v[114:115], v17 offset:7168\n"
"ds_read_b64_tr_b16 v[116:117], v17 offset:10752\n"
"v_pk_mul_f32 v[130:131], v[160:161], v[160:161] op_sel:[0,0] op_sel_hi:[1,1]\n"
"v_pk_mul_f32 v[132:133], v[162:163], v[162:163] op_sel:[0,0] op_sel_hi:[1,1]\n"
"v_pk_mul_f32 v[134:135], v[238:239], v[238:239] op_sel:[0,0] op_sel_hi:[1,1]\n"
"v_pk_mul_f32 v[136:137], v[240:241], v[240:241] op_sel:[0,0] op_sel_hi:[1,1]\n"
"v_pk_mul_f32 v[138:139], v[242:243], v[242:243] op_sel:[0,0] op_sel_hi:[1,1]\n"
"v_pk_mul_f32 v[140:141], v[244:245], v[244:245] op_sel:[0,0] op_sel_hi:[1,1]\n"
"v_pk_mul_f32 v[144:145], v[246:247], v[246:247] op_sel:[0,0] op_sel_hi:[1,1]\n"
"v_pk_mul_f32 v[146:147], v[248:249], v[248:249] op_sel:[0,0] op_sel_hi:[1,1]\n"
"v_pk_fma_f32 v[130:131], v[130:131], v[126:127], v[126:127] op_sel:[0,0,1] op_sel_hi:[1,0,1]\n"
"v_pk_fma_f32 v[132:133], v[132:133], v[126:127], v[126:127] op_sel:[0,0,1] op_sel_hi:[1,0,1]\n"
"v_pk_fma_f32 v[134:135], v[134:135], v[126:127], v[126:127] op_sel:[0,0,1] op_sel_hi:[1,0,1]\n"
"v_pk_fma_f32 v[136:137], v[136:137], v[126:127], v[126:127] op_sel:[0,0,1] op_sel_hi:[1,0,1]\n"
"v_pk_fma_f32 v[138:139], v[138:139], v[126:127], v[126:127] op_sel:[0,0,1] op_sel_hi:[1,0,1]\n"
"v_pk_fma_f32 v[140:141], v[140:141], v[126:127], v[126:127] op_sel:[0,0,1] op_sel_hi:[1,0,1]\n"
"v_pk_fma_f32 v[144:145], v[144:145], v[126:127], v[126:127] op_sel:[0,0,1] op_sel_hi:[1,0,1]\n"
"v_pk_fma_f32 v[146:147], v[146:147], v[126:127], v[126:127] op_sel:[0,0,1] op_sel_hi:[1,0,1]\n"
"v_pk_mul_f32 v[130:131], v[130:131], v[160:161] op_sel:[0,0] op_sel_hi:[1,1]\n"
"v_pk_mul_f32 v[132:133], v[132:133], v[162:163] op_sel:[0,0] op_sel_hi:[1,1]\n"
"v_pk_mul_f32 v[134:135], v[134:135], v[238:239] op_sel:[0,0] op_sel_hi:[1,1]\n"
"v_pk_mul_f32 v[136:137], v[136:137], v[240:241] op_sel:[0,0] op_sel_hi:[1,1]\n"
"v_pk_mul_f32 v[138:139], v[138:139], v[242:243] op_sel:[0,0] op_sel_hi:[1,1]\n"
"v_pk_mul_f32 v[140:141], v[140:141], v[244:245] op_sel:[0,0] op_sel_hi:[1,1]\n"
"v_pk_mul_f32 v[144:145], v[144:145], v[246:247] op_sel:[0,0] op_sel_hi:[1,1]\n"
"v_pk_mul_f32 v[146:147], v[146:147], v[248:249] op_sel:[0,0] op_sel_hi:[1,1]\n"
"v_exp_f32 v130, v130\n"
"v_exp_f32 v131, v131\n"
"v_exp_f32 v132, v132\n"
"v_exp_f32 v133, v133\n"
"v_exp_f32 v134, v134\n"
"v_exp_f32 v135, v135\n"
"v_exp_f32 v136, v136\n"
"v_exp_f32 v137, v137\n"
"v_exp_f32 v138, v138\n"
"v_exp_f32 v139, v139\n"
"v_exp_f32 v140, v140\n"
"v_exp_f32 v141, v141\n"
"v_exp_f32 v144, v144\n"
"v_exp_f32 v145, v145\n"
"v_exp_f32 v146, v146\n"
"v_exp_f32 v147, v147\n"
"v_pk_fma_f32 v[130:131], v[130:131], v[252:253], v[252:253] op_sel:[0,1,1] op_sel_hi:[1,1,1]\n"
"buffer_load_dwordx2 v[164:165], v19, s[84:87], s12 offen  sc1\n"
"v_pk_fma_f32 v[132:133], v[132:133], v[252:253], v[252:253] op_sel:[0,1,1] op_sel_hi:[1,1,1]\n"
"v_pk_fma_f32 v[134:135], v[134:135], v[252:253], v[252:253] op_sel:[0,1,1] op_sel_hi:[1,1,1]\n"
"v_pk_fma_f32 v[136:137], v[136:137], v[252:253], v[252:253] op_sel:[0,1,1] op_sel_hi:[1,1,1]\n"
"v_pk_fma_f32 v[138:139], v[138:139], v[252:253], v[252:253] op_sel:[0,1,1] op_sel_hi:[1,1,1]\n"
"v_pk_fma_f32 v[140:141], v[140:141], v[252:253], v[252:253] op_sel:[0,1,1] op_sel_hi:[1,1,1]\n"
"v_pk_fma_f32 v[144:145], v[144:145], v[252:253], v[252:253] op_sel:[0,1,1] op_sel_hi:[1,1,1]\n"
"v_pk_fma_f32 v[146:147], v[146:147], v[252:253], v[252:253] op_sel:[0,1,1] op_sel_hi:[1,1,1]\n"
"v_rcp_f32 v130, v130\n"
"v_rcp_f32 v131, v131\n"
"v_rcp_f32 v132, v132\n"
"v_rcp_f32 v133, v133\n"
"v_rcp_f32 v134, v134\n"
"v_rcp_f32 v135, v135\n"
"v_rcp_f32 v136, v136\n"
"v_rcp_f32 v137, v137\n"
"v_rcp_f32 v138, v138\n"
"v_rcp_f32 v139, v139\n"
"v_rcp_f32 v140, v140\n"
"v_rcp_f32 v141, v141\n"
"v_rcp_f32 v144, v144\n"
"v_rcp_f32 v145, v145\n"
"v_rcp_f32 v146, v146\n"
"v_rcp_f32 v147, v147\n"
"v_pk_mul_f32 v[160:161], v[160:161], v[130:131] op_sel:[0,0] op_sel_hi:[1,1]\n"
"v_pk_mul_f32 v[162:163], v[162:163], v[132:133] op_sel:[0,0] op_sel_hi:[1,1]\n"
"v_pk_mul_f32 v[238:239], v[238:239], v[134:135] op_sel:[0,0] op_sel_hi:[1,1]\n"
"v_pk_mul_f32 v[240:241], v[240:241], v[136:137] op_sel:[0,0] op_sel_hi:[1,1]\n"
"v_pk_mul_f32 v[242:243], v[242:243], v[138:139] op_sel:[0,0] op_sel_hi:[1,1]\n"
"v_pk_mul_f32 v[244:245], v[244:245], v[140:141] op_sel:[0,0] op_sel_hi:[1,1]\n"
"v_pk_mul_f32 v[246:247], v[246:247], v[144:145] op_sel:[0,0] op_sel_hi:[1,1]\n"
"v_pk_mul_f32 v[248:249], v[248:249], v[146:147] op_sel:[0,0] op_sel_hi:[1,1]\n"
"v_cvt_pk_bf16_f32 v160, v160, v161\n"
"v_cvt_pk_bf16_f32 v161, v162, v163\n"
"v_cvt_pk_bf16_f32 v162, v238, v239\n"
"v_cvt_pk_bf16_f32 v163, v240, v241\n"
"s_add_u32 s10, s98, 524288\n"
"buffer_store_dwordx4 v[160:163], v3, s[84:87], s10 offen  sc1\n"
"v_cvt_pk_bf16_f32 v242, v242, v243\n"
"v_cvt_pk_bf16_f32 v243, v244, v245\n"
"v_cvt_pk_bf16_f32 v244, v246, v247\n"
"v_cvt_pk_bf16_f32 v245, v248, v249\n"
"s_add_u32 s11, s98, 524288\n"
"buffer_store_dwordx4 v[242:245], v3, s[84:87], s11 offen offset:1024 sc1\n"
"s_waitcnt lgkmcnt(8)\n"
"v_mfma_f32_16x16x32_bf16 v[38:41], v[20:23], v[166:169], 0\n"
"ds_read_b64_tr_b16 v[118:119], v252 offset:14464\n"
"ds_read_b64_tr_b16 v[120:121], v252 offset:18048\n"
"v_mfma_f32_16x16x32_bf16 v[42:45], v[24:27], v[166:169], 0\n"
"ds_read_b64_tr_b16 v[122:123], v252 offset:14496\n"
"ds_read_b64_tr_b16 v[124:125], v252 offset:18080\n"
"s_waitcnt lgkmcnt(8)\n"
"v_mfma_f32_16x16x32_bf16 v[46:49], v[28:31], v[166:169], 0\n"
"ds_read_b64_tr_b16 v[20:21], v17 offset:14336\n"
"ds_read_b64_tr_b16 v[22:23], v17 offset:17920\n"
"v_mfma_f32_16x16x32_bf16 v[38:41], v[32:35], v[170:173], v[38:41]\n"
"ds_read_b64_tr_b16 v[24:25], v252 offset:21632\n"
"ds_read_b64_tr_b16 v[26:27], v252 offset:25216\n"
"s_waitcnt lgkmcnt(8)\n"
"v_mfma_f32_16x16x32_bf16 v[42:45], v[110:113], v[170:173], v[42:45]\n"
"ds_read_b64_tr_b16 v[28:29], v252 offset:21664\n"
"ds_read_b64_tr_b16 v[30:31], v252 offset:25248\n"
"v_mfma_f32_16x16x32_bf16 v[46:49], v[114:117], v[170:173], v[46:49]\n"
"ds_read_b64_tr_b16 v[32:33], v17 offset:21504\n"
"ds_read_b64_tr_b16 v[34:35], v17 offset:25088\n"
"s_waitcnt lgkmcnt(8)\n"
"v_mfma_f32_16x16x32_bf16 v[38:41], v[118:121], v[174:177], v[38:41]\n"
"ds_read_b64_tr_b16 v[110:111], v252 offset:28800\n"
"ds_read_b64_tr_b16 v[112:113], v252 offset:32384\n"
"v_mfma_f32_16x16x32_bf16 v[42:45], v[122:125], v[174:177], v[42:45]\n"
"ds_read_b64_tr_b16 v[114:115], v252 offset:28832\n"
"ds_read_b64_tr_b16 v[116:117], v252 offset:32416\n"
"s_waitcnt vmcnt(2)\n"
"v_cmp_ne_u64 vcc, s[60:61], v[164:165]\n"
"s_nop 4\n"
"s_cbranch_vccz L_31_%=\n"
"s_mov_b32 s62, 40000\n"
"buffer_load_dwordx2 v[12:13], v19, s[84:87], s12 offen  sc1\n"
"L_32_%=:\n"
"s_waitcnt vmcnt(0)\n"
"v_cmp_ne_u64 vcc, s[60:61], v[12:13]\n"
"s_nop 4\n"
"s_cbranch_vccz L_31_%=\n"
"buffer_load_dwordx2 v[12:13], v19, s[84:87], s12 offen  sc1\n"
"s_sub_u32 s62, s62, 1\n"
"s_cbranch_scc0 L_32_%=\n"
"L_31_%=:\n"
"s_waitcnt lgkmcnt(8)\n"
"v_mfma_f32_16x16x32_bf16 v[46:49], v[20:23], v[174:177], v[46:49]\n"
"ds_read_b64_tr_b16 v[118:119], v17 offset:28672\n"
"ds_read_b64_tr_b16 v[120:121], v17 offset:32256\n"
"buffer_load_dwordx4 v[246:249], v3, s[84:87], s72 offen  sc1\n"
"v_mfma_f32_16x16x32_bf16 v[38:41], v[24:27], v[178:181], v[38:41]\n"
"ds_read_b64_tr_b16 v[122:123], v252 offset:35968\n"
"ds_read_b64_tr_b16 v[124:125], v252 offset:39552\n"
"s_waitcnt lgkmcnt(8)\n"
"v_mfma_f32_16x16x32_bf16 v[42:45], v[28:31], v[178:181], v[42:45]\n"
"ds_read_b64_tr_b16 v[20:21], v252 offset:36000\n"
"ds_read_b64_tr_b16 v[22:23], v252 offset:39584\n"
"v_mfma_f32_16x16x32_bf16 v[46:49], v[32:35], v[178:181], v[46:49]\n"
"ds_read_b64_tr_b16 v[24:25], v17 offset:35840\n"
"ds_read_b64_tr_b16 v[26:27], v17 offset:39424\n"
"buffer_load_dwordx4 v[148:151], v3, s[84:87], s72 offen offset:1024 sc1\n"
"s_waitcnt lgkmcnt(8)\n"
"v_mfma_f32_16x16x32_bf16 v[38:41], v[110:113], v[182:185], v[38:41]\n"
"ds_read_b64_tr_b16 v[28:29], v252 offset:43136\n"
"ds_read_b64_tr_b16 v[30:31], v252 offset:46720\n"
"v_mfma_f32_16x16x32_bf16 v[42:45], v[114:117], v[182:185], v[42:45]\n"
"ds_read_b64_tr_b16 v[32:33], v252 offset:43168\n"
"ds_read_b64_tr_b16 v[34:35], v252 offset:46752\n"
"s_waitcnt lgkmcnt(8)\n"
"v_mfma_f32_16x16x32_bf16 v[46:49], v[118:121], v[182:185], v[46:49]\n"
"ds_read_b64_tr_b16 v[110:111], v17 offset:43008\n"
"ds_read_b64_tr_b16 v[112:113], v17 offset:46592\n"
"buffer_load_dwordx4 v[152:155], v3, s[84:87], s72 offen offset:2048 sc1\n"
"v_mfma_f32_16x16x32_bf16 v[38:41], v[122:125], v[186:189], v[38:41]\n"
"ds_read_b64_tr_b16 v[114:115], v252 offset:50304\n"
"ds_read_b64_tr_b16 v[116:117], v252 offset:53888\n"
"s_waitcnt lgkmcnt(8)\n"
"v_mfma_f32_16x16x32_bf16 v[42:45], v[20:23], v[186:189], v[42:45]\n"
"ds_read_b64_tr_b16 v[118:119], v252 offset:50336\n"
"ds_read_b64_tr_b16 v[120:121], v252 offset:53920\n"
"v_mfma_f32_16x16x32_bf16 v[46:49], v[24:27], v[186:189], v[46:49]\n"
"ds_read_b64_tr_b16 v[122:123], v17 offset:50176\n"
"ds_read_b64_tr_b16 v[124:125], v17 offset:53760\n"
"buffer_load_dwordx4 v[156:159], v3, s[84:87], s72 offen offset:3072 sc1\n"
"s_waitcnt lgkmcnt(8)\n"
"v_mfma_f32_16x16x32_bf16 v[38:41], v[28:31], v[190:193], v[38:41]\n"
"ds_read_b64_tr_b16 v[20:21], v16 offset:128\n"
"ds_read_b64_tr_b16 v[22:23], v16 offset:3712\n"
"v_mfma_f32_16x16x32_bf16 v[42:45], v[32:35], v[190:193], v[42:45]\n"
"ds_read_b64_tr_b16 v[24:25], v16 offset:160\n"
"ds_read_b64_tr_b16 v[26:27], v16 offset:3744\n"
"s_waitcnt lgkmcnt(8)\n"
"v_mfma_f32_16x16x32_bf16 v[46:49], v[110:113], v[190:193], v[46:49]\n"
"ds_read_b64_tr_b16 v[28:29], v18\n"
"ds_read_b64_tr_b16 v[30:31], v18 offset:3584\n"
"s_add_u32 s13, s72, 4096\n"
"buffer_load_dwordx4 v[230:233], v3, s[84:87], s13 offen  sc1\n"
"v_mfma_f32_16x16x32_bf16 v[38:41], v[114:117], v[194:197], v[38:41]\n"
"ds_read_b64_tr_b16 v[32:33], v16 offset:7296\n"
"ds_read_b64_tr_b16 v[34:35], v16 offset:10880\n"
"s_waitcnt lgkmcnt(8)\n"
"v_mfma_f32_16x16x32_bf16 v[42:45], v[118:121], v[194:197], v[42:45]\n"
"ds_read_b64_tr_b16 v[110:111], v16 offset:7328\n"
"ds_read_b64_tr_b16 v[112:113], v16 offset:10912\n"
"v_mfma_f32_16x16x32_bf16 v[46:49], v[122:125], v[194:197], v[46:49]\n"
"ds_read_b64_tr_b16 v[114:115], v18 offset:7168\n"
"ds_read_b64_tr_b16 v[116:117], v18 offset:10752\n"
"buffer_load_dwordx4 v[234:237], v3, s[84:87], s13 offen offset:1024 sc1\n"
"s_waitcnt lgkmcnt(8)\n"
"v_mfma_f32_16x16x32_bf16 v[38:41], v[20:23], v[198:201], v[38:41]\n"
"ds_read_b64_tr_b16 v[118:119], v16 offset:14464\n"
"ds_read_b64_tr_b16 v[120:121], v16 offset:18048\n"
"v_mfma_f32_16x16x32_bf16 v[42:45], v[24:27], v[198:201], v[42:45]\n"
"ds_read_b64_tr_b16 v[122:123], v16 offset:14496\n"
"ds_read_b64_tr_b16 v[124:125], v16 offset:18080\n"
"s_waitcnt lgkmcnt(8)\n"
"v_mfma_f32_16x16x32_bf16 v[46:49], v[28:31], v[198:201], v[46:49]\n"
"ds_read_b64_tr_b16 v[20:21], v18 offset:14336\n"
"ds_read_b64_tr_b16 v[22:23], v18 offset:17920\n"
"buffer_load_dwordx4 v[238:241], v3, s[84:87], s13 offen offset:2048 sc1\n"
"v_mfma_f32_16x16x32_bf16 v[38:41], v[32:35], v[202:205], v[38:41]\n"
"ds_read_b64_tr_b16 v[24:25], v16 offset:21632\n"
"ds_read_b64_tr_b16 v[26:27], v16 offset:25216\n"
"s_waitcnt lgkmcnt(8)\n"
"v_mfma_f32_16x16x32_bf16 v[42:45], v[110:113], v[202:205], v[42:45]\n"
"ds_read_b64_tr_b16 v[28:29], v16 offset:21664\n"
"ds_read_b64_tr_b16 v[30:31], v16 offset:25248\n"
"v_mfma_f32_16x16x32_bf16 v[46:49], v[114:117], v[202:205], v[46:49]\n"
"ds_read_b64_tr_b16 v[32:33], v18 offset:21504\n"
"ds_read_b64_tr_b16 v[34:35], v18 offset:25088\n"
"s_add_u32 s13, s72, 16384\n"
"buffer_load_dwordx4 v[50:53], v3, s[84:87], s13 offen  sc1\n"
"s_waitcnt vmcnt(8)\n"
"v_mov_b32 v164, s60\n"
"v_mov_b32 v165, s61\n"
"s_add_u32 s8, s78, 1343584\n"
"s_barrier\n"
"s_mov_b64 exec, 1\n"
"buffer_store_dwordx2 v[164:165], off, s[84:87], s8 sc1\n"
"s_mov_b64 exec, -1\n"
"v_mfma_f32_16x16x16_bf16 v[38:41], a[250:251], v[128:129], v[38:41]\n"
"v_mfma_f32_16x16x16_bf16 v[42:45], a[252:253], v[128:129], v[42:45]\n"
"v_mfma_f32_16x16x16_bf16 v[46:49], a[254:255], v[128:129], v[46:49]\n"
"s_waitcnt lgkmcnt(8)\n"
"s_nop 2\n"
"v_mfma_f32_16x16x32_bf16 v[38:41], v[118:121], v[206:209], v[38:41]\n"
"ds_read_b64_tr_b16 v[110:111], v16 offset:28800\n"
"ds_read_b64_tr_b16 v[112:113], v16 offset:32384\n"
"v_mfma_f32_16x16x32_bf16 v[42:45], v[122:125], v[206:209], v[42:45]\n"
"ds_read_b64_tr_b16 v[114:115], v16 offset:28832\n"
"ds_read_b64_tr_b16 v[116:117], v16 offset:32416\n"
"s_waitcnt lgkmcnt(8)\n"
"v_mfma_f32_16x16x32_bf16 v[46:49], v[20:23], v[206:209], v[46:49]\n"
"ds_read_b64_tr_b16 v[118:119], v18 offset:28672\n"
"ds_read_b64_tr_b16 v[120:121], v18 offset:32256\n"
"buffer_load_dwordx4 v[54:57], v3, s[84:87], s13 offen offset:1024 sc1\n"
"v_mfma_f32_16x16x32_bf16 v[38:41], v[24:27], v[210:213], v[38:41]\n"
"ds_read_b64_tr_b16 v[122:123], v16 offset:35968\n"
"ds_read_b64_tr_b16 v[124:125], v16 offset:39552\n"
"s_waitcnt lgkmcnt(8)\n"
"v_mfma_f32_16x16x32_bf16 v[42:45], v[28:31], v[210:213], v[42:45]\n"
"ds_read_b64_tr_b16 v[20:21], v16 offset:36000\n"
"ds_read_b64_tr_b16 v[22:23], v16 offset:39584\n"
"v_mfma_f32_16x16x32_bf16 v[46:49], v[32:35], v[210:213], v[46:49]\n"
"ds_read_b64_tr_b16 v[24:25], v18 offset:35840\n"
"ds_read_b64_tr_b16 v[26:27], v18 offset:39424\n"
"buffer_load_dwordx4 v[58:61], v3, s[84:87], s13 offen offset:2048 sc1\n"
"s_waitcnt lgkmcnt(8)\n"
"v_mfma_f32_16x16x32_bf16 v[38:41], v[110:113], v[214:217], v[38:41]\n"
"ds_read_b64_tr_b16 v[28:29], v16 offset:43136\n"
"ds_read_b64_tr_b16 v[30:31], v16 offset:46720\n"
"v_mfma_f32_16x16x32_bf16 v[42:45], v[114:117], v[214:217], v[42:45]\n"
"ds_read_b64_tr_b16 v[32:33], v16 offset:43168\n"
"ds_read_b64_tr_b16 v[34:35], v16 offset:46752\n"
"s_waitcnt lgkmcnt(8)\n"
"v_mfma_f32_16x16x32_bf16 v[46:49], v[118:121], v[214:217], v[46:49]\n"
"ds_read_b64_tr_b16 v[110:111], v18 offset:43008\n"
"ds_read_b64_tr_b16 v[112:113], v18 offset:46592\n"
"buffer_load_dwordx4 v[62:65], v3, s[84:87], s13 offen offset:3072 sc1\n"
"v_mfma_f32_16x16x32_bf16 v[38:41], v[122:125], v[218:221], v[38:41]\n"
"ds_read_b64_tr_b16 v[114:115], v16 offset:50304\n"
"ds_read_b64_tr_b16 v[116:117], v16 offset:53888\n"
"s_waitcnt lgkmcnt(8)\n"
"v_mfma_f32_16x16x32_bf16 v[42:45], v[20:23], v[218:221], v[42:45]\n"
"ds_read_b64_tr_b16 v[118:119], v16 offset:50336\n"
"ds_read_b64_tr_b16 v[120:121], v16 offset:53920\n"
"v_mfma_f32_16x16x32_bf16 v[46:49], v[24:27], v[218:221], v[46:49]\n"
"ds_read_b64_tr_b16 v[122:123], v18 offset:50176\n"
"ds_read_b64_tr_b16 v[124:125], v18 offset:53760\n"
"s_add_u32 s13, s72, 20480\n"
"buffer_load_dwordx4 v[66:69], v3, s[84:87], s13 offen  sc1\n"
"s_waitcnt lgkmcnt(8)\n"
"v_mfma_f32_16x16x32_bf16 v[38:41], v[28:31], v[222:225], v[38:41]\n"
"v_mfma_f32_16x16x32_bf16 v[42:45], v[32:35], v[222:225], v[42:45]\n"
"s_waitcnt lgkmcnt(4)\n"
"v_mfma_f32_16x16x32_bf16 v[46:49], v[110:113], v[222:225], v[46:49]\n"
"buffer_load_dwordx4 v[70:73], v3, s[84:87], s13 offen offset:1024 sc1\n"
"v_mfma_f32_16x16x32_bf16 v[38:41], v[114:117], v[226:229], v[38:41]\n"
"s_waitcnt lgkmcnt(0)\n"
"v_mfma_f32_16x16x32_bf16 v[42:45], v[118:121], v[226:229], v[42:45]\n"
"v_mfma_f32_16x16x32_bf16 v[46:49], v[122:125], v[226:229], v[46:49]\n"
"buffer_load_dwordx4 v[74:77], v3, s[84:87], s13 offen offset:2048 sc1\n"
"buffer_load_dwordx4 v[242:245], v3, s[84:87], s71 offen  sc1\n"
"buffer_load_dwordx4 v[16:19], v3, s[84:87], s71 offen offset:1024 sc1\n"
"s_lshr_b32 s63, s82, 6\n"
"s_add_u32 s63, s63, 161280\n"
"v_lshl_add_u32 v143, v1, 6, s63\n"
"ds_read_b128 v[90:93], v143\n"
"ds_read_b128 v[94:97], v143 offset:16\n"
"v_add_u32 v252, s73, v3\n"
"v_add_u32 v128, s74, v3\n"
"v_add_u32 v129, s75, v3\n"
"s_mul_i32 s63, s34, 6144\n"
"s_add_u32 s63, s63, 90112\n"
"v_add_u32 v250, s63, v3\n"
"v_lshlrev_b32 v143, 10, v2\n"
"v_lshl_add_u32 v143, v1, 5, v143\n"
"v_and_b32 v251, 3, v2\n"
"v_lshlrev_b32 v251, 7, v251\n"
"s_lshl_b32 s12, s34, 9\n"
"s_add_u32 s12, s12, 1343584\n"
"s_waitcnt vmcnt(16)\n"
"v_mfma_f32_16x16x32_bf16 v[32:35], a[0:3], v[246:249], 0\n"
"buffer_load_dwordx4 v[98:101], v3, s[84:87], s71 offen offset:2048 sc1\n"
"v_mul_f32 v130, v38, v38\n"
"v_mul_f32 v131, v39, v39\n"
"v_mul_f32 v132, v40, v40\n"
"v_mfma_f32_16x16x32_bf16 v[78:81], a[4:7], v[246:249], 0\n"
"v_mul_f32 v133, v41, v41\n"
"v_mul_f32 v134, v42, v42\n"
"v_mul_f32 v135, v43, v43\n"
"v_mfma_f32_16x16x32_bf16 v[82:85], a[8:11], v[246:249], 0\n"
"v_mul_f32 v136, v44, v44\n"
"v_mul_f32 v137, v45, v45\n"
"v_mul_f32 v138, v46, v46\n"
"v_mfma_f32_16x16x32_bf16 v[86:89], a[12:15], v[246:249], 0\n"
"v_mul_f32 v139, v47, v47\n"
"v_mul_f32 v140, v48, v48\n"
"v_mul_f32 v141, v49, v49\n"
"s_waitcnt vmcnt(16)\n"
"v_mfma_f32_16x16x32_bf16 v[32:35], a[16:19], v[148:151], v[32:35]\n"
"buffer_load_dwordx4 v[246:249], v3, s[84:87], s71 offen offset:3072 sc1\n"
"v_fma_f32 v130, v130, v126, v127\n"
"v_fma_f32 v131, v131, v126, v127\n"
"v_fma_f32 v132, v132, v126, v127\n"
"v_mfma_f32_16x16x32_bf16 v[78:81], a[20:23], v[148:151], v[78:81]\n"
"v_fma_f32 v133, v133, v126, v127\n"
"v_fma_f32 v134, v134, v126, v127\n"
"v_fma_f32 v135, v135, v126, v127\n"
"v_mfma_f32_16x16x32_bf16 v[82:85], a[24:27], v[148:151], v[82:85]\n"
"v_fma_f32 v136, v136, v126, v127\n"
"v_fma_f32 v137, v137, v126, v127\n"
"v_fma_f32 v138, v138, v126, v127\n"
"v_mfma_f32_16x16x32_bf16 v[86:89], a[28:31], v[148:151], v[86:89]\n"
"v_fma_f32 v139, v139, v126, v127\n"
"v_fma_f32 v140, v140, v126, v127\n"
"v_fma_f32 v141, v141, v126, v127\n"
"s_waitcnt vmcnt(16)\n"
"v_mfma_f32_16x16x32_bf16 v[32:35], a[32:35], v[152:155], v[32:35]\n"
"s_add_u32 s13, s71, 4096\n"
"buffer_load_dwordx4 v[148:151], v3, s[84:87], s13 offen  sc1\n"
"v_mul_f32 v130, v130, v38\n"
"v_mul_f32 v131, v131, v39\n"
"v_mul_f32 v132, v132, v40\n"
"v_mfma_f32_16x16x32_bf16 v[78:81], a[36:39], v[152:155], v[78:81]\n"
"v_mul_f32 v133, v133, v41\n"
"v_mul_f32 v134, v134, v42\n"
"v_mul_f32 v135, v135, v43\n"
"v_mfma_f32_16x16x32_bf16 v[82:85], a[40:43], v[152:155], v[82:85]\n"
"v_mul_f32 v136, v136, v44\n"
"v_mul_f32 v137, v137, v45\n"
"v_mul_f32 v138, v138, v46\n"
"v_mfma_f32_16x16x32_bf16 v[86:89], a[44:47], v[152:155], v[86:89]\n"
"v_mul_f32 v139, v139, v47\n"
"v_mul_f32 v140, v140, v48\n"
"v_mul_f32 v141, v141, v49\n"
"s_waitcnt vmcnt(16)\n"
"v_mfma_f32_16x16x32_bf16 v[32:35], a[48:51], v[156:159], v[32:35]\n"
"buffer_load_dwordx4 v[152:155], v3, s[84:87], s13 offen offset:1024 sc1\n"
"v_exp_f32 v130, v130\n"
"v_exp_f32 v131, v131\n"
"v_mfma_f32_16x16x32_bf16 v[78:81], a[52:55], v[156:159], v[78:81]\n"
"v_exp_f32 v132, v132\n"
"v_exp_f32 v133, v133\n"
"v_mfma_f32_16x16x32_bf16 v[82:85], a[56:59], v[156:159], v[82:85]\n"
"v_exp_f32 v134, v134\n"
"v_exp_f32 v135, v135\n"
"v_mfma_f32_16x16x32_bf16 v[86:89], a[60:63], v[156:159], v[86:89]\n"
"v_exp_f32 v136, v136\n"
"v_exp_f32 v137, v137\n"
"s_waitcnt vmcnt(16)\n"
"v_mfma_f32_16x16x32_bf16 v[32:35], a[64:67], v[230:233], v[32:35]\n"
"buffer_load_dwordx4 v[102:105], v3, s[84:87], s13 offen offset:2048 sc1\n"
"v_exp_f32 v138, v138\n"
"v_exp_f32 v139, v139\n"
"v_mfma_f32_16x16x32_bf16 v[78:81], a[68:71], v[230:233], v[78:81]\n"
"v_exp_f32 v140, v140\n"
"v_exp_f32 v141, v141\n"
"v_mfma_f32_16x16x32_bf16 v[82:85], a[72:75], v[230:233], v[82:85]\n"
"v_fma_f32 v130, v130, v253, v253\n"
"v_fma_f32 v131, v131, v253, v253\n"
"v_fma_f32 v132, v132, v253, v253\n"
"v_mfma_f32_16x16x32_bf16 v[86:89], a[76:79], v[230:233], v[86:89]\n"
"v_fma_f32 v133, v133, v253, v253\n"
"v_fma_f32 v134, v134, v253, v253\n"
"v_fma_f32 v135, v135, v253, v253\n"
"buffer_load_dwordx2 v[106:107], v251, s[84:87], s12 offen  sc1\n"
"s_waitcnt vmcnt(17)\n"
"v_mfma_f32_16x16x32_bf16 v[32:35], a[80:83], v[234:237], v[32:35]\n"
"s_add_u32 s13, s71, 16384\n"
"buffer_load_dwordx4 v[108:111], v3, s[84:87], s13 offen  sc1\n"
"v_fma_f32 v136, v136, v253, v253\n"
"v_fma_f32 v137, v137, v253, v253\n"
"v_fma_f32 v138, v138, v253, v253\n"
"v_mfma_f32_16x16x32_bf16 v[78:81], a[84:87], v[234:237], v[78:81]\n"
"v_fma_f32 v139, v139, v253, v253\n"
"v_fma_f32 v140, v140, v253, v253\n"
"v_fma_f32 v141, v141, v253, v253\n"
"v_mfma_f32_16x16x32_bf16 v[82:85], a[88:91], v[234:237], v[82:85]\n"
"v_rcp_f32 v130, v130\n"
"v_rcp_f32 v131, v131\n"
"v_mfma_f32_16x16x32_bf16 v[86:89], a[92:95], v[234:237], v[86:89]\n"
"v_rcp_f32 v132, v132\n"
"v_rcp_f32 v133, v133\n"
"s_waitcnt vmcnt(17)\n"
"v_mfma_f32_16x16x32_bf16 v[32:35], a[96:99], v[238:241], v[32:35]\n"
"buffer_load_dwordx4 v[112:115], v3, s[84:87], s13 offen offset:1024 sc1\n"
"v_rcp_f32 v134, v134\n"
"v_rcp_f32 v135, v135\n"
"v_mfma_f32_16x16x32_bf16 v[78:81], a[100:103], v[238:241], v[78:81]\n"
"v_rcp_f32 v136, v136\n"
"v_rcp_f32 v137, v137\n"
"v_mfma_f32_16x16x32_bf16 v[82:85], a[104:107], v[238:241], v[82:85]\n"
"v_rcp_f32 v138, v138\n"
"v_rcp_f32 v139, v139\n"
"v_mfma_f32_16x16x32_bf16 v[86:89], a[108:111], v[238:241], v[86:89]\n"
"v_rcp_f32 v140, v140\n"
"v_rcp_f32 v141, v141\n"
"s_waitcnt vmcnt(17)\n"
"v_mfma_f32_16x16x32_bf16 v[32:35], a[112:115], v[50:53], v[32:35]\n"
"buffer_load_dwordx4 v[116:119], v3, s[84:87], s13 offen offset:2048 sc1\n"
"v_mul_f32 v38, v38, v130\n"
"v_mul_f32 v39, v39, v131\n"
"v_mul_f32 v40, v40, v132\n"
"v_mfma_f32_16x16x32_bf16 v[78:81], a[116:119], v[50:53], v[78:81]\n"
"v_mul_f32 v41, v41, v133\n"
"v_mul_f32 v42, v42, v134\n"
"v_mul_f32 v43, v43, v135\n"
"v_mfma_f32_16x16x32_bf16 v[82:85], a[120:123], v[50:53], v[82:85]\n"
"v_mul_f32 v44, v44, v136\n"
"v_mul_f32 v45, v45, v137\n"
"v_mul_f32 v46, v46, v138\n"
"v_mfma_f32_16x16x32_bf16 v[86:89], a[124:127], v[50:53], v[86:89]\n"
"v_mul_f32 v47, v47, v139\n"
"v_mul_f32 v48, v48, v140\n"
"v_mul_f32 v49, v49, v141\n"
"s_waitcnt vmcnt(16)\n"
"v_mfma_f32_16x16x32_bf16 v[32:35], a[128:131], v[54:57], v[32:35]\n"
"buffer_load_dwordx4 v[50:53], v3, s[84:87], s13 offen offset:3072 sc1\n"
"v_cvt_pk_bf16_f32 v38, v38, v39\n"
"v_cvt_pk_bf16_f32 v39, v40, v41\n"
"v_cvt_pk_bf16_f32 v40, v42, v43\n"
"v_mfma_f32_16x16x32_bf16 v[78:81], a[132:135], v[54:57], v[78:81]\n"
"v_cvt_pk_bf16_f32 v41, v44, v45\n"
"s_add_u32 s9, s98, 524288\n"
"buffer_store_dwordx4 v[38:41], v3, s[84:87], s9 offen offset:2048 sc1\n"
"v_cvt_pk_bf16_f32 v46, v46, v47\n"
"v_mfma_f32_16x16x32_bf16 v[82:85], a[136:139], v[54:57], v[82:85]\n"
"v_cvt_pk_bf16_f32 v47, v48, v49\n"
"s_add_u32 s10, s99, 524288\n"
"buffer_store_dwordx2 v[46:47], v3, s[84:87], s10 offen offset:3072 sc1\n"
"v_mfma_f32_16x16x32_bf16 v[86:89], a[140:143], v[54:57], v[86:89]\n"
"s_waitcnt vmcnt(18)\n"
"v_mfma_f32_16x16x32_bf16 v[32:35], a[144:147], v[58:61], v[32:35]\n"
"s_add_u32 s13, s71, 20480\n"
"buffer_load_dwordx4 v[54:57], v3, s[84:87], s13 offen  sc1\n"
"v_mfma_f32_16x16x32_bf16 v[78:81], a[148:151], v[58:61], v[78:81]\n"
"v_mfma_f32_16x16x32_bf16 v[82:85], a[152:155], v[58:61], v[82:85]\n"
"v_mfma_f32_16x16x32_bf16 v[86:89], a[156:159], v[58:61], v[86:89]\n"
"s_waitcnt vmcnt(18)\n"
"v_mfma_f32_16x16x32_bf16 v[32:35], a[160:163], v[62:65], v[32:35]\n"
"buffer_load_dwordx4 v[58:61], v3, s[84:87], s13 offen offset:1024 sc1\n"
"v_mfma_f32_16x16x32_bf16 v[78:81], a[164:167], v[62:65], v[78:81]\n"
"v_mfma_f32_16x16x32_bf16 v[82:85], a[168:171], v[62:65], v[82:85]\n"
"v_mfma_f32_16x16x32_bf16 v[86:89], a[172:175], v[62:65], v[86:89]\n"
"s_waitcnt vmcnt(18)\n"
"v_mfma_f32_16x16x32_bf16 v[32:35], a[176:179], v[66:69], v[32:35]\n"
"buffer_load_dwordx4 v[62:65], v3, s[84:87], s13 offen offset:2048 sc1\n"
"v_mfma_f32_16x16x32_bf16 v[78:81], a[180:183], v[66:69], v[78:81]\n"
"v_mfma_f32_16x16x32_bf16 v[82:85], a[184:187], v[66:69], v[82:85]\n"
"v_mfma_f32_16x16x32_bf16 v[86:89], a[188:191], v[66:69], v[86:89]\n"
"s_waitcnt vmcnt(9)\n"
"v_cmp_ne_u64 vcc, s[60:61], v[106:107]\n"
"s_nop 4\n"
"s_cbranch_vccz L_33_%=\n"
"s_mov_b32 s62, 40000\n"
"buffer_load_dwordx2 v[12:13], v251, s[84:87], s12 offen  sc1\n"
"L_34_%=:\n"
"s_waitcnt vmcnt(0)\n"
"v_cmp_ne_u64 vcc, s[60:61], v[12:13]\n"
"s_nop 4\n"
"s_cbranch_vccz L_33_%=\n"
"buffer_load_dwordx2 v[12:13], v251, s[84:87], s12 offen  sc1\n"
"s_sub_u32 s62, s62, 1\n"
"s_cbranch_scc0 L_34_%=\n"
"L_33_%=:\n"
"s_add_u32 s13, s72, 524288\n"
"buffer_load_dwordx4 v[66:69], v3, s[84:87], s13 offen  sc1\n"
"v_mfma_f32_16x16x32_bf16 v[32:35], a[192:195], v[70:73], v[32:35]\n"
"v_mfma_f32_16x16x32_bf16 v[78:81], a[196:199], v[70:73], v[78:81]\n"
"v_mfma_f32_16x16x32_bf16 v[82:85], a[200:203], v[70:73], v[82:85]\n"
"v_mfma_f32_16x16x32_bf16 v[86:89], a[204:207], v[70:73], v[86:89]\n"
"buffer_load_dwordx4 v[70:73], v3, s[84:87], s13 offen offset:1024 sc1\n"
"v_mfma_f32_16x16x32_bf16 v[32:35], a[208:211], v[74:77], v[32:35]\n"
"v_mfma_f32_16x16x32_bf16 v[78:81], a[212:215], v[74:77], v[78:81]\n"
"v_mfma_f32_16x16x32_bf16 v[82:85], a[216:219], v[74:77], v[82:85]\n"
"v_mfma_f32_16x16x32_bf16 v[86:89], a[220:223], v[74:77], v[86:89]\n"
"s_add_u32 s13, s72, 528384\n"
"buffer_load_dwordx4 v[74:77], v3, s[84:87], s13 offen  sc1\n"
"v_mfma_f32_16x16x32_bf16 v[24:27], a[8:11], v[242:245], 0\n"
"v_mfma_f32_16x16x32_bf16 v[28:31], a[12:15], v[242:245], 0\n"
"s_nop 0\n"
"ds_write_b128 v128, v[32:35]\n"
"v_mfma_f32_16x16x32_bf16 v[24:27], a[24:27], v[16:19], v[24:27]\n"
"v_mfma_f32_16x16x32_bf16 v[28:31], a[28:31], v[16:19], v[28:31]\n"
"v_lshlrev_b32 v144, 16, v4\n"
"buffer_load_dwordx4 v[120:123], v3, s[84:87], s13 offen offset:1024 sc1\n"
"v_mfma_f32_16x16x32_bf16 v[24:27], a[40:43], v[98:101], v[24:27]\n"
"v_and_b32 v145, 0xffff0000, v4\n"
"v_mfma_f32_16x16x32_bf16 v[28:31], a[44:47], v[98:101], v[28:31]\n"
"v_lshlrev_b32 v146, 16, v5\n"
"ds_write_b128 v128, v[78:81] offset:1024\n"
"v_mfma_f32_16x16x32_bf16 v[24:27], a[56:59], v[246:249], v[24:27]\n"
"v_and_b32 v147, 0xffff0000, v5\n"
"v_mfma_f32_16x16x32_bf16 v[28:31], a[60:63], v[246:249], v[28:31]\n"
"v_lshlrev_b32 v20, 16, v6\n"
"s_add_u32 s13, s72, 540672\n"
"buffer_load_dwordx4 v[124:127], v3, s[84:87], s13 offen  sc1\n"
"v_mfma_f32_16x16x32_bf16 v[24:27], a[72:75], v[148:151], v[24:27]\n"
"v_and_b32 v21, 0xffff0000, v6\n"
"v_mfma_f32_16x16x32_bf16 v[28:31], a[76:79], v[148:151], v[28:31]\n"
"L_pad14_%=:\n"
"s_cbranch_execz L_ret_%=\n"
"v_lshlrev_b32 v22, 16, v7\n"
"ds_write_b128 v129, v[82:85]\n"
"v_mfma_f32_16x16x32_bf16 v[24:27], a[88:91], v[152:155], v[24:27]\n"
"v_and_b32 v23, 0xffff0000, v7\n"
"v_mfma_f32_16x16x32_bf16 v[28:31], a[92:95], v[152:155], v[28:31]\n"
"s_waitcnt lgkmcnt(4)\n"
"v_add_f32 v144, v144, v90\n"
"buffer_load_dwordx4 v[130:133], v3, s[84:87], s13 offen offset:1024 sc1\n"
"v_mfma_f32_16x16x32_bf16 v[24:27], a[104:107], v[102:105], v[24:27]\n"
"v_add_f32 v145, v145, v91\n"
"v_mfma_f32_16x16x32_bf16 v[28:31], a[108:111], v[102:105], v[28:31]\n"
"v_add_f32 v146, v146, v92\n"
"ds_write_b128 v129, v[86:89] offset:1024\n"
"s_waitcnt vmcnt(14)\n"
"v_mfma_f32_16x16x32_bf16 v[24:27], a[120:123], v[108:111], v[24:27]\n"
"v_add_f32 v147, v147, v93\n"
"v_mfma_f32_16x16x32_bf16 v[28:31], a[124:127], v[108:111], v[28:31]\n"
"s_waitcnt lgkmcnt(4)\n"
"v_add_f32 v20, v20, v94\n"
"s_waitcnt vmcnt(9)\n"
"v_mov_b32 v106, s60\n"
"v_mov_b32 v107, s61\n"
"s_add_u32 s11, s78, 1343592\n"
"s_barrier\n"
"s_mov_b64 exec, 1\n"
"buffer_store_dwordx2 v[106:107], off, s[84:87], s11 sc1\n"
"s_mov_b64 exec, -1\n"
"s_add_u32 s13, s72, 544768\n"
"buffer_load_dwordx4 v[134:137], v3, s[84:87], s13 offen  sc1\n"
"v_mfma_f32_16x16x32_bf16 v[24:27], a[136:139], v[112:115], v[24:27]\n"
"v_add_f32 v21, v21, v95\n"
"v_mfma_f32_16x16x32_bf16 v[28:31], a[140:143], v[112:115], v[28:31]\n"
"v_add_f32 v22, v22, v96\n"
"v_mfma_f32_16x16x32_bf16 v[24:27], a[152:155], v[116:119], v[24:27]\n"
"v_add_f32 v23, v23, v97\n"
"v_mfma_f32_16x16x32_bf16 v[28:31], a[156:159], v[116:119], v[28:31]\n"
"buffer_load_dwordx4 v[138:141], v3, s[84:87], s13 offen offset:1024 sc1\n"
"v_mfma_f32_16x16x32_bf16 v[24:27], a[168:171], v[50:53], v[24:27]\n"
"v_mfma_f32_16x16x32_bf16 v[28:31], a[172:175], v[50:53], v[28:31]\n"
"s_waitcnt vmcnt(11)\n"
"v_mfma_f32_16x16x32_bf16 v[24:27], a[184:187], v[54:57], v[24:27]\n"
"v_mfma_f32_16x16x32_bf16 v[28:31], a[188:191], v[54:57], v[28:31]\n"
"s_add_u32 s13, s71, 524288\n"
"buffer_load_dwordx4 v[36:39], v3, s[84:87], s13 offen  sc1\n"
"s_waitcnt vmcnt(11)\n"
"v_mfma_f32_16x16x32_bf16 v[24:27], a[200:203], v[58:61], v[24:27]\n"
"v_mfma_f32_16x16x32_bf16 v[28:31], a[204:207], v[58:61], v[28:31]\n"
"s_waitcnt vmcnt(10)\n"
"v_mfma_f32_16x16x32_bf16 v[24:27], a[216:219], v[62:65], v[24:27]\n"
"v_mfma_f32_16x16x32_bf16 v[28:31], a[220:223], v[62:65], v[28:31]\n"
"buffer_load_dwordx4 v[40:43], v3, s[84:87], s13 offen offset:1024 sc1\n"
"v_mfma_f32_16x16x32_bf16 v[144:147], a[0:3], v[242:245], v[144:147]\n"
"v_mfma_f32_16x16x32_bf16 v[20:23], a[4:7], v[242:245], v[20:23]\n"
"s_nop 3\n"
"ds_write_b128 v252, v[24:27]\n"
"v_mfma_f32_16x16x32_bf16 v[144:147], a[16:19], v[16:19], v[144:147]\n"
"v_mfma_f32_16x16x32_bf16 v[20:23], a[20:23], v[16:19], v[20:23]\n"
"s_add_u32 s13, s71, 528384\n"
"buffer_load_dwordx4 v[16:19], v3, s[84:87], s13 offen  sc1\n"
"v_mfma_f32_16x16x32_bf16 v[144:147], a[32:35], v[98:101], v[144:147]\n"
"v_mfma_f32_16x16x32_bf16 v[20:23], a[36:39], v[98:101], v[20:23]\n"
"ds_write_b128 v252, v[28:31] offset:1024\n"
"v_mfma_f32_16x16x32_bf16 v[144:147], a[48:51], v[246:249], v[144:147]\n"
"v_mfma_f32_16x16x32_bf16 v[20:23], a[52:55], v[246:249], v[20:23]\n"
"buffer_load_dwordx4 v[98:101], v3, s[84:87], s13 offen offset:1024 sc1\n"
"v_mfma_f32_16x16x32_bf16 v[144:147], a[64:67], v[148:151], v[144:147]\n"
"v_mfma_f32_16x16x32_bf16 v[20:23], a[68:71], v[148:151], v[20:23]\n"
"v_mfma_f32_16x16x32_bf16 v[144:147], a[80:83], v[152:155], v[144:147]\n"
"v_mfma_f32_16x16x32_bf16 v[20:23], a[84:87], v[152:155], v[20:23]\n"
"s_add_u32 s13, s71, 540672\n"
"buffer_load_dwordx4 v[44:47], v3, s[84:87], s13 offen  sc1\n"
"s_waitcnt lgkmcnt(0)\n"
"s_barrier\n"
"ds_read_b128 v[24:27], v250\n"
"ds_read_b128 v[28:31], v250 offset:1024\n"
"v_mfma_f32_16x16x32_bf16 v[144:147], a[96:99], v[102:105], v[144:147]\n"
"ds_read_b128 v[32:35], v250 offset:2048\n"
"ds_read_b128 v[78:81], v250 offset:3072\n"
"v_mfma_f32_16x16x32_bf16 v[20:23], a[100:103], v[102:105], v[20:23]\n"
"ds_read_b128 v[82:85], v250 offset:4096\n"
"ds_read_b128 v[86:89], v250 offset:5120\n"
"v_mfma_f32_16x16x32_bf16 v[144:147], a[112:115], v[108:111], v[144:147]\n"
"v_mfma_f32_16x16x32_bf16 v[20:23], a[116:119], v[108:111], v[20:23]\n"
"buffer_load_dwordx4 v[102:105], v3, s[84:87], s13 offen offset:1024 sc1\n"
"v_mfma_f32_16x16x32_bf16 v[144:147], a[128:131], v[112:115], v[144:147]\n"
"v_mfma_f32_16x16x32_bf16 v[20:23], a[132:135], v[112:115], v[20:23]\n"
"v_mfma_f32_16x16x32_bf16 v[144:147], a[144:147], v[116:119], v[144:147]\n"
"v_mfma_f32_16x16x32_bf16 v[20:23], a[148:151], v[116:119], v[20:23]\n"
"s_add_u32 s13, s71, 544768\n"
"buffer_load_dwordx4 v[106:109], v3, s[84:87], s13 offen  sc1\n"
"v_mfma_f32_16x16x32_bf16 v[144:147], a[160:163], v[50:53], v[144:147]\n"
"v_mfma_f32_16x16x32_bf16 v[20:23], a[164:167], v[50:53], v[20:23]\n"
"v_mfma_f32_16x16x32_bf16 v[144:147], a[176:179], v[54:57], v[144:147]\n"
"v_mfma_f32_16x16x32_bf16 v[20:23], a[180:183], v[54:57], v[20:23]\n"
"buffer_load_dwordx4 v[48:51], v3, s[84:87], s13 offen offset:1024 sc1\n"
"v_mfma_f32_16x16x32_bf16 v[144:147], a[192:195], v[58:61], v[144:147]\n"
"v_mfma_f32_16x16x32_bf16 v[20:23], a[196:199], v[58:61], v[20:23]\n"
"v_mfma_f32_16x16x32_bf16 v[144:147], a[208:211], v[62:65], v[144:147]\n"
"v_mfma_f32_16x16x32_bf16 v[20:23], a[212:215], v[62:65], v[20:23]\n"
"s_waitcnt lgkmcnt(5)\n"
"s_nop 5\n"
"v_pk_add_f32 v[24:25], v[24:25], v[144:145] op_sel:[0,0] op_sel_hi:[1,1]\n"
"s_waitcnt lgkmcnt(1)\n"
"v_pk_add_f32 v[32:33], v[32:33], v[82:83] op_sel:[0,0] op_sel_hi:[1,1]\n"
"v_pk_add_f32 v[26:27], v[26:27], v[146:147] op_sel:[0,0] op_sel_hi:[1,1]\n"
"v_pk_add_f32 v[34:35], v[34:35], v[84:85] op_sel:[0,0] op_sel_hi:[1,1]\n"
"v_pk_add_f32 v[28:29], v[28:29], v[20:21] op_sel:[0,0] op_sel_hi:[1,1]\n"
"s_waitcnt lgkmcnt(0)\n"
"v_pk_add_f32 v[78:79], v[78:79], v[86:87] op_sel:[0,0] op_sel_hi:[1,1]\n"
"v_pk_add_f32 v[30:31], v[30:31], v[22:23] op_sel:[0,0] op_sel_hi:[1,1]\n"
"v_pk_add_f32 v[80:81], v[80:81], v[88:89] op_sel:[0,0] op_sel_hi:[1,1]\n"
"v_pk_add_f32 v[24:25], v[24:25], v[32:33] op_sel:[0,0] op_sel_hi:[1,1]\n"
"v_pk_add_f32 v[26:27], v[26:27], v[34:35] op_sel:[0,0] op_sel_hi:[1,1]\n"
"v_pk_add_f32 v[28:29], v[28:29], v[78:79] op_sel:[0,0] op_sel_hi:[1,1]\n"
"v_pk_add_f32 v[30:31], v[30:31], v[80:81] op_sel:[0,0] op_sel_hi:[1,1]\n"
"v_cvt_pk_bf16_f32 v4, v24, v25\n"
"v_cvt_pk_bf16_f32 v5, v26, v27\n"
"v_cvt_pk_bf16_f32 v6, v28, v29\n"
"v_cvt_pk_bf16_f32 v7, v30, v31\n"
"buffer_store_dwordx4 v[4:7], v143, s[48:51], s76 offen\n"
"v_and_b32 v142, 3, v2\n"
"v_lshlrev_b32 v142, 7, v142\n"
"s_lshl_b32 s12, s34, 9\n"
"s_add_u32 s12, s12, 1343592\n"
"s_waitcnt vmcnt(17)\n"
"v_mfma_f32_16x16x32_bf16 v[32:35], a[0:3], v[66:69], 0\n"
"v_mfma_f32_16x16x32_bf16 v[78:81], a[4:7], v[66:69], 0\n"
"v_lshlrev_b32 v144, 16, v8\n"
"v_mfma_f32_16x16x32_bf16 v[82:85], a[8:11], v[66:69], 0\n"
"v_and_b32 v145, 0xffff0000, v8\n"
"v_mfma_f32_16x16x32_bf16 v[86:89], a[12:15], v[66:69], 0\n"
"v_lshlrev_b32 v146, 16, v9\n"
"s_waitcnt vmcnt(16)\n"
"v_mfma_f32_16x16x32_bf16 v[32:35], a[16:19], v[70:73], v[32:35]\n"
"v_and_b32 v147, 0xffff0000, v9\n"
"v_mfma_f32_16x16x32_bf16 v[78:81], a[20:23], v[70:73], v[78:81]\n"
"v_lshlrev_b32 v20, 16, v10\n"
"v_mfma_f32_16x16x32_bf16 v[82:85], a[24:27], v[70:73], v[82:85]\n"
"v_and_b32 v21, 0xffff0000, v10\n"
"v_mfma_f32_16x16x32_bf16 v[86:89], a[28:31], v[70:73], v[86:89]\n"
"v_lshlrev_b32 v22, 16, v11\n"
"s_waitcnt vmcnt(15)\n"
"v_mfma_f32_16x16x32_bf16 v[32:35], a[64:67], v[74:77], v[32:35]\n"
"v_and_b32 v23, 0xffff0000, v11\n"
"v_mfma_f32_16x16x32_bf16 v[78:81], a[68:71], v[74:77], v[78:81]\n"
"v_add_f32 v144, v144, v90\n"
"v_mfma_f32_16x16x32_bf16 v[82:85], a[72:75], v[74:77], v[82:85]\n"
"v_add_f32 v145, v145, v91\n"
"v_mfma_f32_16x16x32_bf16 v[86:89], a[76:79], v[74:77], v[86:89]\n"
"v_add_f32 v146, v146, v92\n"
"s_waitcnt vmcnt(14)\n"
"v_mfma_f32_16x16x32_bf16 v[32:35], a[80:83], v[120:123], v[32:35]\n"
"v_add_f32 v147, v147, v93\n"
"v_mfma_f32_16x16x32_bf16 v[78:81], a[84:87], v[120:123], v[78:81]\n"
"v_add_f32 v20, v20, v94\n"
"buffer_load_dwordx2 v[110:111], v142, s[84:87], s12 offen  sc1\n"
"v_mfma_f32_16x16x32_bf16 v[82:85], a[88:91], v[120:123], v[82:85]\n"
"v_add_f32 v21, v21, v95\n"
"v_mfma_f32_16x16x32_bf16 v[86:89], a[92:95], v[120:123], v[86:89]\n"
"v_add_f32 v22, v22, v96\n"
"s_waitcnt vmcnt(14)\n"
"v_mfma_f32_16x16x32_bf16 v[32:35], a[112:115], v[124:127], v[32:35]\n"
"v_add_f32 v23, v23, v97\n"
"v_mfma_f32_16x16x32_bf16 v[78:81], a[116:119], v[124:127], v[78:81]\n"
"v_mfma_f32_16x16x32_bf16 v[82:85], a[120:123], v[124:127], v[82:85]\n"
"v_mfma_f32_16x16x32_bf16 v[86:89], a[124:127], v[124:127], v[86:89]\n"
"s_waitcnt vmcnt(13)\n"
"v_mfma_f32_16x16x32_bf16 v[32:35], a[128:131], v[130:133], v[32:35]\n"
"v_mfma_f32_16x16x32_bf16 v[78:81], a[132:135], v[130:133], v[78:81]\n"
"v_mfma_f32_16x16x32_bf16 v[82:85], a[136:139], v[130:133], v[82:85]\n"
"v_mfma_f32_16x16x32_bf16 v[86:89], a[140:143], v[130:133], v[86:89]\n"
"s_waitcnt vmcnt(11)\n"
"v_mfma_f32_16x16x32_bf16 v[32:35], a[176:179], v[134:137], v[32:35]\n"
"v_mfma_f32_16x16x32_bf16 v[78:81], a[180:183], v[134:137], v[78:81]\n"
"v_mfma_f32_16x16x32_bf16 v[82:85], a[184:187], v[134:137], v[82:85]\n"
"v_mfma_f32_16x16x32_bf16 v[86:89], a[188:191], v[134:137], v[86:89]\n"
"s_waitcnt vmcnt(10)\n"
"v_mfma_f32_16x16x32_bf16 v[32:35], a[192:195], v[138:141], v[32:35]\n"
"v_mfma_f32_16x16x32_bf16 v[78:81], a[196:199], v[138:141], v[78:81]\n"
"v_mfma_f32_16x16x32_bf16 v[82:85], a[200:203], v[138:141], v[82:85]\n"
"v_mfma_f32_16x16x32_bf16 v[86:89], a[204:207], v[138:141], v[86:89]\n"
"s_waitcnt vmcnt(9)\n"
"v_mfma_f32_16x16x32_bf16 v[144:147], a[0:3], v[36:39], v[144:147]\n"
"v_mfma_f32_16x16x32_bf16 v[20:23], a[4:7], v[36:39], v[20:23]\n"
"v_mfma_f32_16x16x32_bf16 v[24:27], a[8:11], v[36:39], 0\n"
"v_mfma_f32_16x16x32_bf16 v[28:31], a[12:15], v[36:39], 0\n"
"s_waitcnt vmcnt(0)\n"
"v_cmp_ne_u64 vcc, s[60:61], v[110:111]\n"
"s_nop 4\n"
"s_cbranch_vccz L_35_%=\n"
"s_mov_b32 s62, 40000\n"
"buffer_load_dwordx2 v[12:13], v142, s[84:87], s12 offen  sc1\n"
"L_36_%=:\n"
"s_waitcnt vmcnt(0)\n"
"v_cmp_ne_u64 vcc, s[60:61], v[12:13]\n"
"s_nop 4\n"
"s_cbranch_vccz L_35_%=\n"
"buffer_load_dwordx2 v[12:13], v142, s[84:87], s12 offen  sc1\n"
"s_sub_u32 s62, s62, 1\n"
"s_cbranch_scc0 L_36_%=\n"
"L_35_%=:\n"
"s_add_u32 s13, s72, 524288\n"
"buffer_load_dwordx4 v[36:39], v3, s[84:87], s13 offen offset:2048 sc1\n"
"v_mfma_f32_16x16x32_bf16 v[144:147], a[16:19], v[40:43], v[144:147]\n"
"v_mfma_f32_16x16x32_bf16 v[20:23], a[20:23], v[40:43], v[20:23]\n"
"v_mfma_f32_16x16x32_bf16 v[24:27], a[24:27], v[40:43], v[24:27]\n"
"buffer_load_dwordx4 v[130:133], v3, s[84:87], s13 offen offset:3072 sc1\n"
"v_mfma_f32_16x16x32_bf16 v[28:31], a[28:31], v[40:43], v[28:31]\n"
"v_mfma_f32_16x16x32_bf16 v[144:147], a[64:67], v[16:19], v[144:147]\n"
"v_mfma_f32_16x16x32_bf16 v[20:23], a[68:71], v[16:19], v[20:23]\n"
"s_add_u32 s13, s72, 528384\n"
"buffer_load_dwordx4 v[40:43], v3, s[84:87], s13 offen offset:2048 sc1\n"
"v_mfma_f32_16x16x32_bf16 v[24:27], a[72:75], v[16:19], v[24:27]\n"
"v_mfma_f32_16x16x32_bf16 v[28:31], a[76:79], v[16:19], v[28:31]\n"
"v_mfma_f32_16x16x32_bf16 v[144:147], a[80:83], v[98:101], v[144:147]\n"
"s_add_u32 s13, s72, 540672\n"
"buffer_load_dwordx4 v[16:19], v3, s[84:87], s13 offen offset:2048 sc1\n"
"v_mfma_f32_16x16x32_bf16 v[20:23], a[84:87], v[98:101], v[20:23]\n"
"v_mfma_f32_16x16x32_bf16 v[24:27], a[88:91], v[98:101], v[24:27]\n"
"v_mfma_f32_16x16x32_bf16 v[28:31], a[92:95], v[98:101], v[28:31]\n"
"buffer_load_dwordx4 v[98:101], v3, s[84:87], s13 offen offset:3072 sc1\n"
"v_mfma_f32_16x16x32_bf16 v[144:147], a[112:115], v[44:47], v[144:147]\n"
"v_mfma_f32_16x16x32_bf16 v[20:23], a[116:119], v[44:47], v[20:23]\n"
"v_mfma_f32_16x16x32_bf16 v[24:27], a[120:123], v[44:47], v[24:27]\n"
"s_add_u32 s13, s72, 544768\n"
"buffer_load_dwordx4 v[134:137], v3, s[84:87], s13 offen offset:2048 sc1\n"
"v_mfma_f32_16x16x32_bf16 v[28:31], a[124:127], v[44:47], v[28:31]\n"
"v_mfma_f32_16x16x32_bf16 v[144:147], a[128:131], v[102:105], v[144:147]\n"
"v_mfma_f32_16x16x32_bf16 v[20:23], a[132:135], v[102:105], v[20:23]\n"
"s_add_u32 s13, s71, 524288\n"
"buffer_load_dwordx4 v[44:47], v3, s[84:87], s13 offen offset:2048 sc1\n"
"v_mfma_f32_16x16x32_bf16 v[24:27], a[136:139], v[102:105], v[24:27]\n"
"v_mfma_f32_16x16x32_bf16 v[28:31], a[140:143], v[102:105], v[28:31]\n"
"v_mfma_f32_16x16x32_bf16 v[144:147], a[176:179], v[106:109], v[144:147]\n"
"buffer_load_dwordx4 v[102:105], v3, s[84:87], s13 offen offset:3072 sc1\n"
"v_mfma_f32_16x16x32_bf16 v[20:23], a[180:183], v[106:109], v[20:23]\n"
"v_mfma_f32_16x16x32_bf16 v[24:27], a[184:187], v[106:109], v[24:27]\n"
"v_mfma_f32_16x16x32_bf16 v[28:31], a[188:191], v[106:109], v[28:31]\n"
"s_add_u32 s13, s71, 528384\n"
"buffer_load_dwordx4 v[138:141], v3, s[84:87], s13 offen offset:2048 sc1\n"
"v_mfma_f32_16x16x32_bf16 v[144:147], a[192:195], v[48:51], v[144:147]\n"
"v_mfma_f32_16x16x32_bf16 v[20:23], a[196:199], v[48:51], v[20:23]\n"
"v_mfma_f32_16x16x32_bf16 v[24:27], a[200:203], v[48:51], v[24:27]\n"
"s_add_u32 s13, s71, 540672\n"
"buffer_load_dwordx4 v[106:109], v3, s[84:87], s13 offen offset:2048 sc1\n"
"v_mfma_f32_16x16x32_bf16 v[28:31], a[204:207], v[48:51], v[28:31]\n"
"s_waitcnt vmcnt(9)\n"
"v_mfma_f32_16x16x32_bf16 v[32:35], a[32:35], v[36:39], v[32:35]\n"
"v_mfma_f32_16x16x32_bf16 v[78:81], a[36:39], v[36:39], v[78:81]\n"
"buffer_load_dwordx4 v[110:113], v3, s[84:87], s13 offen offset:3072 sc1\n"
"v_mfma_f32_16x16x32_bf16 v[82:85], a[40:43], v[36:39], v[82:85]\n"
"v_mfma_f32_16x16x32_bf16 v[86:89], a[44:47], v[36:39], v[86:89]\n"
"s_waitcnt vmcnt(9)\n"
"v_mfma_f32_16x16x32_bf16 v[32:35], a[48:51], v[130:133], v[32:35]\n"
"s_add_u32 s13, s71, 544768\n"
"buffer_load_dwordx4 v[36:39], v3, s[84:87], s13 offen offset:2048 sc1\n"
"v_mfma_f32_16x16x32_bf16 v[78:81], a[52:55], v[130:133], v[78:81]\n"
"v_mfma_f32_16x16x32_bf16 v[82:85], a[56:59], v[130:133], v[82:85]\n"
"v_mfma_f32_16x16x32_bf16 v[86:89], a[60:63], v[130:133], v[86:89]\n"
"s_waitcnt vmcnt(9)\n"
"v_mfma_f32_16x16x32_bf16 v[32:35], a[96:99], v[40:43], v[32:35]\n"
"v_mfma_f32_16x16x32_bf16 v[78:81], a[100:103], v[40:43], v[78:81]\n"
"v_mfma_f32_16x16x32_bf16 v[82:85], a[104:107], v[40:43], v[82:85]\n"
"v_mfma_f32_16x16x32_bf16 v[86:89], a[108:111], v[40:43], v[86:89]\n"
"s_waitcnt vmcnt(8)\n"
"v_mfma_f32_16x16x32_bf16 v[32:35], a[144:147], v[16:19], v[32:35]\n"
"v_mfma_f32_16x16x32_bf16 v[78:81], a[148:151], v[16:19], v[78:81]\n"
"v_mfma_f32_16x16x32_bf16 v[82:85], a[152:155], v[16:19], v[82:85]\n"
"v_mfma_f32_16x16x32_bf16 v[86:89], a[156:159], v[16:19], v[86:89]\n"
"s_barrier\n"
"s_waitcnt vmcnt(7)\n"
"v_mfma_f32_16x16x32_bf16 v[32:35], a[160:163], v[98:101], v[32:35]\n"
"v_mfma_f32_16x16x32_bf16 v[78:81], a[164:167], v[98:101], v[78:81]\n"
"v_mfma_f32_16x16x32_bf16 v[82:85], a[168:171], v[98:101], v[82:85]\n"
"v_mfma_f32_16x16x32_bf16 v[86:89], a[172:175], v[98:101], v[86:89]\n"
"s_waitcnt vmcnt(6)\n"
"v_mfma_f32_16x16x32_bf16 v[32:35], a[208:211], v[134:137], v[32:35]\n"
"v_mfma_f32_16x16x32_bf16 v[78:81], a[212:215], v[134:137], v[78:81]\n"
"v_mfma_f32_16x16x32_bf16 v[82:85], a[216:219], v[134:137], v[82:85]\n"
"v_mfma_f32_16x16x32_bf16 v[86:89], a[220:223], v[134:137], v[86:89]\n"
"s_waitcnt vmcnt(5)\n"
"v_mfma_f32_16x16x32_bf16 v[24:27], a[40:43], v[44:47], v[24:27]\n"
"v_mfma_f32_16x16x32_bf16 v[28:31], a[44:47], v[44:47], v[28:31]\n"
"s_nop 1\n"
"ds_write_b128 v128, v[32:35]\n"
"s_waitcnt vmcnt(4)\n"
"v_mfma_f32_16x16x32_bf16 v[24:27], a[56:59], v[102:105], v[24:27]\n"
"v_mfma_f32_16x16x32_bf16 v[28:31], a[60:63], v[102:105], v[28:31]\n"
"s_waitcnt vmcnt(3)\n"
"v_mfma_f32_16x16x32_bf16 v[24:27], a[104:107], v[138:141], v[24:27]\n"
"ds_write_b128 v128, v[78:81] offset:1024\n"
"v_mfma_f32_16x16x32_bf16 v[28:31], a[108:111], v[138:141], v[28:31]\n"
"s_waitcnt vmcnt(2)\n"
"v_mfma_f32_16x16x32_bf16 v[24:27], a[152:155], v[106:109], v[24:27]\n"
"v_mfma_f32_16x16x32_bf16 v[28:31], a[156:159], v[106:109], v[28:31]\n"
"ds_write_b128 v129, v[82:85]\n"
"s_waitcnt vmcnt(1)\n"
"v_mfma_f32_16x16x32_bf16 v[24:27], a[168:171], v[110:113], v[24:27]\n"
"v_mfma_f32_16x16x32_bf16 v[28:31], a[172:175], v[110:113], v[28:31]\n"
"s_waitcnt vmcnt(0)\n"
"v_mfma_f32_16x16x32_bf16 v[24:27], a[216:219], v[36:39], v[24:27]\n"
"ds_write_b128 v129, v[86:89] offset:1024\n"
"v_mfma_f32_16x16x32_bf16 v[28:31], a[220:223], v[36:39], v[28:31]\n"
"v_mfma_f32_16x16x32_bf16 v[144:147], a[32:35], v[44:47], v[144:147]\n"
"v_mfma_f32_16x16x32_bf16 v[20:23], a[36:39], v[44:47], v[20:23]\n"
"s_nop 3\n"
"ds_write_b128 v252, v[24:27]\n"
"v_mfma_f32_16x16x32_bf16 v[144:147], a[48:51], v[102:105], v[144:147]\n"
"v_mfma_f32_16x16x32_bf16 v[20:23], a[52:55], v[102:105], v[20:23]\n"
"ds_write_b128 v252, v[28:31] offset:1024\n"
"v_mfma_f32_16x16x32_bf16 v[144:147], a[96:99], v[138:141], v[144:147]\n"
"v_mfma_f32_16x16x32_bf16 v[20:23], a[100:103], v[138:141], v[20:23]\n"
"s_waitcnt lgkmcnt(0)\n"
"s_barrier\n"
"ds_read_b128 v[24:27], v250\n"
"ds_read_b128 v[28:31], v250 offset:1024\n"
"v_mfma_f32_16x16x32_bf16 v[144:147], a[144:147], v[106:109], v[144:147]\n"
"ds_read_b128 v[32:35], v250 offset:2048\n"
"ds_read_b128 v[78:81], v250 offset:3072\n"
"v_mfma_f32_16x16x32_bf16 v[20:23], a[148:151], v[106:109], v[20:23]\n"
"ds_read_b128 v[82:85], v250 offset:4096\n"
"ds_read_b128 v[86:89], v250 offset:5120\n"
"v_mfma_f32_16x16x32_bf16 v[144:147], a[160:163], v[110:113], v[144:147]\n"
"v_mfma_f32_16x16x32_bf16 v[20:23], a[164:167], v[110:113], v[20:23]\n"
"v_mfma_f32_16x16x32_bf16 v[144:147], a[208:211], v[36:39], v[144:147]\n"
"v_mfma_f32_16x16x32_bf16 v[20:23], a[212:215], v[36:39], v[20:23]\n"
"s_waitcnt lgkmcnt(5)\n"
"s_nop 5\n"
"v_pk_add_f32 v[24:25], v[24:25], v[144:145] op_sel:[0,0] op_sel_hi:[1,1]\n"
"s_waitcnt lgkmcnt(1)\n"
"v_pk_add_f32 v[32:33], v[32:33], v[82:83] op_sel:[0,0] op_sel_hi:[1,1]\n"
"v_pk_add_f32 v[26:27], v[26:27], v[146:147] op_sel:[0,0] op_sel_hi:[1,1]\n"
"v_pk_add_f32 v[34:35], v[34:35], v[84:85] op_sel:[0,0] op_sel_hi:[1,1]\n"
"v_pk_add_f32 v[28:29], v[28:29], v[20:21] op_sel:[0,0] op_sel_hi:[1,1]\n"
"s_waitcnt lgkmcnt(0)\n"
"v_pk_add_f32 v[78:79], v[78:79], v[86:87] op_sel:[0,0] op_sel_hi:[1,1]\n"
"v_pk_add_f32 v[30:31], v[30:31], v[22:23] op_sel:[0,0] op_sel_hi:[1,1]\n"
"v_pk_add_f32 v[80:81], v[80:81], v[88:89] op_sel:[0,0] op_sel_hi:[1,1]\n"
"v_pk_add_f32 v[24:25], v[24:25], v[32:33] op_sel:[0,0] op_sel_hi:[1,1]\n"
"v_pk_add_f32 v[26:27], v[26:27], v[34:35] op_sel:[0,0] op_sel_hi:[1,1]\n"
"v_pk_add_f32 v[28:29], v[28:29], v[78:79] op_sel:[0,0] op_sel_hi:[1,1]\n"
"v_pk_add_f32 v[30:31], v[30:31], v[80:81] op_sel:[0,0] op_sel_hi:[1,1]\n"
"v_cvt_pk_bf16_f32 v8, v24, v25\n"
"v_cvt_pk_bf16_f32 v9, v26, v27\n"
"v_cvt_pk_bf16_f32 v10, v28, v29\n"
"v_cvt_pk_bf16_f32 v11, v30, v31\n"
"s_add_u32 s8, s76, 65536\n"
"buffer_store_dwordx4 v[8:11], v143, s[48:51], s8 offen\n"
"v_mov_b32 v16, s60\n"
"v_mov_b32 v17, s61\n"
"s_or_b32 s63, %2, s34\n"
"s_mov_b32 s68, 0\n"
"s_cmp_eq_u32 s63, 0\n"
"s_cselect_b32 s63, s68, 2147483392\n"
"buffer_store_dwordx2 v[16:17], off, s[52:55], s63 sc1\n"
"s_waitcnt vmcnt(0)\n"
"L_end_%=:\n"
  :: "s"(__builtin_amdgcn_kernarg_segment_ptr()), "v"(threadIdx.x), "s"(blockIdx.x), "s"(ldsb)
  : "v1", "v2", "v3", "v4", "v5", "v6", "v7", "v8", "v9", "v10", "v11", "v12", "v13", "v14", "v15", "v16", "v17", "v18", "v19", "v20", "v21", "v22", "v23", "v24", "v25", "v26", "v27", "v28", "v29", "v30", "v31", "v32", "v33", "v34", "v35", "v36", "v37", "v38", "v39", "v40", "v41", "v42", "v43", "v44", "v45", "v46", "v47", "v48", "v49", "v50", "v51", "v52", "v53", "v54", "v55", "v56", "v57", "v58", "v59", "v60", "v61", "v62", "v63", "v64", "v65", "v66", "v67", "v68", "v69", "v70", "v71", "v72", "v73", "v74", "v75", "v76", "v77", "v78", "v79", "v80", "v81", "v82", "v83", "v84", "v85", "v86", "v87", "v88", "v89", "v90", "v91", "v92", "v93", "v94", "v95", "v96", "v97", "v98", "v99", "v100", "v101", "v102", "v103", "v104", "v105", "v106", "v107", "v108", "v109", "v110", "v111", "v112", "v113", "v114", "v115", "v116", "v117", "v118", "v119", "v120", "v121", "v122", "v123", "v124", "v125", "v126", "v127", "v128", "v129", "v130", "v131", "v132", "v133", "v134", "v135", "v136", "v137", "v138", "v139", "v140", "v141", "v142", "v143", "v144", "v145", "v146", "v147", "v148", "v149", "v150", "v151", "v152", "v153", "v154", "v155", "v156", "v157", "v158", "v159", "v160", "v161", "v162", "v163", "v164", "v165", "v166", "v167", "v168", "v169", "v170", "v171", "v172", "v173", "v174", "v175", "v176", "v177", "v178", "v179", "v180", "v181", "v182", "v183", "v184", "v185", "v186", "v187", "v188", "v189", "v190", "v191", "v192", "v193", "v194", "v195", "v196", "v197", "v198", "v199", "v200", "v201", "v202", "v203", "v204", "v205", "v206", "v207", "v208", "v209", "v210", "v211", "v212", "v213", "v214", "v215", "v216", "v217", "v218", "v219", "v220", "v221", "v222", "v223", "v224", "v225", "v226", "v227", "v228", "v229", "v230", "v231", "v232", "v233", "v234", "v235", "v236", "v237", "v238", "v239", "v240", "v241", "v242", "v243", "v244", "v245", "v246", "v247", "v248", "v249", "v250", "v251", "v252", "v253", "v254", "v255", "a0", "a1", "a2", "a3", "a4", "a5", "a6", "a7", "a8", "a9", "a10", "a11", "a12", "a13", "a14", "a15", "a16", "a17", "a18", "a19", "a20", "a21", "a22", "a23", "a24", "a25", "a26", "a27", "a28", "a29", "a30", "a31", "a32", "a33", "a34", "a35", "a36", "a37", "a38", "a39", "a40", "a41", "a42", "a43", "a44", "a45", "a46", "a47", "a48", "a49", "a50", "a51", "a52", "a53", "a54", "a55", "a56", "a57", "a58", "a59", "a60", "a61", "a62", "a63", "a64", "a65", "a66", "a67", "a68", "a69", "a70", "a71", "a72", "a73", "a74", "a75", "a76", "a77", "a78", "a79", "a80", "a81", "a82", "a83", "a84", "a85", "a86", "a87", "a88", "a89", "a90", "a91", "a92", "a93", "a94", "a95", "a96", "a97", "a98", "a99", "a100", "a101", "a102", "a103", "a104", "a105", "a106", "a107", "a108", "a109", "a110", "a111", "a112", "a113", "a114", "a115", "a116", "a117", "a118", "a119", "a120", "a121", "a122", "a123", "a124", "a125", "a126", "a127", "a128", "a129", "a130", "a131", "a132", "a133", "a134", "a135", "a136", "a137", "a138", "a139", "a140", "a141", "a142", "a143", "a144", "a145", "a146", "a147", "a148", "a149", "a150", "a151", "a152", "a153", "a154", "a155", "a156", "a157", "a158", "a159", "a160", "a161", "a162", "a163", "a164", "a165", "a166", "a167", "a168", "a169", "a170", "a171", "a172", "a173", "a174", "a175", "a176", "a177", "a178", "a179", "a180", "a181", "a182", "a183", "a184", "a185", "a186", "a187", "a188", "a189", "a190", "a191", "a192", "a193", "a194", "a195", "a196", "a197", "a198", "a199", "a200", "a201", "a202", "a203", "a204", "a205", "a206", "a207", "a208", "a209", "a210", "a211", "a212", "a213", "a214", "a215", "a216", "a217", "a218", "a219", "a220", "a221", "a222", "a223", "a224", "a225", "a226", "a227", "a228", "a229", "a230", "a231", "a232", "a233", "a234", "a235", "a236", "a237", "a238", "a239", "a240", "a241", "a242", "a243", "a244", "a245", "a246", "a247", "a248", "a249", "a250", "a251", "a252", "a253", "a254", "a255", "s8", "s9", "s10", "s11", "s12", "s13", "s14", "s15", "s16", "s17", "s18", "s19", "s20", "s21", "s22", "s23", "s24", "s25", "s26", "s27", "s28", "s29", "s30", "s31", "s34", "s35", "s36", "s37", "s38", "s39", "s40", "s41", "s42", "s43", "s44", "s45", "s46", "s47", "s48", "s49", "s50", "s51", "s52", "s53", "s54", "s55", "s56", "s57", "s58", "s59", "s60", "s61", "s62", "s63", "s64", "s65", "s66", "s67", "s68", "s69", "s70", "s71", "s72", "s73", "s74", "s75", "s76", "s77", "s78", "s79", "s80", "s81", "s82", "s83", "s84", "s85", "s86", "s87", "s88", "s89", "s90", "s91", "s92", "s93", "s94", "s95", "s96", "s97", "s98", "s99", "vcc", "scc", "memory");
}
extern "C" void kernel_launch(void* const* d_in, const int* in_sizes, int n_in, void* d_out, int out_size,
                              void* d_ws, size_t ws_size, hipStream_t stream) {
  Args a;
  a.p[0] = d_in[0]; a.p[1] = d_in[1]; a.p[2] = d_in[2]; a.p[3] = d_in[3]; a.p[4] = d_in[5];
  a.p[5] = d_in[6]; a.p[6] = d_in[7]; a.p[7] = d_in[8]; a.p[8] = d_out; a.p[9] = d_ws;
  fused_kernel<<<256, 256, 0, stream>>>(a);
}
```
